```python
import torch

def _softmax(x, axis=-1):
    return torch.softmax(x, dim=axis)
def _gelu(x):
    return torch.nn.functional.gelu(x, approximate="tanh")
def _silu(x):
    return torch.nn.functional.silu(x)
def _sigmoid(x):
    return torch.sigmoid(x)
def _layernorm(x, axis=-1, eps=1e-5):
    m = x.mean(dim=axis, keepdim=True)
    v = x.var(dim=axis, keepdim=True, unbiased=False)
    return (x - m) / torch.sqrt(v + eps)
def _scatter_add(tgt, idx, src, axis=0):
    return tgt.clone().index_add_(axis, idx, src)
def _segment_sum(src, seg, n):
    out = torch.zeros((n,) + src.shape[1:], dtype=src.dtype)
    return out.index_add_(0, seg, src)
def _pad(x, pw):
    flat = tuple(v for pair in reversed(pw) for v in pair)
    return torch.nn.functional.pad(x, flat)

kernel_name = 'parallel_sum__spectral_gfnet__glu_gated_bilinear__prompt'

def setup_inputs():

    _w = (lambda a: (lambda t: t.to(torch.bfloat16) if t.is_floating_point() else t)(torch.as_tensor(a)))
    def _A_setup():
        B, S, D = 1, 2048, 512
        Df = D // 2 + 1
        return {
            "x": _w((lambda *s: torch.randn(*s, dtype=torch.bfloat16) * 0.05)(B, S, D)),
            "w_r": _w((lambda *s: torch.randn(*s, dtype=torch.bfloat16) * 0.4)(S, Df) * 0.02),
            "w_i": _w((lambda *s: torch.randn(*s, dtype=torch.bfloat16) * 0.4)(S, Df) * 0.02),
        }
    _A_in = _A_setup()

    def _B_setup():
        B, S, D, F = 1, 2048, 512, 1792
        return {
            "x": _w((lambda *s: torch.randn(*s, dtype=torch.bfloat16) * 0.05)(B, S, D)),
            "w_gate": _w((lambda *s: torch.randn(*s, dtype=torch.bfloat16) * 0.2)(D, F) * (D ** -0.5)),
            "w_up": _w((lambda *s: torch.randn(*s, dtype=torch.bfloat16) * 0.2)(D, F) * (D ** -0.5)),
            "w_down": _w((lambda *s: torch.randn(*s, dtype=torch.bfloat16) * 0.2)(F, D) * (F ** -0.5)),
        }
    _B_in = _B_setup()

    return {
        "A_x": _A_in["x"],
        "A_w_r": _A_in["w_r"],
        "A_w_i": _A_in["w_i"],
        "B_x": _B_in["x"],
        "B_w_gate": _B_in["w_gate"],
        "B_w_up": _B_in["w_up"],
        "B_w_down": _B_in["w_down"],
    }

def reference(A_x, A_w_r, A_w_i, B_x, B_w_gate, B_w_up, B_w_down):
    def _A_ref(x, w_r, w_i):
        B, S, D = x.shape
        X = (lambda x, axes=(-2,-1): torch.fft.rfft2(x.float(), dim=axes))(x, axes=(-2, -1))
        W = w_r + 1j * w_i
        X = X * W
        y = (lambda x, s, axes=(-2,-1): torch.fft.irfft2(x, s=s, dim=axes).to(torch.bfloat16))(X, (S, D), axes=(-2, -1))
        return y

    def _B_ref(x, w_gate, w_up, w_down):
        g = torch.einsum("bsd,df->bsf", x, w_gate)
        u = torch.einsum("bsd,df->bsf", x, w_up)
        h = g * u
        return torch.einsum("bsf,fd->bsd", h, w_down)

    x = A_x
    xn = _layernorm(x, axis=-1)
    return x + _A_ref(xn, A_w_r, A_w_i) + _B_ref(xn, B_w_gate, B_w_up, B_w_down)
```

```cpp
#include <hip/hip_runtime.h>
#include <stdint.h>

#define LAS __attribute__((address_space(3)))
#define GAS __attribute__((address_space(1)))
#define DI __device__ __forceinline__
typedef __bf16 bf8 __attribute__((ext_vector_type(8)));
typedef __bf16 bf2 __attribute__((ext_vector_type(2)));
typedef short s16x4 __attribute__((ext_vector_type(4)));
typedef short s16x8 __attribute__((ext_vector_type(8)));
typedef float f32x4 __attribute__((ext_vector_type(4)));
typedef float f32x2 __attribute__((ext_vector_type(2)));
typedef unsigned u32x4 __attribute__((ext_vector_type(4)));
typedef unsigned u32x2 __attribute__((ext_vector_type(2)));
typedef GAS unsigned gu32;
typedef GAS unsigned long long gu64;
#define RLX_AGENT __ATOMIC_RELAXED, __HIP_MEMORY_SCOPE_AGENT

namespace tabs {
constexpr double PI = 3.14159265358979323846264338327950288;
constexpr double cos_q(double x) {
    double x2 = x * x, t = 1.0, s = 1.0;
    for (int i = 1; i <= 14; ++i) { t *= -x2 / ((2 * i - 1) * (2 * i)); s += t; }
    return s;
}
constexpr uint16_t f2bf(float f) {
    uint32_t u = __builtin_bit_cast(uint32_t, f);
    u += 0x7FFFu + ((u >> 16) & 1u);
    return (uint16_t)(u >> 16);
}
struct Base { uint16_t v[512];
    constexpr Base() : v{} {
        for (int m = 0; m <= 128; ++m) {
            double c = (m == 128) ? 0.0 : cos_q(2.0 * PI * m / 512.0);
            uint16_t b = f2bf((float)c);
            uint16_t nb = (m == 128) ? b : (uint16_t)(b ^ 0x8000u);
            v[m] = b; v[(512 - m) & 511] = b;
            v[256 - m] = nb; v[(256 + m) & 511] = nb;
        }
    }
};
constexpr Base base{};
constexpr uint16_t cosb(int m) { return base.v[m & 511]; }
constexpr uint16_t nsinb(int m) { return base.v[(m + 128) & 511]; }
constexpr uint16_t sinb(int m) { return base.v[(m + 384) & 511]; }
struct Chunk { uint16_t v[16384]; };
constexpr Chunk mk_fwd(int blk) {
    Chunk r{};
    for (int dd = 0; dd < 32; ++dd) { const int d = blk * 32 + dd;
        for (int n = 0; n < 512; ++n) { const int k = 16 * (n >> 5) + (n & 15);
            r.v[dd * 512 + n] = base.v[(((n >> 4) & 1) ? (k == 0 ? d * 256 : d * k + 128) : d * k) & 511]; } }
    return r;
}
constexpr Chunk mk_inv(int blk) {
    Chunk r{};
    const int cc = blk >> 1;
    for (int e = 0; e < 16384; e += 8) {
        const int l = (e >> 3) & 63, dt = (e >> 9) & 3, kg = (e >> 11) & 3, s = ((blk & 1) << 1) + (e >> 13);
        const int kt = 4 * kg + s, d = 64 * cc + 16 * dt + (l & 15), k0 = 16 * kt + 4 * (l >> 4);
        for (int j = 0; j < 4; ++j) { const int k = k0 + j;
            r.v[e + j] = base.v[(k * d) & 511];
            r.v[e + 4 + j] = base.v[(k == 0 ? d * 256 : k * d + 128) & 511]; }
    }
    return r;
}
template <int I> constexpr Chunk fwd_c = mk_fwd(I);
template <int I> constexpr Chunk inv_c = mk_inv(I);
struct Big { Chunk c[16]; };
struct Small {
    uint16_t e1[4 * 4 * 64 * 8];
    uint16_t e2[2 * 2 * 64 * 8];
    uint16_t ea[2 * 2 * 64 * 8];
    constexpr Small() : e1{}, e2{}, ea{} {
        for (int w = 0; w < 4; ++w) for (int t = 0; t < 4; ++t) for (int l = 0; l < 64; ++l) for (int j = 0; j < 4; ++j) {
            const int a = 16 * w + (l & 15), b = 16 * t + 4 * (l >> 4) + j, o = (((w * 4 + t) * 64) + l) * 8;
            e1[o + j] = cosb(a * b * 8); e1[o + 4 + j] = nsinb(a * b * 8); }
        for (int st = 0; st < 2; ++st) for (int t = 0; t < 2; ++t) for (int l = 0; l < 64; ++l) for (int j = 0; j < 4; ++j) {
            const int s1 = 16 * st + (l & 15), j1 = 16 * t + 4 * (l >> 4) + j, o = (((st * 2 + t) * 64) + l) * 8;
            e2[o + j] = cosb(s1 * j1 * 16); e2[o + 4 + j] = nsinb(s1 * j1 * 16); }
        for (int jt = 0; jt < 2; ++jt) for (int l = 0; l < 64; ++l) for (int j = 0; j < 8; ++j) {
            const int j1 = 16 * jt + (l & 15), s1 = 8 * (l >> 4) + j;
            ea[((jt * 2 + 0) * 64 + l) * 8 + j] = cosb(j1 * s1 * 16); ea[((jt * 2 + 1) * 64 + l) * 8 + j] = sinb(j1 * s1 * 16); }
    }
};
}
#define C16(N) {{tabs::N<0>, tabs::N<1>, tabs::N<2>, tabs::N<3>, tabs::N<4>, tabs::N<5>, tabs::N<6>, tabs::N<7>, \
                 tabs::N<8>, tabs::N<9>, tabs::N<10>, tabs::N<11>, tabs::N<12>, tabs::N<13>, tabs::N<14>, tabs::N<15>}}
alignas(256) static __device__ const tabs::Big g_fwd = C16(fwd_c);
alignas(256) static __device__ const tabs::Big g_inv = C16(inv_c);
alignas(256) static __device__ const tabs::Small g_small{};

constexpr int S = 2048, D = 512, F = 1792, DF = 257, NT = 512;
constexpr size_t WS_CTRL = 0;
constexpr size_t WS_STAT = 65536;
constexpr size_t WS_AP = 131072;
constexpr size_t WS_AX = WS_AP + (2u << 20);
constexpr size_t WS_T = WS_AX + 65536;
constexpr size_t WS_H = WS_T + (2u << 20);
constexpr size_t WS_END = WS_H + (size_t)16 * 8 * 56 * 1024;
constexpr int W_FA = 256, W_FH = 512, W_FT = 1024, W_TMO = 2048;

struct Params {
    const uint16_t* x; const uint16_t* wr; const uint16_t* wi;
    const uint16_t* wg; const uint16_t* wu; const uint16_t* wd;
    uint16_t* out; unsigned char* ws;
};

DI f32x4 mfma(u32x4 a, u32x4 b, f32x4 c) {
    return __builtin_amdgcn_mfma_f32_16x16x32_bf16(__builtin_bit_cast(bf8, a), __builtin_bit_cast(bf8, b), c, 0, 0, 0);
}
DI unsigned pk_bf16(float lo, float hi) { bf2 p; p[0] = (__bf16)lo; p[1] = (__bf16)hi; return __builtin_bit_cast(unsigned, p); }
DI float bf_lo(unsigned u) { return __builtin_bit_cast(float, u << 16); }
DI float bf_hi(unsigned u) { return __builtin_bit_cast(float, u & 0xffff0000u); }
DI float bfr(float x) { return bf_lo(pk_bf16(x, x)); }
DI u32x4 lds128(LAS unsigned char* L, int off) { return *(LAS u32x4*)(L + off); }
DI u32x2 lds_tr(LAS unsigned char* L, int off) {
    return __builtin_bit_cast(u32x2, __builtin_amdgcn_ds_read_tr16_b64_v4i16((LAS s16x4*)(L + off)));
}
DI u32x4 lds_tr2(LAS unsigned char* L, int off0, int off1) {
    const u32x2 a = lds_tr(L, off0), b = lds_tr(L, off1);
    return u32x4{a.x, a.y, b.x, b.y};
}
DI void glds16(const void* gsrc, unsigned lds_addr) {
    asm volatile("s_mov_b32 m0, %1\n\ts_nop 0\n\tglobal_load_lds_dwordx4 %0, off" :: "v"(gsrc), "s"(lds_addr) : "memory");
}
#define WAIT_VM(n) asm volatile("s_waitcnt vmcnt(" #n ")" ::: "memory")
#define WAIT_LGKM0() asm volatile("s_waitcnt lgkmcnt(0)" ::: "memory")
#define BAR() do { asm volatile("" ::: "memory"); __builtin_amdgcn_s_barrier(); asm volatile("" ::: "memory"); } while (0)
DI float dot2(unsigned a, unsigned b, float c) {
    return __builtin_amdgcn_fdot2_f32_bf16(__builtin_bit_cast(bf2, a), __builtin_bit_cast(bf2, b), c, false);
}
DI u32x4 ld_sc1(__amdgpu_buffer_rsrc_t rs, int off) { return __builtin_amdgcn_raw_buffer_load_b128(rs, off, 0, 16); }
DI void st_sc1(__amdgpu_buffer_rsrc_t rs, int off, u32x4 v) { __builtin_amdgcn_raw_buffer_store_b128(v, rs, off, 0, 16); }
DI void st_sc1_64(__amdgpu_buffer_rsrc_t rs, int off, u32x2 v) { __builtin_amdgcn_raw_buffer_store_b64(v, rs, off, 0, 16); }
DI float wave_sum(float v) {
    v += __shfl_xor(v, 1); v += __shfl_xor(v, 2); v += __shfl_xor(v, 4); v += __shfl_xor(v, 8); v += __shfl_xor(v, 16); v += __shfl_xor(v, 32);
    return v;
}

constexpr int P1_STAGE = 49152, P1_BOFF = 16384;
constexpr int L_MISC = 147456;
constexpr int L_XI = L_MISC;
constexpr int L_ST = L_MISC + 8704;
constexpr int L_CS = L_ST + 1024;
constexpr int L_END = L_CS + 1024;
static_assert(L_END <= 163840, "lds");
constexpr int LDS_BYTES = 163840;

DI int panel_row(int r, int m) { return 64 * (m & 31) + 4 * r + (m >> 5); }

template <int WR, bool DFTW>
DI void p1_loop(LAS unsigned char* L, unsigned Lb, const unsigned char* (&srcA)[2], const unsigned char* (&srcB)[4], int strideB64,
                int wid, int wc, int lane, f32x4 (&acc)[4][4], float (&cs)[2]) {
    const int fr = lane & 15, g = lane >> 4;
    const int qq = fr >> 2, p = lane & 3;
    const int arow = 64 * WR + fr;
    const int aoff0 = arow * 128 + (((0 + g) ^ (fr & 7)) << 4);
    const int aoff1 = arow * 128 + (((4 + g) ^ (fr & 7)) << 4);
    const int sw = 2 * qq + 8 * (g & 1);
    int boff[4];
#pragma unroll
    for (int q = 0; q < 4; ++q) boff[q] = P1_BOFF + (8 * g + qq) * 512 + (((8 * wc + 2 * q + (p >> 1)) ^ sw) << 4) + 8 * (p & 1);
    const int b0 = wid & 1, b2 = (wid >> 1) & 1, hi = wid >> 2;
    auto issue = [&](int j) {
        const unsigned sb = Lb + (j % 3) * P1_STAGE;
#pragma unroll
        for (int i = 0; i < 2; ++i) { glds16(srcA[i], sb + (2 * wid + i) * 1024); srcA[i] += 128; }
#pragma unroll
        for (int u = 0; u < 4; ++u) { const int i = b0 + 2 * (u & 1) + 4 * b2 + 8 * (u >> 1) + 16 * hi;
            glds16(srcB[u], sb + P1_BOFF + i * 1024); srcB[u] += strideB64; }
    };
    u32x4 xa[2][4], wb[2][4];
    auto rd = [&](int j, int ks, int set) {
        const int sb = (j % 3) * P1_STAGE;
#pragma unroll
        for (int q = 0; q < 4; ++q) wb[set][q] = lds_tr2(L, sb + boff[q] + ks * 16384, sb + boff[q] + ks * 16384 + 2048);
#pragma unroll
        for (int mt = 0; mt < 4; ++mt) xa[set][mt] = lds128(L, sb + (ks ? aoff1 : aoff0) + mt * 2048);
    };
    auto mm = [&](int set) {
#pragma unroll
        for (int q = 0; q < 4; ++q)
#pragma unroll
            for (int mt = 0; mt < 4; ++mt) {
                if (DFTW && q >= 2) acc[q][mt] = mfma(xa[set][mt], wb[set][q], acc[q][mt]);
                else acc[q][mt] = mfma(wb[set][q], xa[set][mt], acc[q][mt]);
            }
        if (DFTW) {
#pragma unroll
            for (int e = 0; e < 4; ++e) cs[0] = dot2(wb[set][WR][e], 0x3F803F80u, cs[0]);
        } else {
#pragma unroll
            for (int i = 0; i < 2; ++i)
#pragma unroll
                for (int e = 0; e < 4; ++e) cs[i] = dot2(wb[set][2 * WR + i][e], 0x3F803F80u, cs[i]);
        }
    };
    issue(0); issue(1); issue(2);
    WAIT_VM(12); BAR();
    rd(0, 0, 0);
#pragma unroll
    for (int j = 0; j < 8; ++j) {
        rd(j, 1, 1);
        mm(0);
        if (j < 7) {
            if (j < 6) WAIT_VM(6); else WAIT_VM(0);
            WAIT_LGKM0(); BAR();
            if (j + 3 < 8) issue(j + 3);
            rd(j + 1, 0, 0);
        }
        mm(1);
    }
}

__global__ void __launch_bounds__(NT, 2) k_p1(Params p) {
    extern __shared__ __attribute__((aligned(16))) unsigned char smem[];
    LAS unsigned char* L = (LAS unsigned char*)smem;
    const unsigned Lb = (unsigned)(size_t)L;
    const int tid = threadIdx.x, lane = tid & 63, wid = __builtin_amdgcn_readfirstlane(tid >> 6);
    const int wr = wid >> 2, wc = wid & 3, fr = lane & 15, g = lane >> 4;
    const int b = blockIdx.x, xcd = b & 7, bi = b >> 3;
    const int r = (xcd & 1) * 8 + (bi & 7), c = (xcd >> 1) * 4 + (bi >> 3);
    gu32* ctrl = (gu32*)(p.ws + WS_CTRL);
    const unsigned tag = __hip_atomic_load(ctrl, RLX_AGENT) + 1u;
    const __amdgpu_buffer_rsrc_t rs = __builtin_amdgcn_make_buffer_rsrc((void*)p.ws, 0, (int)WS_END, 0x00020000);

    const unsigned char* srcA[2];
#pragma unroll
    for (int i = 0; i < 2; ++i) { const int m = 8 * (2 * wid + i) + (lane >> 3);
        srcA[i] = (const unsigned char*)p.x + (size_t)panel_row(r, m) * 1024 + (((lane & 7) ^ (lane >> 3)) << 4); }
    const unsigned char* srcB[4]; int strideB;
    {
        const int b0 = wid & 1, b2 = (wid >> 1) & 1, hi = wid >> 2;
        const int swz = 2 * (2 * b0 + (lane >> 5)) + 8 * b2;
        const int ch = (lane & 31) ^ swz, nt = ch >> 1, half = ch & 1, cw = nt >> 2, q = nt & 3;
        const int odd = c & 1;
        const unsigned char* base; int col;
        if (cw < 3) { const int ft = odd + 2 * cw + (q & 1); base = (const unsigned char*)(q < 2 ? p.wg : p.wu); col = 112 * c + 16 * ft + 8 * half; strideB = F * 2; }
        else if (q < 2) { const int ft = odd ? 0 : 6; base = (const unsigned char*)(q == 0 ? p.wg : p.wu); col = 112 * c + 16 * ft + 8 * half; strideB = F * 2; }
        else { base = (const unsigned char*)&g_fwd; col = 32 * c + 16 * (q - 2) + 8 * half; strideB = 1024; }
#pragma unroll
        for (int u = 0; u < 4; ++u) { const int i = b0 + 2 * (u & 1) + 4 * b2 + 8 * (u >> 1) + 16 * hi, row = 2 * i + (lane >> 5);
            srcB[u] = base + (size_t)row * strideB + col * 2; }
    }
    const int srow = panel_row(r, 8 * c + wid);
    const u32x4 xrow = *(const GAS u32x4*)((const GAS unsigned char*)p.x + (size_t)srow * 1024 + lane * 16);

    f32x4 acc[4][4];
#pragma unroll
    for (int q = 0; q < 4; ++q)
#pragma unroll
        for (int mt = 0; mt < 4; ++mt) acc[q][mt] = f32x4{0.f, 0.f, 0.f, 0.f};
    float cs[2] = {0.f, 0.f};
    {
        float v[8];
#pragma unroll
        for (int e = 0; e < 4; ++e) { v[2 * e] = bf_lo(xrow[e]); v[2 * e + 1] = bf_hi(xrow[e]); }
        float s = ((v[0] + v[1]) + (v[2] + v[3])) + ((v[4] + v[5]) + (v[6] + v[7]));
        s = wave_sum(s);
        const float mean = s * (1.0f / 512.0f);
        float q2 = 0.f;
#pragma unroll
        for (int e = 0; e < 8; ++e) { const float d = v[e] - mean; q2 += d * d; }
        q2 = wave_sum(q2);
        const float var = q2 * (1.0f / 512.0f);
        const float m_bf = bfr(mean), v_bf = bfr(var), s_bf = bfr(sqrtf(bfr(v_bf + 1e-5f)));
        const unsigned val = (__builtin_bit_cast(unsigned, m_bf) & 0xffff0000u) | (__builtin_bit_cast(unsigned, s_bf) >> 16);
        if (lane == 0) __hip_atomic_store((gu64*)(p.ws + WS_STAT) + (r * 128 + 8 * c + wid), ((unsigned long long)tag << 32) | val, RLX_AGENT);
    }
    const int sB64 = strideB * 64;
    if (wid == 0 || wid == 1 || wid == 2) p1_loop<0, false>(L, Lb, srcA, srcB, sB64, wid, wc, lane, acc, cs);
    else if (wid == 3) p1_loop<0, true>(L, Lb, srcA, srcB, sB64, wid, wc, lane, acc, cs);
    else if (wid == 7) p1_loop<1, true>(L, Lb, srcA, srcB, sB64, wid, wc, lane, acc, cs);
    else p1_loop<1, false>(L, Lb, srcA, srcB, sB64, wid, wc, lane, acc, cs);

    LAS float* CS = (LAS float*)(L + L_CS);
    LAS f32x2* ST = (LAS f32x2*)(L + L_ST);
    {
        float a = cs[0], bq = cs[1];
        a += __shfl_xor(a, 16); a += __shfl_xor(a, 32); bq += __shfl_xor(bq, 16); bq += __shfl_xor(bq, 32);
        if (g == 0) {
            if (wc < 3) { CS[(4 * wc + 2 * wr) * 16 + fr] = a; CS[(4 * wc + 2 * wr + 1) * 16 + fr] = bq; }
            else CS[(12 + wr) * 16 + fr] = a;
        }
        if (tid < 128) {
            gu64* gp = (gu64*)(p.ws + WS_STAT) + (r * 128 + tid);
            unsigned long long w; unsigned spins = 0;
            for (;;) { w = __hip_atomic_load(gp, RLX_AGENT); if (__all((unsigned)(w >> 32) == tag)) break;
                if (++spins > (1u << 20)) { ctrl[W_TMO] = 1; break; } __builtin_amdgcn_s_sleep(1); }
            const unsigned val = (unsigned)w;
            ST[tid] = f32x2{bf_hi(val), 1.0f / bf_lo(val)};
        }
    }
    __syncthreads();
    if (wc == 3) {
#pragma unroll
        for (int q = 2; q < 4; ++q)
#pragma unroll
            for (int mt = 0; mt < 4; ++mt) {
                const int m0 = 64 * wr + 16 * mt + 4 * g;
                const float csf = (c == 0 && q == 2 && fr == 0) ? 512.0f : 0.0f;
                float o[4];
#pragma unroll
                for (int e = 0; e < 4; ++e) { const f32x2 st = ST[m0 + e]; o[e] = (acc[q][mt][e] - st.x * csf) * st.y; }
                const int n = (q - 2) * 16 + fr, s2l = 2 * wr + (mt >> 1), s1 = 16 * (mt & 1) + 4 * g;
                *(LAS u32x2*)(L + L_XI + n * 272 + s2l * 64 + s1 * 2) = u32x2{pk_bf16(o[0], o[1]), pk_bf16(o[2], o[3])};
            }
    }
    __syncthreads();
    {
        const int mt = wid & 3, jt = wid >> 2;
        const int kq = fr >> 2, s2l = fr & 3;
        const u32x4 are = lds128(L, L_XI + (4 * mt + kq) * 272 + s2l * 64 + g * 16);
        const u32x4 aim = lds128(L, L_XI + (16 + 4 * mt + kq) * 272 + s2l * 64 + g * 16);
        const GAS u32x4* ea = (const GAS u32x4*)g_small.ea;
        const u32x4 Cf = ea[(jt * 2 + 0) * 64 + lane], Sf = ea[(jt * 2 + 1) * 64 + lane];
        const f32x4 z = {0.f, 0.f, 0.f, 0.f};
        const f32x4 Prc = mfma(are, Cf, z), Pis = mfma(aim, Sf, z), Prs = mfma(are, Sf, z), Pic = mfma(aim, Cf, z);
        const int j1 = 16 * jt + fr;
        const bool special = (c == 0 && mt == 0 && g == 0);
        float pr[4], qi[4], pr2[4], qi2[4];
#pragma unroll
        for (int e = 0; e < 4; ++e) {
            const float th = (float)(j1 * (4 * r + e)) * (1.0f / 2048.0f);
            const float cw = __builtin_amdgcn_cosf(th), sw = __builtin_amdgcn_sinf(th);
            const float ar = special ? Prc[e] : Prc[e] + Pis[e], ai = special ? -Prs[e] : Pic[e] - Prs[e];
            pr[e] = ar * cw + ai * sw; qi[e] = -(ai * cw - ar * sw);
            const float br = Pic[e], bi2 = -Pis[e];
            pr2[e] = br * cw + bi2 * sw; qi2[e] = -(bi2 * cw - br * sw);
        }
        const int U = (j1 >> 2) * 64 + 4 * c + mt, lp = 16 * (r & 3) + 4 * g + (j1 & 3);
        st_sc1(rs, (int)WS_AP + (((U * 4 + (r >> 2)) * 64 + lp) << 4),
               u32x4{pk_bf16(pr[0], pr[1]), pk_bf16(pr[2], pr[3]), pk_bf16(qi[0], qi[1]), pk_bf16(qi[2], qi[3])});
        if (special)
            st_sc1(rs, (int)WS_AX + (((((j1 >> 2) * 4 + (r >> 2)) * 4 + (r & 3)) * 4 + (j1 & 3)) << 4),
                   u32x4{pk_bf16(pr2[0], pr2[1]), pk_bf16(pr2[2], pr2[3]), pk_bf16(qi2[0], qi2[1]), pk_bf16(qi2[2], qi2[3])});
    }
    {
        const int odd = c & 1;
        if (wc < 3) {
            const f32x4 cg0 = *(LAS f32x4*)(CS + (4 * wc + 0) * 16 + 4 * g), cg1 = *(LAS f32x4*)(CS + (4 * wc + 1) * 16 + 4 * g);
            const f32x4 cu0 = *(LAS f32x4*)(CS + (4 * wc + 2) * 16 + 4 * g), cu1 = *(LAS f32x4*)(CS + (4 * wc + 3) * 16 + 4 * g);
            const int ks = (7 * c + odd + 2 * wc) >> 1;
#pragma unroll
            for (int mt = 0; mt < 4; ++mt) {
                const f32x2 st = ST[64 * wr + 16 * mt + fr];
                const float r2 = st.y * st.y;
                const f32x4 h0 = (acc[0][mt] - st.x * cg0) * (acc[2][mt] - st.x * cu0) * r2;
                const f32x4 h1 = (acc[1][mt] - st.x * cg1) * (acc[3][mt] - st.x * cu1) * r2;
                st_sc1(rs, (int)WS_H + ((((r * 8 + 4 * wr + mt) * 56 + ks) * 64 + lane) << 4),
                       u32x4{pk_bf16(h0[0], h0[1]), pk_bf16(h0[2], h0[3]), pk_bf16(h1[0], h1[1]), pk_bf16(h1[2], h1[3])});
            }
        } else {
            const f32x4 cg0 = *(LAS f32x4*)(CS + 12 * 16 + 4 * g), cu0 = *(LAS f32x4*)(CS + 13 * 16 + 4 * g);
            const int ft = 7 * c + (odd ? 0 : 6), ks = ft >> 1, half = ft & 1;
#pragma unroll
            for (int mt = 0; mt < 4; ++mt) {
                const f32x2 st = ST[64 * wr + 16 * mt + fr];
                const float r2 = st.y * st.y;
                const f32x4 h0 = (acc[0][mt] - st.x * cg0) * (acc[1][mt] - st.x * cu0) * r2;
                st_sc1_64(rs, (int)WS_H + ((((r * 8 + 4 * wr + mt) * 56 + ks) * 64 + lane) << 4) + half * 8,
                          u32x2{pk_bf16(h0[0], h0[1]), pk_bf16(h0[2], h0[3])});
            }
        }
    }
}

__global__ void __launch_bounds__(NT, 2) k_p2(Params p) {
    extern __shared__ __attribute__((aligned(16))) unsigned char smem[];
    LAS unsigned char* L = (LAS unsigned char*)smem;
    const int tid = threadIdx.x, lane = tid & 63, wid = __builtin_amdgcn_readfirstlane(tid >> 6);
    const int fr = lane & 15, g = lane >> 4;
    const int b = blockIdx.x, jq = b & 7, kq = b >> 3;
    const int u = wid >> 2, w = wid & 3;
    const __amdgpu_buffer_rsrc_t rs = __builtin_amdgcn_make_buffer_rsrc((void*)p.ws, 0, (int)WS_END, 0x00020000);
    const int k4 = 2 * kq + u;
    const int U = jq * 64 + k4;
    const int kk = fr >> 2, j1 = 4 * jq + (fr & 3), k = 4 * k4 + kk;
    float wre[4], wim[4];
#pragma unroll
    for (int e = 0; e < 4; ++e) { const int j = j1 + 32 * (16 * w + 4 * g + e);
        wre[e] = bf_lo(p.wr[j * DF + k]); wim[e] = bf_lo(p.wi[j * DF + k]); }
    const GAS u32x4* e1 = (const GAS u32x4*)g_small.e1;
    u32x4 E[4], Bf[4];
#pragma unroll
    for (int t = 0; t < 4; ++t) { E[t] = e1[(w * 4 + t) * 64 + lane]; Bf[t] = ld_sc1(rs, (int)WS_AP + (((U * 4 + t) * 64 + lane) << 4)); }
    f32x4 a1 = {0.f, 0.f, 0.f, 0.f}, a2 = a1;
#pragma unroll
    for (int t = 0; t < 4; ++t) {
        a1 = mfma(E[t], Bf[t], a1);
        a2 = mfma(E[t], u32x4{Bf[t].z, Bf[t].w, Bf[t].x ^ 0x80008000u, Bf[t].y ^ 0x80008000u}, a2);
    }
    constexpr float SC = 2.0f / (2048.0f * 512.0f);
    float yr[4], yi[4];
#pragma unroll
    for (int e = 0; e < 4; ++e) { const float xr = a1[e], xi = -a2[e];
        yr[e] = (xr * wre[e] - xi * wim[e]) * SC; yi[e] = (xr * wim[e] + xi * wre[e]) * SC; }
    if (k4 == 0) {
        f32x4 n1 = {0.f, 0.f, 0.f, 0.f}, n2 = n1;
#pragma unroll
        for (int t = 0; t < 4; ++t) {
            const u32x4 bx = ld_sc1(rs, (int)WS_AX + ((((jq * 4 + t) * 4 + g) * 4 + (fr & 3)) << 4));
            n1 = mfma(E[t], bx, n1);
            n2 = mfma(E[t], u32x4{bx.z, bx.w, bx.x ^ 0x80008000u, bx.y ^ 0x80008000u}, n2);
        }
        if (kk == 0) {
#pragma unroll
            for (int e = 0; e < 4; ++e) { const int j = j1 + 32 * (16 * w + 4 * g + e), jm = (2048 - j) & 2047;
                const float h0r = 0.5f * (wre[e] + bf_lo(p.wr[jm * DF])), h0i = 0.5f * (wim[e] - bf_lo(p.wi[jm * DF]));
                const float h2r = 0.5f * (bf_lo(p.wr[j * DF + 256]) + bf_lo(p.wr[jm * DF + 256]));
                const float h2i = 0.5f * (bf_lo(p.wi[j * DF + 256]) - bf_lo(p.wi[jm * DF + 256]));
                const float x0r = a1[e], x0i = -a2[e], x2r = n1[e], x2i = -n2[e];
                const float y0r = x0r * h0r - x0i * h0i, y0i = x0r * h0i + x0i * h0r;
                const float y2r = x2r * h2r - x2i * h2i, y2i = x2r * h2i + x2i * h2r;
                yr[e] = (y0r - y2i) * (0.5f * SC); yi[e] = (y0i + y2r) * (0.5f * SC); }
        }
    }
    *(LAS u32x4*)(L + ((u * 4 + w) * 64 + lane) * 16) = u32x4{pk_bf16(yr[0], yr[1]), pk_bf16(yr[2], yr[3]), pk_bf16(yi[0], yi[1]), pk_bf16(yi[2], yi[3])};
    __syncthreads();
    f32x4 t1 = {0.f, 0.f, 0.f, 0.f}, t2 = t1;
#pragma unroll
    for (int t = 0; t < 4; ++t) {
        const u32x4 y = lds128(L, ((u * 4 + t) * 64 + lane) * 16);
        t1 = mfma(y, E[t], t1);
        t2 = mfma(u32x4{y.z, y.w, y.x ^ 0x80008000u, y.y ^ 0x80008000u}, E[t], t2);
    }
    {
        const int s2 = 16 * w + fr, ko = 4 * k4 + g;
        float tr[4], ti[4];
#pragma unroll
        for (int e = 0; e < 4; ++e) {
            const float th = (float)((4 * jq + e) * s2) * (1.0f / 2048.0f);
            const float cw = __builtin_amdgcn_cosf(th), sw = __builtin_amdgcn_sinf(th);
            tr[e] = t1[e] * cw - t2[e] * sw; ti[e] = t2[e] * cw + t1[e] * sw; }
        st_sc1(rs, (int)WS_T + ((((s2 * 16 + (ko >> 4)) * 2 + (jq >> 2)) * 64 + (jq & 3) * 16 + (ko & 15)) << 4),
               u32x4{pk_bf16(tr[0], tr[1]), pk_bf16(tr[2], tr[3]), pk_bf16(ti[0], ti[1]), pk_bf16(ti[2], ti[3])});
    }
}

constexpr int P3_STAGE = 16384, P3_NBUF = 6;
__global__ void __launch_bounds__(NT, 2) k_p3(Params p) {
    extern __shared__ __attribute__((aligned(16))) unsigned char smem[];
    LAS unsigned char* L = (LAS unsigned char*)smem;
    const unsigned Lb = (unsigned)(size_t)L;
    const int tid = threadIdx.x, lane = tid & 63, wid = __builtin_amdgcn_readfirstlane(tid >> 6);
    const int fr = lane & 15, g = lane >> 4;
    const int b = blockIdx.x, xcd = b & 7, bi = b >> 3;
    const int hp = xcd * 4 + (bi & 3), cc = bi >> 2;
    const int r = hp >> 1, h = hp & 1;
    const int kg = wid & 3, rh = wid >> 2;
    const __amdgpu_buffer_rsrc_t rs = __builtin_amdgcn_make_buffer_rsrc((void*)p.ws, 0, (int)WS_END, 0x00020000);

    const unsigned char* srcW[2];
#pragma unroll
    for (int i = 0; i < 2; ++i) { const int row = 8 * (2 * wid + i) + (lane >> 3), sw = 2 * ((row >> 1) & 3);
        srcW[i] = (const unsigned char*)p.wd + (size_t)row * 1024 + cc * 128 + (((lane & 7) ^ sw) << 4); }
    const unsigned char* srcF = (const unsigned char*)&g_inv + (size_t)cc * 65536 + wid * 2048 + lane * 16;
    auto issue = [&](int s) {
        const unsigned sb = Lb + (s % P3_NBUF) * P3_STAGE + wid * 2048;
        if (s < 14) { glds16(srcW[0] + (size_t)s * 131072, sb); glds16(srcW[1] + (size_t)s * 131072, sb + 1024); }
        else { glds16(srcF + (s - 14) * 16384, sb); glds16(srcF + (s - 14) * 16384 + 1024, sb + 1024); }
    };
    const int qq = fr >> 2, pp = lane & 3;
    int woff[4];
    {
        const int row = 32 * kg + 4 * g + qq;
        const int sw = 2 * ((row >> 1) & 3);
#pragma unroll
        for (int dt = 0; dt < 4; ++dt) woff[dt] = row * 128 + (((2 * dt + (pp >> 1)) ^ sw) << 4) + 8 * (pp & 1);
    }
    f32x4 acc[4][2];
#pragma unroll
    for (int dt = 0; dt < 4; ++dt) { acc[dt][0] = f32x4{0.f, 0.f, 0.f, 0.f}; acc[dt][1] = acc[dt][0]; }
    const int hbase = (int)WS_H + (((r * 8 + 4 * h + 2 * rh) * 56 + kg) * 64 + lane) * 16;
#pragma unroll
    for (int s = 0; s < P3_NBUF - 1; ++s) issue(s);
    const int s2 = 4 * r + 2 * h + rh;
    const GAS u32x4* e2 = (const GAS u32x4*)g_small.e2;
    for (int s = 0; s < 18; ++s) {
        u32x4 bfr0, bfr1;
        if (s < 14) {
            bfr0 = ld_sc1(rs, hbase + s * 4096); bfr1 = ld_sc1(rs, hbase + 56 * 1024 + s * 4096);
        } else {
            const int kt = 4 * kg + (s - 14);
            const int tb = (int)WS_T + (((s2 * 16 + kt) * 2) * 64 + lane) * 16;
            const u32x4 T0 = ld_sc1(rs, tb), T1 = ld_sc1(rs, tb + 1024);
            const u32x4 T0s = u32x4{T0.z, T0.w, T0.x ^ 0x80008000u, T0.y ^ 0x80008000u}, T1s = u32x4{T1.z, T1.w, T1.x ^ 0x80008000u, T1.y ^ 0x80008000u};
            u32x4 zf[2];
#pragma unroll
            for (int st = 0; st < 2; ++st) {
                const u32x4 Ea = e2[(st * 2 + 0) * 64 + lane], Eb = e2[(st * 2 + 1) * 64 + lane];
                f32x4 zr = {0.f, 0.f, 0.f, 0.f}, zi = zr;
                zr = mfma(T0, Ea, zr); zr = mfma(T1, Eb, zr);
                zi = mfma(T0s, Ea, zi); zi = mfma(T1s, Eb, zi);
                zf[st] = u32x4{pk_bf16(zr[0], zr[1]), pk_bf16(zr[2], zr[3]), pk_bf16(zi[0], zi[1]), pk_bf16(zi[2], zi[3])};
            }
            bfr0 = zf[0]; bfr1 = zf[1];
        }
        WAIT_VM(0);
        BAR();
        if (s + P3_NBUF - 1 < 18) issue(s + P3_NBUF - 1);
        const int sb = (s % P3_NBUF) * P3_STAGE;
#pragma unroll
        for (int dt = 0; dt < 4; ++dt) {
            u32x4 wf;
            if (s < 14) wf = lds_tr2(L, sb + woff[dt], sb + woff[dt] + 2048);
            else wf = lds128(L, sb + (kg * 4 + dt) * 1024 + lane * 16);
            acc[dt][0] = mfma(wf, bfr0, acc[dt][0]);
            acc[dt][1] = mfma(wf, bfr1, acc[dt][1]);
        }
        WAIT_LGKM0();
    }
    __syncthreads();
    constexpr int RP = 272;
#pragma unroll
    for (int dt = 0; dt < 4; ++dt)
#pragma unroll
        for (int st = 0; st < 2; ++st)
            *(LAS f32x4*)(L + (kg * 64 + 32 * rh + 16 * st + fr) * RP + (16 * dt + 4 * g) * 4) = acc[dt][st];
    __syncthreads();
    {
        const int mm = tid >> 3, cg = tid & 7;
        f32x4 v0 = {0.f, 0.f, 0.f, 0.f}, v1 = v0;
#pragma unroll
        for (int q = 0; q < 4; ++q) { v0 += *(LAS f32x4*)(L + (q * 64 + mm) * RP + cg * 32); v1 += *(LAS f32x4*)(L + (q * 64 + mm) * RP + cg * 32 + 16); }
        const int row = panel_row(r, 64 * h + mm);
        const size_t o = (size_t)row * 512 + 64 * cc + 8 * cg;
        const u32x4 xv = *(const GAS u32x4*)(p.x + o);
        *(GAS u32x4*)(p.out + o) = u32x4{pk_bf16(bf_lo(xv.x) + v0[0], bf_hi(xv.x) + v0[1]), pk_bf16(bf_lo(xv.y) + v0[2], bf_hi(xv.y) + v0[3]),
                                         pk_bf16(bf_lo(xv.z) + v1[0], bf_hi(xv.z) + v1[1]), pk_bf16(bf_lo(xv.w) + v1[2], bf_hi(xv.w) + v1[3])};
    }
    if (b == 0 && tid == 0) { gu32* ctrl = (gu32*)(p.ws + WS_CTRL); __hip_atomic_store(ctrl, __hip_atomic_load(ctrl, RLX_AGENT) + 1u, RLX_AGENT); }
}

extern "C" void kernel_launch(void* const* d_in, const int* in_sizes, int n_in, void* d_out, int out_size, void* d_ws, size_t ws_size,
                              hipStream_t stream) {
    Params p;
    p.x = (const uint16_t*)d_in[0]; p.wr = (const uint16_t*)d_in[1]; p.wi = (const uint16_t*)d_in[2];
    p.wg = (const uint16_t*)d_in[4]; p.wu = (const uint16_t*)d_in[5]; p.wd = (const uint16_t*)d_in[6];
    p.out = (uint16_t*)d_out; p.ws = (unsigned char*)d_ws;
    (void)hipFuncSetAttribute((const void*)k_p1, hipFuncAttributeMaxDynamicSharedMemorySize, LDS_BYTES);
    (void)hipFuncSetAttribute((const void*)k_p3, hipFuncAttributeMaxDynamicSharedMemorySize, P3_STAGE * P3_NBUF);
    k_p1<<<256, NT, LDS_BYTES, stream>>>(p);
    k_p2<<<256, NT, 8192, stream>>>(p);
    k_p3<<<256, NT, P3_STAGE * P3_NBUF, stream>>>(p);
}
```

```cpp
#include <hip/hip_runtime.h>
#include <stdint.h>

#define LAS __attribute__((address_space(3)))
#define GAS __attribute__((address_space(1)))
#define DI __device__ __forceinline__
typedef __bf16 bf8 __attribute__((ext_vector_type(8)));
typedef __bf16 bf2 __attribute__((ext_vector_type(2)));
typedef short s16x4 __attribute__((ext_vector_type(4)));
typedef short s16x8 __attribute__((ext_vector_type(8)));
typedef float f32x4 __attribute__((ext_vector_type(4)));
typedef float f32x2 __attribute__((ext_vector_type(2)));
typedef unsigned u32x4 __attribute__((ext_vector_type(4)));
typedef unsigned u32x2 __attribute__((ext_vector_type(2)));
typedef GAS unsigned gu32;
typedef GAS unsigned long long gu64;
#define RLX_AGENT __ATOMIC_RELAXED, __HIP_MEMORY_SCOPE_AGENT

namespace tabs {
constexpr double PI = 3.14159265358979323846264338327950288;
constexpr double cos_q(double x) {
    double x2 = x * x, t = 1.0, s = 1.0;
    for (int i = 1; i <= 14; ++i) { t *= -x2 / ((2 * i - 1) * (2 * i)); s += t; }
    return s;
}
constexpr uint16_t f2bf(float f) {
    uint32_t u = __builtin_bit_cast(uint32_t, f);
    u += 0x7FFFu + ((u >> 16) & 1u);
    return (uint16_t)(u >> 16);
}
struct Base { uint16_t v[512];
    constexpr Base() : v{} {
        for (int m = 0; m <= 128; ++m) {
            double c = (m == 128) ? 0.0 : cos_q(2.0 * PI * m / 512.0);
            uint16_t b = f2bf((float)c);
            uint16_t nb = (m == 128) ? b : (uint16_t)(b ^ 0x8000u);
            v[m] = b; v[(512 - m) & 511] = b;
            v[256 - m] = nb; v[(256 + m) & 511] = nb;
        }
    }
};
constexpr Base base{};
constexpr uint16_t cosb(int m) { return base.v[m & 511]; }
constexpr uint16_t nsinb(int m) { return base.v[(m + 128) & 511]; }
constexpr uint16_t sinb(int m) { return base.v[(m + 384) & 511]; }
struct Chunk { uint16_t v[16384]; };
constexpr Chunk mk_fwd(int c) {
    Chunk r{};
    for (int d = 0; d < 512; ++d)
        for (int n = 0; n < 32; ++n) { const int k = 16 * c + (n & 15);
            r.v[d * 32 + n] = base.v[((n >> 4) ? (k == 0 ? d * 256 : d * k + 128) : d * k) & 511]; }
    return r;
}
constexpr Chunk mk_inv(int blk) {
    Chunk r{};
    const int cc = blk >> 1;
    for (int e = 0; e < 16384; e += 8) {
        const int l = (e >> 3) & 63, dt = (e >> 9) & 3, kg = (e >> 11) & 3, s = ((blk & 1) << 1) + (e >> 13);
        const int kt = 4 * kg + s, d = 64 * cc + 16 * dt + (l & 15), k0 = 16 * kt + 4 * (l >> 4);
        for (int j = 0; j < 4; ++j) { const int k = k0 + j;
            r.v[e + j] = base.v[(k * d) & 511];
            r.v[e + 4 + j] = base.v[(k == 0 ? d * 256 : k * d + 128) & 511]; }
    }
    return r;
}
template <int I> constexpr Chunk fwd_c = mk_fwd(I);
template <int I> constexpr Chunk inv_c = mk_inv(I);
struct Big { Chunk c[16]; };
}
#define C16(N) {{tabs::N<0>, tabs::N<1>, tabs::N<2>, tabs::N<3>, tabs::N<4>, tabs::N<5>, tabs::N<6>, tabs::N<7>, \
                 tabs::N<8>, tabs::N<9>, tabs::N<10>, tabs::N<11>, tabs::N<12>, tabs::N<13>, tabs::N<14>, tabs::N<15>}}
alignas(256) static __device__ const tabs::Big g_fwd = C16(fwd_c);
alignas(256) static __device__ const tabs::Big g_inv = C16(inv_c);

constexpr int S = 2048, D = 512, F = 1792, DF = 257, NT = 512;
constexpr size_t WS_CTRL = 0;
constexpr size_t WS_STAT = 98304;
constexpr size_t WS_AP = 131072;
constexpr size_t WS_AX = WS_AP + (2u << 20);
constexpr size_t WS_T = WS_AX + 65536;
constexpr size_t WS_H = WS_T + (2u << 20);
constexpr size_t WS_H2 = WS_H + (size_t)16 * 8 * 48 * 1024;
constexpr size_t WS_END = WS_H2 + (size_t)16 * 8 * 16 * 512;
#define FSTR 32
#define PSLEEP 8
constexpr int W_CT = 1024, W_TMO = 2048, W_FA = 4096, W_FH = W_FA + 256 * 32;
constexpr unsigned SPIN_CAP = 1u << 16;

struct Params {
    const uint16_t* x; const uint16_t* wr; const uint16_t* wi;
    const uint16_t* wg; const uint16_t* wu; const uint16_t* wd;
    uint16_t* out; unsigned char* ws;
};

template <int I> struct IC { static constexpr int v = I; };
template <int B, int E, class Fn> DI void static_for(Fn&& f) { if constexpr (B < E) { f(IC<B>{}); static_for<B + 1, E>(f); } }
DI f32x4 mfma(u32x4 a, u32x4 b, f32x4 c) {
    return __builtin_amdgcn_mfma_f32_16x16x32_bf16(__builtin_bit_cast(bf8, a), __builtin_bit_cast(bf8, b), c, 0, 0, 0);
}
DI unsigned pk_bf16(float lo, float hi) { bf2 p; p[0] = (__bf16)lo; p[1] = (__bf16)hi; return __builtin_bit_cast(unsigned, p); }
DI float bf_lo(unsigned u) { return __builtin_bit_cast(float, u << 16); }
DI float bf_hi(unsigned u) { return __builtin_bit_cast(float, u & 0xffff0000u); }
DI float bfr(float x) { return bf_lo(pk_bf16(x, x)); }
DI u32x4 lds128(LAS unsigned char* L, int off) { return *(LAS u32x4*)(L + off); }
DI u32x2 lds_tr(LAS unsigned char* L, int off) {
    return __builtin_bit_cast(u32x2, __builtin_amdgcn_ds_read_tr16_b64_v4i16((LAS s16x4*)(L + off)));
}
DI u32x4 lds_tr2(LAS unsigned char* L, int off0, int off1) {
    const u32x2 a = lds_tr(L, off0), b = lds_tr(L, off1);
    return u32x4{a.x, a.y, b.x, b.y};
}
DI u32x4 swap_neg(u32x4 v) { return u32x4{v.z, v.w, v.x ^ 0x80008000u, v.y ^ 0x80008000u}; }
DI u32x4 pack8(const float (&a)[4], const float (&b)[4]) { return u32x4{pk_bf16(a[0], a[1]), pk_bf16(a[2], a[3]), pk_bf16(b[0], b[1]), pk_bf16(b[2], b[3])}; }
DI u32x4 pack8(f32x4 a, f32x4 b) { return u32x4{pk_bf16(a[0], a[1]), pk_bf16(a[2], a[3]), pk_bf16(b[0], b[1]), pk_bf16(b[2], b[3])}; }
DI void glds16(const void* gsrc, unsigned lds_addr) {
    asm volatile("s_mov_b32 m0, %1\n\ts_nop 0\n\tglobal_load_lds_dwordx4 %0, off" :: "v"(gsrc), "s"(lds_addr) : "memory");
}
DI void glds16s(const void* sbase, unsigned voff, unsigned lds_addr) {
    asm volatile("s_mov_b32 m0, %2\n\ts_nop 0\n\tglobal_load_lds_dwordx4 %0, %1" :: "v"(voff), "s"(sbase), "s"(lds_addr) : "memory");
}
DI void glds4(const void* gsrc, unsigned lds_addr) {
    asm volatile("s_mov_b32 m0, %1\n\ts_nop 0\n\tglobal_load_lds_dword %0, off" :: "v"(gsrc), "s"(lds_addr) : "memory");
}
DI void gl128(u32x4& d, const void* p) { asm volatile("global_load_dwordx4 %0, %1, off" : "=v"(d) : "v"(p) : "memory"); }
DI void gl128_sc1(u32x4& d, const void* base, unsigned off) { asm volatile("global_load_dwordx4 %0, %1, %2 sc1" : "=v"(d) : "v"(off), "s"(base) : "memory"); }
DI void gl64_sc1(u32x2& d, const void* base, unsigned off) { asm volatile("global_load_dwordx2 %0, %1, %2 sc1" : "=v"(d) : "v"(off), "s"(base) : "memory"); }
DI void gl32_sc1(unsigned& d, const void* base, unsigned off) { asm volatile("global_load_dword %0, %1, %2 sc1" : "=v"(d) : "v"(off), "s"(base) : "memory"); }
DI void gl16(unsigned& d, const void* base, unsigned off) { asm volatile("global_load_ushort %0, %1, %2" : "=v"(d) : "v"(off), "s"(base) : "memory"); }
template <int N> DI void wait_vm() { static_assert(N >= 0 && N < 64, "vmcnt"); asm volatile("s_waitcnt vmcnt(%0)" :: "n"(N) : "memory"); }
template <class T> DI void pin(T& v) { asm volatile("" : "+v"(v)); }
#define WAIT_LGKM0() asm volatile("s_waitcnt lgkmcnt(0)" ::: "memory")
#define BAR() do { asm volatile("" ::: "memory"); __builtin_amdgcn_s_barrier(); asm volatile("" ::: "memory"); } while (0)
#define SB() __builtin_amdgcn_sched_barrier(0)
DI void st_sc1(__amdgpu_buffer_rsrc_t rs, int off, u32x4 v) { __builtin_amdgcn_raw_buffer_store_b128(v, rs, off, 0, 16); }
DI void st_sc1_64(__amdgpu_buffer_rsrc_t rs, int off, u32x2 v) { __builtin_amdgcn_raw_buffer_store_b64(v, rs, off, 0, 16); }
template <int CTRL> DI float dpp_add(float v) {
    return v + __builtin_bit_cast(float, __builtin_amdgcn_update_dpp(0, __builtin_bit_cast(int, v), CTRL, 0xf, 0xf, true));
}
DI float wave_sum(float v) {
    v = dpp_add<0xB1>(v);
    v = dpp_add<0x4E>(v);
    v = dpp_add<0x141>(v);
    v = dpp_add<0x140>(v);
    const int vi = __builtin_bit_cast(int, v);
    return (__builtin_bit_cast(float, __builtin_amdgcn_readlane(vi, 0)) + __builtin_bit_cast(float, __builtin_amdgcn_readlane(vi, 16))) +
           (__builtin_bit_cast(float, __builtin_amdgcn_readlane(vi, 32)) + __builtin_bit_cast(float, __builtin_amdgcn_readlane(vi, 48)));
}
DI void give_up(const unsigned char* ws, int lane) { if (lane == 0) __hip_atomic_store((gu32*)ws + W_TMO, 1u, RLX_AGENT); }
DI void poll16(const unsigned char* ws, int word, unsigned tag, int lane) {
#pragma unroll 1
    for (unsigned spins = 0;; ++spins) {
        unsigned f; gl32_sc1(f, ws, (unsigned)(word + (lane & 15) * FSTR) * 4u); wait_vm<0>(); pin(f);
        if (__all(f == tag)) break;
        if (spins > SPIN_CAP) { give_up(ws, lane); break; }
        __builtin_amdgcn_s_sleep(PSLEEP);
    }
}
DI void wh_load(unsigned (&w)[8], unsigned oj, unsigned om, const void* wr, const void* wi, int kq) {
    asm volatile("s_cmp_lg_u32 %12, 0\n\ts_cbranch_scc1 .Lnowh%=\n\t"
                 "global_load_ushort %0, %8, %10\n\tglobal_load_ushort %1, %8, %11\n\tglobal_load_ushort %2, %9, %10\n\tglobal_load_ushort %3, %9, %11\n\t"
                 "global_load_ushort %4, %8, %10 offset:512\n\tglobal_load_ushort %5, %8, %11 offset:512\n\tglobal_load_ushort %6, %9, %10 offset:512\n\tglobal_load_ushort %7, %9, %11 offset:512\n"
                 ".Lnowh%=:" : "=&v"(w[0]), "=&v"(w[1]), "=&v"(w[2]), "=&v"(w[3]), "=&v"(w[4]), "=&v"(w[5]), "=&v"(w[6]), "=&v"(w[7])
                 : "v"(oj), "v"(om), "s"(wr), "s"(wi), "s"(kq) : "memory", "scc");
}
DI float cosr(float x) { return __builtin_amdgcn_cosf(x); }
DI float sinr(float x) { return __builtin_amdgcn_sinf(x); }
DI u32x4 frag_cs(int a, int b0, int n) {
    float c[4], sn[4];
#pragma unroll
    for (int j = 0; j < 4; ++j) { const float th = (float)((a * (b0 + j)) & (n - 1)) * (1.0f / n); c[j] = cosr(th); sn[j] = -sinr(th); }
    return pack8(c, sn);
}

#define STAMP(i) do {} while (0)
#define TS(i) do {} while (0)

constexpr int P1_STAGE = 49152, P1_BOFF = 16384;
constexpr int L_MISC = 147456;
constexpr int L_XI = L_MISC;
constexpr int L_ST = L_MISC + 8704;
constexpr int L_WH = L_ST + 1024;
constexpr int L_SCR = L_WH + 4096;
constexpr int L_FLG = L_SCR + 256;
constexpr int L_END = L_FLG + 16;
static_assert(L_END <= 163840, "lds");
constexpr int LDS_BYTES = 163840;
constexpr int P3_SLOT = 16384;
DI constexpr int p3_slot(int s) { return (s % 9) * P3_SLOT; }

DI int panel_row(int r, int m) { return 64 * (m & 31) + 4 * r + (m >> 5); }
DI int p3_ks(int s, int kg) { const int n = 4 * s + kg; if (s >= 12) return 7 * (n - 48) + 3; const int p = n / 6, o = n - 6 * p; return 7 * p + o + (o >= 3); }

struct LdSrc { unsigned a[4]; const unsigned char* b[8]; long sB64; const unsigned char* t; long tadv; };
DI void p1_issue(LdSrc& s, const unsigned char* x, unsigned Lb, int lw, int j) {
    const unsigned sb = Lb + (j % 3) * P1_STAGE;
#pragma unroll
    for (int i = 0; i < 4; ++i) glds16s(x + j * 128, s.a[i], sb + (4 * lw + i) * 1024);
#pragma unroll
    for (int u = 0; u < 8; ++u) { const int i = (lw & 1) + 2 * (u & 1) + 4 * (lw >> 1) + 8 * ((u >> 1) & 1) + 16 * (u >> 2);
        glds16(s.b[u], sb + P1_BOFF + i * 1024); s.b[u] += s.sB64; }
}
DI void p1_touch(LdSrc& s, unsigned Lb) { glds4(s.t, Lb + L_SCR); s.t += s.tadv; }
DI void p3_issue(const Params& p, unsigned Lb, int lw, int cc, int s, unsigned wv, unsigned fv) {
    const unsigned sb = Lb + p3_slot(s) + lw * 4096;
    if (s < 14) { const unsigned char* base = (const unsigned char*)p.wd + (size_t)p3_ks(s, lw) * 32768;
#pragma unroll
        for (int i = 0; i < 4; ++i) glds16s(base + i * 8192, wv, sb + i * 1024); }
    else { const unsigned char* base = (const unsigned char*)&g_inv + cc * 65536 + (s - 14) * 16384 + lw * 4096;
#pragma unroll
        for (int i = 0; i < 4; ++i) glds16s(base + i * 1024, fv, sb + i * 1024); }
}

template <bool DFTW>
DI void p1_compute(LAS unsigned char* L, unsigned Lb, int wc, int lane, f32x4 (&acc)[4][8], f32x4 (&csm)[4], unsigned (&tsv)[32]) {
    asm volatile("" : "+v"(lane));
    const int fr = lane & 15, g = lane >> 4;
    const int qq = fr >> 2, p = lane & 3;
    int aoff0 = fr * 128 + (((0 + g) ^ (fr & 7)) << 4);
    int aoff1 = fr * 128 + (((4 + g) ^ (fr & 7)) << 4);
    const int sw = 2 * qq + 8 * (g & 1);
    int boff[4];
#pragma unroll
    for (int q = 0; q < 4; ++q) boff[q] = P1_BOFF + (8 * g + qq) * 512 + (((8 * wc + 2 * q + (p >> 1)) ^ sw) << 4) + 8 * (p & 1);
    u32x4 xa[2][4], wb[2][4];
    auto rdx = [&](int ks, int mh, int i) { xa[mh][i] = lds128(L, (ks ? aoff1 : aoff0) + (4 * mh + i) * 2048); };
    auto rdw = [&](int ks, int q) { const int o = boff[q] + ks * 16384; wb[ks][q] = lds_tr2(L, o, o + 2048); };
    auto rebase = [&](int d) { aoff0 += d; aoff1 += d; boff[0] += d; boff[1] += d; boff[2] += d; boff[3] += d;
        asm volatile("" : "+v"(aoff0), "+v"(aoff1), "+v"(boff[0]), "+v"(boff[1]), "+v"(boff[2]), "+v"(boff[3])); };
    auto step = [&](int ks, int mh, bool more) {
#pragma unroll
        for (int i = 0; i < 16; ++i) {
            const int q = i >> 2, mt = i & 3;
            if (DFTW && q >= 2) acc[q][4 * mh + mt] = mfma(xa[mh][mt], wb[ks][q], acc[q][4 * mh + mt]);
            else acc[q][4 * mh + mt] = mfma(wb[ks][q], xa[mh][mt], acc[q][4 * mh + mt]);
            if (more) {
                if (mh == 0) { if (i >= 1 && i <= 4) rdx(ks, 1, i - 1); }
                else { if (i < 4) rdx(ks ^ 1, 0, i); else if (i < 8) rdw(ks ^ 1, i - 4); }
            }
            if (mh == 0 && (i & 3) == 3) { const int d = i >> 2; if (!DFTW || d < 2) csm[d] = mfma(wb[ks][d], u32x4{0x3F803F80u, 0x3F803F80u, 0x3F803F80u, 0x3F803F80u}, csm[d]); }
            SB();
        }
    };
    BAR();
    TS(0);
#pragma unroll
    for (int i = 0; i < 4; ++i) { rdx(0, 0, i); rdw(0, i); }
    SB();
    static_for<0, 8>([&](auto J) {
        constexpr int j = J.v;
        step(0, 0, true);
        step(0, 1, true);
        step(1, 0, true);
        if constexpr (j < 7) { WAIT_LGKM0(); BAR(); TS(j + 1); rebase((j + 1) % 3 == 0 ? -2 * P1_STAGE : P1_STAGE);
            SB(); }
        step(1, 1, j < 7);
    });
    TS(8);
}

constexpr int HD = 5;
constexpr int FTS = 6;
enum { K_OP, K_TST, K_W, K_FT };
constexpr int p3_after(int kind, int idx, int s_now, bool LD) {
    int n = -1;
    auto hit = [&](bool is, int cnt) { if (is) n = 0; else if (n >= 0) n += cnt; };
    for (int o = 0; o < HD; ++o) hit(kind == K_OP && idx == o, 2);
    hit(kind == K_TST, 1); hit(false, 1);
    for (int s = 0; s < s_now; ++s) {
        if (s % 3 == 0 && LD && s >= 3 && s + 6 < 18) hit(kind == K_W && idx == s + 6, 12);
        if (s == 14 - HD) hit(false, 1);
        if (s + HD < 18) hit(kind == K_OP && idx == s + HD, (s + HD == 12 || s + HD == 13) ? 4 : 2);
        if (s == FTS) hit(kind == K_FT, 1);
    }
    return n;
}
constexpr int cmin(int a, int b) { return a < b ? a : b; }
constexpr int p3_wait(int s, bool LD) {
    int n = p3_after(K_OP, s, s, LD);
    if (s % 3 != 0 && LD) { const int g0 = s - s % 3; if (g0 >= 3 && g0 + 6 < 18) {   } }
    if (s == 2) n = cmin(n, p3_after(K_TST, 0, s, LD));
    if (s % 3 == 0 && LD && s >= 6 && s + 3 < 18) n = cmin(n, p3_after(K_W, s + 3, s, LD));
    if (s == 14 - HD) n = cmin(n, p3_after(K_FT, 0, s, LD));
    return n;
}
static_assert(p3_wait(0, false) == 2 * (HD - 1) + 2 && p3_wait(2, false) == 1 + 4, "queue model");

struct P3Ctx { unsigned hbase, h2base, tbase; int woff[4]; };
DI void p3_rdw(LAS unsigned char* L, const P3Ctx& c, int kg, int lane, int s, u32x4 (&wf)[4]) {
#pragma unroll
    for (int dt = 0; dt < 4; ++dt) {
        if (s < 14) wf[dt] = lds_tr2(L, p3_slot(s) + c.woff[dt], p3_slot(s) + c.woff[dt] + 2048);
        else wf[dt] = lds128(L, p3_slot(s) + (kg * 4 + dt) * 1024 + lane * 16);
    }
}

__global__ void __launch_bounds__(NT, 2) k_fused(Params p) {
    extern __shared__ __attribute__((aligned(16))) unsigned char smem[];
    LAS unsigned char* L = (LAS unsigned char*)smem;
    const unsigned Lb = (unsigned)(size_t)L;
    const int wid = __builtin_amdgcn_readfirstlane(threadIdx.x >> 6);
    const int wc = wid & 3;
    const int b = blockIdx.x, xcd = b & 7, bi = b >> 3;
    const int r = (xcd & 1) * 8 + (bi & 7), c = (xcd >> 1) * 4 + (bi >> 3);
    const int jq = b & 7, kq = b >> 3;
    const int cc = (xcd & 1) * 4 + (bi & 3), hp = (xcd >> 1) * 8 + (bi >> 2), r3 = hp >> 1, h3 = hp & 1;
    const unsigned char* ws = p.ws;
    unsigned tsv[32];
#pragma unroll
    for (int i = 0; i < 32; ++i) tsv[i] = 0;
    STAMP(0);
    TS(31);
    const int odd = c & 1;
    const int u2 = wid >> 2, w2 = wid & 3, k4 = 2 * kq + u2;
    LAS f32x2* ST = (LAS f32x2*)(L + L_ST);
    f32x4 acc[4][8], csm[4];
#pragma unroll
    for (int i = 0; i < 4; ++i) csm[i] = f32x4{0.f, 0.f, 0.f, 0.f};
    unsigned tag;
    {
    const int tid = threadIdx.x, lane = tid & 63;
    if (wid >= 4) {
        const int lw = wid - 4;
        unsigned epoch; gl32_sc1(epoch, ws, 0u);
        LdSrc ls;
        {
            const int idx = lw * 22 + (lane < 22 ? lane : 0);
            if (idx < 32) { ls.t = (const unsigned char*)p.x + (size_t)panel_row(r, 32 * (c & 3) + idx) * 1024; ls.tadv = 128; }
            else { const int i = idx - 32, row = 8 * (r & 7) + i / 7, it = i % 7;
                if (it == 6) { ls.t = (const unsigned char*)&g_fwd + c * 32768 + row * 64; ls.tadv = 64 * 64; }
                else { const int it3 = it % 3; ls.t = (const unsigned char*)(it < 3 ? p.wg : p.wu) + (size_t)row * (F * 2) + c * 224 + (it3 == 2 ? 220 : it3 * 128); ls.tadv = 64L * F * 2; } }
        }
        STAMP(12);
        p1_touch(ls, Lb);
        SB();
#pragma unroll
        for (int i = 0; i < 4; ++i) { const int m = 8 * (4 * lw + i) + (lane >> 3); ls.a[i] = (unsigned)panel_row(r, m) * 1024u + (((lane & 7) ^ (lane >> 3)) << 4); }
        {
            const int b0 = lw & 1, b2 = lw >> 1;
            const int swz = 2 * (2 * b0 + (lane >> 5)) + 8 * b2;
            const int ch = (lane & 31) ^ swz, nt = ch >> 1, half = ch & 1, cw = nt >> 2, q = nt & 3;
            const unsigned char* base; int col, strideB;
            if (cw < 3) { const int ft = odd + 2 * cw + (q & 1); base = (const unsigned char*)(q < 2 ? p.wg : p.wu); col = 112 * c + 16 * ft + 8 * half; strideB = F * 2; }
            else if (q < 2) { const int ft = odd ? 0 : 6; base = (const unsigned char*)(q == 0 ? p.wg : p.wu); col = 112 * c + 16 * ft + 8 * half; strideB = F * 2; }
            else { base = (const unsigned char*)&g_fwd + c * 32768; col = 16 * (q - 2) + 8 * half; strideB = 64; }
            const unsigned char* b00 = base + (size_t)(2 * b0 + 8 * b2 + (lane >> 5)) * strideB + col * 2;
#pragma unroll
            for (int u = 0; u < 8; ++u) ls.b[u] = b00 + (size_t)(4 * (u & 1) + 16 * ((u >> 1) & 1) + 32 * (u >> 2)) * strideB;
            ls.sB64 = (long)strideB * 64;
        }
        p1_issue(ls, (const unsigned char*)p.x, Lb, lw, 0);
        STAMP(13);
        p1_touch(ls, Lb); p1_touch(ls, Lb); p1_touch(ls, Lb);
        p1_issue(ls, (const unsigned char*)p.x, Lb, lw, 1);
        STAMP(14);
        if (tid == 256) { *(volatile LAS unsigned*)(L + L_FLG + 4) = 0u; *(volatile LAS unsigned*)(L + L_FLG + 8) = 0u; *(volatile LAS unsigned*)(L + L_FLG + 12) = 0u; }
        wait_vm<15>(); pin(epoch); tag = epoch + 1u;
        if (r == 0 && tid == 256) __hip_atomic_store((gu32*)p.ws + W_CT + c * 32, tag << 8, RLX_AGENT);
        WAIT_LGKM0();
        STAMP(15);
        BAR();
        p1_touch(ls, Lb);
        p1_issue(ls, (const unsigned char*)p.x, Lb, lw, 2);
        SB();
        const unsigned char *st0, *st1;
        {
            auto wline = [&](int i) { const int st = i >> 2, ln = (bi >> 2) * 16 + lw * 4 + (i & 3);
                return st < 14 ? (const unsigned char*)p.wd + (size_t)(32 * p3_ks(st, ln >> 5) + (ln & 31)) * 1024 + cc * 128
                               : (const unsigned char*)&g_inv + (size_t)cc * 65536 + (st - 14) * 16384 + ln * 128; };
            st0 = wline(lane);
            const int fi = (bi * 4 + lw) * 20 + (lane >= 8 && lane < 28 ? lane - 8 : 0);
            const int arr = fi / 1280, rr = (fi % 1280) / 5, ll = (fi % 1280) % 5;
            const int jrow = 4 * jq + (rr & 3) + 32 * (rr >> 2);
            st1 = lane < 8 ? wline(64 + lane) : (const unsigned char*)(arr ? p.wi : p.wr) + (size_t)jrow * (DF * 2) + min(ll * 128, 510);
        }
        unsigned wh8[8];
        u32x2 gran; const unsigned goff = (unsigned)WS_STAT + (unsigned)(r * 128 + (tid & 127)) * 8u;
        static_for<0, 7>([&](auto J) {
            constexpr int j = J.v;
            if constexpr (j < 4) wait_vm<13>(); else if constexpr (j == 4) wait_vm<12>();
            else if constexpr (j == 5) { if (kq == 0) wait_vm<23>(); else wait_vm<15>(); } else { if (kq == 0) wait_vm<11>(); else wait_vm<3>(); }
            BAR();
            if constexpr (j < 3) p1_touch(ls, Lb);
            if constexpr (j + 3 < 8) p1_issue(ls, (const unsigned char*)p.x, Lb, lw, j + 3);
            if constexpr (j == 4) {
                gl64_sc1(gran, ws, goff);
                const int jr = (lane & 3) + 4 * jq + 32 * (16 * lw + (lane >> 2)), jm = (2048 - jr) & 2047;
                wh_load(wh8, (unsigned)(jr * DF) * 2u, (unsigned)(jm * DF) * 2u, p.wr, p.wi, kq);
                glds4(st0, Lb + L_SCR); glds4(st1, Lb + L_SCR);
            }
        });
        wait_vm<2>();
        pin(wh8[0]); pin(wh8[1]); pin(wh8[2]); pin(wh8[3]); pin(wh8[4]); pin(wh8[5]); pin(wh8[6]); pin(wh8[7]);
        pin(gran);
        if (lw < 2) {
            if (!__all(gran.y == tag)) {
#pragma unroll 1
                for (unsigned spins = 0;; ++spins) {
                    gl64_sc1(gran, ws, goff); wait_vm<0>(); pin(gran);
                    if (__all(gran.y == tag)) break;
                    if (spins > SPIN_CAP) { give_up(ws, lane); break; }
                    __builtin_amdgcn_s_sleep(1);
                }
            }
            ST[tid & 127] = f32x2{bf_hi(gran.x), 1.0f / bf_lo(gran.x)};
        }
        if (kq == 0) {
            *(LAS f32x4*)(L + L_WH + (lw * 64 + lane) * 16) = f32x4{0.5f * (bf_lo(wh8[0]) + bf_lo(wh8[2])), 0.5f * (bf_lo(wh8[1]) - bf_lo(wh8[3])),
                                                                      0.5f * (bf_lo(wh8[4]) + bf_lo(wh8[6])), 0.5f * (bf_lo(wh8[5]) - bf_lo(wh8[7]))};
        }
#pragma unroll
        for (int q = 0; q < 4; ++q)
#pragma unroll
            for (int mt = 0; mt < 8; ++mt) acc[q][mt] = f32x4{0.f, 0.f, 0.f, 0.f};
    } else {
        unsigned epoch; gl32_sc1(epoch, ws, 0u);
        u32x4 xrow[2];
#pragma unroll
        for (int i = 0; i < 2; ++i) gl128(xrow[i], (const unsigned char*)p.x + (size_t)panel_row(r, 8 * c + 2 * wid + i) * 1024 + lane * 16);
        wait_vm<0>(); pin(epoch); pin(xrow[0]); pin(xrow[1]);
        tag = epoch + 1u;
#pragma unroll
        for (int i = 0; i < 2; ++i) {
            float v[8];
#pragma unroll
            for (int e = 0; e < 4; ++e) { v[2 * e] = bf_lo(xrow[i][e]); v[2 * e + 1] = bf_hi(xrow[i][e]); }
            float s = ((v[0] + v[1]) + (v[2] + v[3])) + ((v[4] + v[5]) + (v[6] + v[7]));
            s = wave_sum(s);
            const float mean = s * (1.0f / 512.0f);
            float q2 = 0.f;
#pragma unroll
            for (int e = 0; e < 8; ++e) { const float d = v[e] - mean; q2 += d * d; }
            q2 = wave_sum(q2);
            const float var = q2 * (1.0f / 512.0f);
            const float m_bf = bfr(mean), v_bf = bfr(var), s_bf = bfr(sqrtf(bfr(v_bf + 1e-5f)));
            const unsigned val = (__builtin_bit_cast(unsigned, m_bf) & 0xffff0000u) | (__builtin_bit_cast(unsigned, s_bf) >> 16);
            if (lane == 0) __hip_atomic_store((gu64*)(p.ws + WS_STAT) + (r * 128 + 8 * c + 2 * wid + i), ((unsigned long long)tag << 32) | val, RLX_AGENT);
        }
#pragma unroll
        for (int q = 0; q < 4; ++q)
#pragma unroll
            for (int mt = 0; mt < 8; ++mt) acc[q][mt] = f32x4{0.f, 0.f, 0.f, 0.f};
        STAMP(1);
        if (wid < 3) p1_compute<false>(L, Lb, wc, lane, acc, csm, tsv); else p1_compute<true>(L, Lb, wc, lane, acc, csm, tsv);
        STAMP(2);
    }
    }
    int tz = threadIdx.x; asm volatile("" : "+v"(tz));
    const int tid = tz, lane = tz & 63, fr = lane & 15, g = lane >> 4;
    const __amdgpu_buffer_rsrc_t rs = __builtin_amdgcn_make_buffer_rsrc((void*)p.ws, 0, (int)WS_END, 0x00020000);
    const unsigned wv = (unsigned)(lane >> 3) * 1024u + cc * 128u + (unsigned)(((lane & 7) ^ (2 * ((lane >> 4) & 3))) << 4), fv = lane * 16u;
    volatile LAS unsigned* flg = (volatile LAS unsigned*)(L + L_FLG);
    WAIT_LGKM0(); BAR();
    STAMP(3);
    const int fj1 = 4 * jq + (fr & 3), fk = 4 * k4 + (fr >> 2);
    unsigned fw[4], fwi[4];
    auto signal = [&](int cnt, int word) {
        if (lane == 0) { const unsigned old = __hip_atomic_fetch_add((LAS unsigned*)(L + L_FLG) + cnt, 1u, __ATOMIC_RELAXED, __HIP_MEMORY_SCOPE_WORKGROUP);
            if (old == 3u) __hip_atomic_store((gu32*)p.ws + word, tag, RLX_AGENT); }
    };
    if (wid < 3) {
        const f32x4 cg0 = csm[0], cg1 = csm[1], cu0 = csm[2], cu1 = csm[3];
#pragma unroll
        for (int mt = 0; mt < 8; ++mt) {
            const f32x2 st = ST[16 * mt + fr];
            const float r2 = st.y * st.y;
            const f32x4 h0 = (acc[0][mt] - st.x * cg0) * (acc[2][mt] - st.x * cu0) * r2;
            const f32x4 h1 = (acc[1][mt] - st.x * cg1) * (acc[3][mt] - st.x * cu1) * r2;
            st_sc1(rs, (int)WS_H + ((((r * 8 + mt) * 48 + 3 * c + wc) * 64 + lane) << 4), pack8(h0, h1));
        }
    } else if (wid == 3) {
        const float csf = (c == 0 && fr == 0) ? 512.0f : 0.0f;
#pragma unroll
        for (int mt = 0; mt < 8; ++mt) {
            const int m0 = 16 * mt + 4 * g;
            const f32x4 s01 = *(LAS f32x4*)(ST + m0), s23 = *(LAS f32x4*)(ST + m0 + 2);
            const int s2l = mt >> 1, s1 = 16 * (mt & 1) + 4 * g;
            *(LAS u32x2*)(L + L_XI + fr * 272 + s2l * 64 + s1 * 2) =
                u32x2{pk_bf16((acc[2][mt][0] - s01[0] * csf) * s01[1], (acc[2][mt][1] - s01[2] * csf) * s01[3]),
                      pk_bf16((acc[2][mt][2] - s23[0] * csf) * s23[1], (acc[2][mt][3] - s23[2] * csf) * s23[3])};
            *(LAS u32x2*)(L + L_XI + (16 + fr) * 272 + s2l * 64 + s1 * 2) =
                u32x2{pk_bf16(acc[3][mt][0] * s01[1], acc[3][mt][1] * s01[3]), pk_bf16(acc[3][mt][2] * s23[1], acc[3][mt][3] * s23[3])};
        }
        WAIT_LGKM0();
        flg[0] = tag;
        const f32x4 cg0 = csm[0], cu0 = csm[1];
#pragma unroll
        for (int mt = 0; mt < 8; ++mt) {
            const f32x2 st = ST[16 * mt + fr];
            const float r2 = st.y * st.y;
            const f32x4 h0 = (acc[0][mt] - st.x * cg0) * (acc[1][mt] - st.x * cu0) * r2;
            st_sc1_64(rs, (int)WS_H2 + ((((r * 8 + mt) * 16 + c) * 64 + lane) << 3), u32x2{pk_bf16(h0[0], h0[1]), pk_bf16(h0[2], h0[3])});
        }
    } else {
        const int mt = wid - 4;
        u32x4 eaC[2], eaS[2];
        float tc[2][4], tsn[2][4];
#pragma unroll
        for (int jt = 0; jt < 2; ++jt) {
            float cv[8], sv[8];
#pragma unroll
            for (int j = 0; j < 8; ++j) { const float th = (float)(((16 * jt + fr) * (8 * g + j)) & 31) * (1.0f / 32.0f); cv[j] = cosr(th); sv[j] = sinr(th); }
            eaC[jt] = u32x4{pk_bf16(cv[0], cv[1]), pk_bf16(cv[2], cv[3]), pk_bf16(cv[4], cv[5]), pk_bf16(cv[6], cv[7])};
            eaS[jt] = u32x4{pk_bf16(sv[0], sv[1]), pk_bf16(sv[2], sv[3]), pk_bf16(sv[4], sv[5]), pk_bf16(sv[6], sv[7])};
#pragma unroll
            for (int e = 0; e < 4; ++e) { const float th = (float)((16 * jt + fr) * (4 * r + e)) * (1.0f / 2048.0f); tc[jt][e] = cosr(th); tsn[jt][e] = sinr(th); }
        }
        SB();
#pragma unroll 1
        for (unsigned spins = 0; flg[0] != tag && spins < (1u << 20); ++spins) __builtin_amdgcn_s_sleep(1);
        asm volatile("" ::: "memory");
        const int kq_ = fr >> 2, s2l = fr & 3;
        const u32x4 are = lds128(L, L_XI + (4 * mt + kq_) * 272 + s2l * 64 + g * 16);
        const u32x4 aim = lds128(L, L_XI + (16 + 4 * mt + kq_) * 272 + s2l * 64 + g * 16);
        const f32x4 z = {0.f, 0.f, 0.f, 0.f};
        const bool special = (c == 0 && mt == 0 && g == 0);
#pragma unroll
        for (int jt = 0; jt < 2; ++jt) {
            const f32x4 Prc = mfma(are, eaC[jt], z), Pis = mfma(aim, eaS[jt], z), Prs = mfma(are, eaS[jt], z), Pic = mfma(aim, eaC[jt], z);
            const int j1 = 16 * jt + fr;
            float pr[4], qi[4], pr2[4], qi2[4];
#pragma unroll
            for (int e = 0; e < 4; ++e) {
                const float cw = tc[jt][e], sw = tsn[jt][e];
                const float ar = special ? Prc[e] : Prc[e] + Pis[e], ai = special ? -Prs[e] : Pic[e] - Prs[e];
                pr[e] = ar * cw + ai * sw; qi[e] = -(ai * cw - ar * sw);
                const float br = Pic[e], bi2 = -Pis[e];
                pr2[e] = br * cw + bi2 * sw; qi2[e] = -(bi2 * cw - br * sw);
            }
            const int U = (j1 >> 2) * 64 + 4 * c + mt, lp = 16 * (r & 3) + 4 * g + (j1 & 3);
            st_sc1(rs, (int)WS_AP + (((U * 4 + (r >> 2)) * 64 + lp) << 4), pack8(pr, qi));
            if (special) st_sc1(rs, (int)WS_AX + (((((j1 >> 2) * 4 + (r >> 2)) * 4 + (r & 3)) * 4 + (j1 & 3)) << 4), pack8(pr2, qi2));
        }
#pragma unroll
        for (int st = 0; st < 3; ++st) p3_issue(p, Lb, mt, cc, st, wv, fv);
        wait_vm<12>();
        signal(1, W_FA + (c * 16 + r) * FSTR);
#pragma unroll
        for (int st = 3; st < 9; ++st) p3_issue(p, Lb, mt, cc, st, wv, fv);
    }
#pragma unroll
    for (int e = 0; e < 4; ++e) { const unsigned o = (unsigned)((fj1 + 32 * (16 * w2 + 4 * g + e)) * DF + fk) * 2u; gl16(fw[e], p.wr, o); gl16(fwi[e], p.wi, o); }
    if (wid < 4) { wait_vm<8>(); signal(2, W_FH + (r * 16 + c) * FSTR); }
    STAMP(5);
    {
        const int kg = wid & 3, rh = wid >> 2;
        u32x4 E1[4], E2[4];
#pragma unroll
        for (int t = 0; t < 4; ++t) E1[t] = frag_cs(16 * w2 + fr, 16 * t + 4 * g, 64);
#pragma unroll
        for (int i = 0; i < 4; ++i) E2[i] = frag_cs(16 * (i >> 1) + fr, 16 * (i & 1) + 4 * g, 32);
        float tc[4], tsn[4];
#pragma unroll
        for (int e = 0; e < 4; ++e) { const float th = (float)((4 * jq + e) * (16 * w2 + fr)) * (1.0f / 2048.0f); tc[e] = cosr(th); tsn[e] = sinr(th); }
        P3Ctx cx;
        {
            const int qq = fr >> 2, pp = lane & 3, row = 32 * kg + 4 * g + qq, sw = 2 * ((row >> 1) & 3);
#pragma unroll
            for (int dt = 0; dt < 4; ++dt) cx.woff[dt] = row * 128 + (((2 * dt + (pp >> 1)) ^ sw) << 4) + 8 * (pp & 1);
            cx.hbase = (unsigned)WS_H + (unsigned)(((r3 * 8 + 4 * h3 + 2 * rh) * 48 + kg) * 64 + lane) * 16u;
            cx.h2base = (unsigned)WS_H2 + (unsigned)(((r3 * 8 + 4 * h3 + 2 * rh) * 16 + 2 * kg) * 64 + lane) * 8u;
            cx.tbase = (unsigned)WS_T + (unsigned)((((4 * r3 + 2 * h3 + rh) * 16 + 4 * kg) * 2) * 64 + lane) * 16u;
        }
        SB();
        STAMP(6);
        if (wid == 0) poll16(ws, W_FA + (kq >> 1) * 16 * FSTR, tag, lane);
        if (wid == 1) poll16(ws, W_FH + r3 * 16 * FSTR, tag, lane);
        BAR();
        STAMP(7);
        const int U = jq * 64 + k4;
        u32x4 Bf[4], Bx[4];
        u32x4 hb[HD][2]; u32x2 hh[2][4];
#pragma unroll
        for (int t = 0; t < 4; ++t) gl128_sc1(Bf[t], ws, (unsigned)WS_AP + (unsigned)((U * 4 + t) * 64 + lane) * 16u);
        {
            const unsigned xo = (unsigned)WS_AX + (unsigned)((jq * 16 + g) * 4 + (fr & 3)) * 16u;
            asm volatile("s_cmp_lg_u32 %6, 0\n\ts_cbranch_scc1 .Lnox%=\n\t"
                         "global_load_dwordx4 %0, %4, %5 sc1\n\tglobal_load_dwordx4 %1, %4, %5 offset:256 sc1\n\t"
                         "global_load_dwordx4 %2, %4, %5 offset:512 sc1\n\tglobal_load_dwordx4 %3, %4, %5 offset:768 sc1\n"
                         ".Lnox%=:" : "=&v"(Bx[0]), "=&v"(Bx[1]), "=&v"(Bx[2]), "=&v"(Bx[3]) : "v"(xo), "s"(ws), "s"(k4) : "memory", "scc");
        }
        {
            const int l56 = lane < 56 ? lane : 0, st = l56 >> 2, ln = (bi & 3) * 32 + wid * 4 + (l56 & 3), mtl = r3 * 8 + 4 * h3 + (ln >> 5), kx = (ln >> 3) & 3;
            glds4(ws + (st < 12 ? WS_H + (size_t)(mtl * 48 + 4 * st + kx) * 1024 : WS_H2 + (size_t)(mtl * 16 + 2 * (4 * (st - 12) + kx)) * 512) + (ln & 7) * 128, Lb + L_SCR);
        }
        static_for<0, HD>([&](auto Sx) { constexpr int o = Sx.v;
            gl128_sc1(hb[o][0], ws, cx.hbase + o * 4096u); gl128_sc1(hb[o][1], ws, cx.hbase + 48u * 1024u + o * 4096u); });
        wait_vm<1 + 2 * HD>();
        pin(Bf[0]); pin(Bf[1]); pin(Bf[2]); pin(Bf[3]); pin(Bx[0]); pin(Bx[1]); pin(Bx[2]); pin(Bx[3]);
        pin(fw[0]); pin(fw[1]); pin(fw[2]); pin(fw[3]); pin(fwi[0]); pin(fwi[1]); pin(fwi[2]); pin(fwi[3]);
        STAMP(8);
        {
            f32x4 a1 = {0.f, 0.f, 0.f, 0.f}, a2 = a1;
#pragma unroll
            for (int t = 0; t < 4; ++t) { a1 = mfma(E1[t], Bf[t], a1); a2 = mfma(E1[t], swap_neg(Bf[t]), a2); }
            constexpr float SC = 2.0f / (2048.0f * 512.0f);
            float yr[4], yi[4];
#pragma unroll
            for (int e = 0; e < 4; ++e) { const float xr = a1[e], xi = -a2[e], wre = bf_lo(fw[e]), wim = bf_lo(fwi[e]);
                yr[e] = (xr * wre - xi * wim) * SC; yi[e] = (xr * wim + xi * wre) * SC; }
            if (k4 == 0) {
                f32x4 n1 = {0.f, 0.f, 0.f, 0.f}, n2 = n1;
#pragma unroll
                for (int t = 0; t < 4; ++t) { n1 = mfma(E1[t], Bx[t], n1); n2 = mfma(E1[t], swap_neg(Bx[t]), n2); }
                if ((fr >> 2) == 0) {
#pragma unroll
                    for (int e = 0; e < 4; ++e) {
                        const f32x4 wh = *(LAS f32x4*)(L + L_WH + (w2 * 64 + 4 * (4 * g + e) + (fr & 3)) * 16);
                        const float x0r = a1[e], x0i = -a2[e], x2r = n1[e], x2i = -n2[e];
                        const float y0r = x0r * wh[0] - x0i * wh[1], y0i = x0r * wh[1] + x0i * wh[0];
                        const float y2r = x2r * wh[2] - x2i * wh[3], y2i = x2r * wh[3] + x2i * wh[2];
                        yr[e] = (y0r - y2i) * (0.5f * SC); yi[e] = (y0i + y2r) * (0.5f * SC); }
                }
            }
            *(LAS u32x4*)(L + L_XI + ((u2 * 4 + w2) * 64 + lane) * 16) = pack8(yr, yi);
        }
        WAIT_LGKM0(); BAR();
        u32x4 wf[2][4];
        p3_rdw(L, cx, kg, lane, 0, wf[0]);
        {
            f32x4 t1 = {0.f, 0.f, 0.f, 0.f}, t2 = t1;
#pragma unroll
            for (int t = 0; t < 4; ++t) { const u32x4 y = lds128(L, L_XI + ((u2 * 4 + t) * 64 + lane) * 16);
                t1 = mfma(y, E1[t], t1); t2 = mfma(swap_neg(y), E1[t], t2); }
            const int s2 = 16 * w2 + fr, ko = 4 * k4 + g;
            float tr[4], ti[4];
#pragma unroll
            for (int e = 0; e < 4; ++e) { tr[e] = t1[e] * tc[e] - t2[e] * tsn[e]; ti[e] = t2[e] * tc[e] + t1[e] * tsn[e]; }
            st_sc1(rs, (int)WS_T + ((((s2 * 16 + (ko >> 4)) * 2 + (jq >> 2)) * 64 + (jq & 3) * 16 + (ko & 15)) << 4), pack8(tr, ti));
        }
        u32x4 xres; gl128(xres, (const unsigned char*)p.x + ((size_t)panel_row(r3, 64 * h3 + (tid >> 3)) * 512 + 64 * cc + 8 * (tid & 7)) * 2);
        STAMP(9);
        f32x4 acc3[4][2];
#pragma unroll
        for (int dt = 0; dt < 4; ++dt) { acc3[dt][0] = f32x4{0.f, 0.f, 0.f, 0.f}; acc3[dt][1] = acc3[dt][0]; }
        unsigned ftf;
        static_for<0, 18>([&](auto Sx) {
            constexpr int s = Sx.v;
            if (wid >= 4) wait_vm<p3_wait(s, true)>(); else wait_vm<p3_wait(s, false)>();
            if constexpr (s == 12 || s == 13) { pin(hh[s - 12][0]); pin(hh[s - 12][1]); pin(hh[s - 12][2]); pin(hh[s - 12][3]); }
            else { pin(hb[s % HD][0]); pin(hb[s % HD][1]); }
            if constexpr (s == 2) {
                if (lane == 0) { const unsigned old = __hip_atomic_fetch_add((LAS unsigned*)(L + L_FLG) + 3, 1u, __ATOMIC_RELAXED, __HIP_MEMORY_SCOPE_WORKGROUP);
                    if (old == 7u) __hip_atomic_fetch_add((gu32*)p.ws + W_CT + (kq >> 1) * 32, 1u, RLX_AGENT); }
            }
            if constexpr (s == 14 - HD) {
                pin(ftf);
                if (wid == 0) {
                    if (!__all(ftf == (tag << 8) + 16u)) {
#pragma unroll 1
                        for (unsigned spins = 0;; ++spins) {
                            __builtin_amdgcn_s_sleep(PSLEEP);
                            unsigned f2; gl32_sc1(f2, ws, (unsigned)(W_CT + (lane & 15) * 32) * 4u); wait_vm<0>(); pin(f2);
                            if (__all(f2 == (tag << 8) + 16u)) break;
                            if (spins > SPIN_CAP) { give_up(ws, lane); break; }
                        }
                    }
                    if (b == 0 && lane == 0) __hip_atomic_store((gu32*)p.ws, tag, RLX_AGENT);
                }
            }
            if constexpr (s % 3 == 0 && s > 0) {
                WAIT_LGKM0(); BAR();
                if constexpr (s + 6 < 18) { if (wid >= 4) { p3_issue(p, Lb, wid - 4, cc, s + 6, wv, fv); p3_issue(p, Lb, wid - 4, cc, s + 7, wv, fv); p3_issue(p, Lb, wid - 4, cc, s + 8, wv, fv); } }
            }
            TS(10 + s);
            if constexpr (s + 1 < 18) p3_rdw(L, cx, kg, lane, s + 1, wf[(s + 1) & 1]);
            u32x4 b0, b1;
            if constexpr (s == 12 || s == 13) { b0 = u32x4{hh[s - 12][0].x, hh[s - 12][0].y, hh[s - 12][1].x, hh[s - 12][1].y}; b1 = u32x4{hh[s - 12][2].x, hh[s - 12][2].y, hh[s - 12][3].x, hh[s - 12][3].y}; }
            else { b0 = hb[s % HD][0]; b1 = hb[s % HD][1]; }
            if constexpr (s >= 14) {
                const u32x4 T0 = b0, T1 = b1, T0s = swap_neg(T0), T1s = swap_neg(T1);
                u32x4 zf[2];
#pragma unroll
                for (int st = 0; st < 2; ++st) {
                    f32x4 zr = {0.f, 0.f, 0.f, 0.f}, zi = zr;
                    zr = mfma(T0, E2[st * 2 + 0], zr); zr = mfma(T1, E2[st * 2 + 1], zr);
                    zi = mfma(T0s, E2[st * 2 + 0], zi); zi = mfma(T1s, E2[st * 2 + 1], zi);
                    zf[st] = pack8(zr, zi);
                }
                b0 = zf[0]; b1 = zf[1];
            }
#pragma unroll
            for (int dt = 0; dt < 4; ++dt) { acc3[dt][0] = mfma(wf[s & 1][dt], b0, acc3[dt][0]); acc3[dt][1] = mfma(wf[s & 1][dt], b1, acc3[dt][1]); }
            SB();
            if constexpr (s == 14 - HD) {
                const int ln = (bi & 3) * 128 + wid * 16 + (lane & 15);
                glds4(ws + WS_T + (size_t)(4 * r3 + 2 * h3 + (ln >> 8)) * 32768 + (ln & 255) * 128, Lb + L_SCR);
            }
            if constexpr (s + HD < 18) {
                constexpr int o = s + HD;
                if constexpr (o < 12) { gl128_sc1(hb[o % HD][0], ws, cx.hbase + o * 4096u); gl128_sc1(hb[o % HD][1], ws, cx.hbase + 48u * 1024u + o * 4096u); }
                else if constexpr (o < 14) {
                    gl64_sc1(hh[o - 12][0], ws, cx.h2base + (o - 12) * 4096u); gl64_sc1(hh[o - 12][1], ws, cx.h2base + (o - 12) * 4096u + 512u);
                    gl64_sc1(hh[o - 12][2], ws, cx.h2base + 8192u + (o - 12) * 4096u); gl64_sc1(hh[o - 12][3], ws, cx.h2base + 8192u + (o - 12) * 4096u + 512u); }
                else { gl128_sc1(hb[o % HD][0], ws, cx.tbase + (o - 14) * 2048u); gl128_sc1(hb[o % HD][1], ws, cx.tbase + (o - 14) * 2048u + 1024u); }
            }
            if constexpr (s == FTS) gl32_sc1(ftf, ws, (unsigned)(W_CT + (wid == 0 ? (lane & 15) * 32 : 0)) * 4u);
            SB();
        });
        STAMP(10);
        TS(28);
        wait_vm<0>(); pin(xres);
        WAIT_LGKM0(); BAR();
        constexpr int RP = 272;
#pragma unroll
        for (int dt = 0; dt < 4; ++dt)
#pragma unroll
            for (int st = 0; st < 2; ++st)
                *(LAS f32x4*)(L + (kg * 64 + 32 * rh + 16 * st + fr) * RP + (16 * dt + 4 * g) * 4) = acc3[dt][st];
        WAIT_LGKM0(); BAR();
        const int mm = tid >> 3, cg = tid & 7;
        f32x4 v0 = {0.f, 0.f, 0.f, 0.f}, v1 = v0;
#pragma unroll
        for (int q = 0; q < 4; ++q) { v0 += *(LAS f32x4*)(L + (q * 64 + mm) * RP + cg * 32); v1 += *(LAS f32x4*)(L + (q * 64 + mm) * RP + cg * 32 + 16); }
        const size_t o = (size_t)panel_row(r3, 64 * h3 + mm) * 512 + 64 * cc + 8 * cg;
        *(GAS u32x4*)(p.out + o) = u32x4{pk_bf16(bf_lo(xres.x) + v0[0], bf_hi(xres.x) + v0[1]), pk_bf16(bf_lo(xres.y) + v0[2], bf_hi(xres.y) + v0[3]),
                                         pk_bf16(bf_lo(xres.z) + v1[0], bf_hi(xres.z) + v1[1]), pk_bf16(bf_lo(xres.w) + v1[2], bf_hi(xres.w) + v1[3])};
    }
    STAMP(11);
}

extern "C" void kernel_launch(void* const* d_in, const int* in_sizes, int n_in, void* d_out, int out_size, void* d_ws, size_t ws_size,
                              hipStream_t stream) {
    Params p;
    p.x = (const uint16_t*)d_in[0]; p.wr = (const uint16_t*)d_in[1]; p.wi = (const uint16_t*)d_in[2];
    p.wg = (const uint16_t*)d_in[4]; p.wu = (const uint16_t*)d_in[5]; p.wd = (const uint16_t*)d_in[6];
    p.out = (uint16_t*)d_out; p.ws = (unsigned char*)d_ws;
    (void)hipFuncSetAttribute((const void*)k_fused, hipFuncAttributeMaxDynamicSharedMemorySize, LDS_BYTES);
    k_fused<<<256, NT, LDS_BYTES, stream>>>(p);
}
```

```cpp
#include <hip/hip_runtime.h>
#include <stdint.h>

#define LAS __attribute__((address_space(3)))
#define GAS __attribute__((address_space(1)))
#define DI __device__ __forceinline__
typedef __bf16 bf8 __attribute__((ext_vector_type(8)));
typedef __bf16 bf2 __attribute__((ext_vector_type(2)));
typedef short s16x4 __attribute__((ext_vector_type(4)));
typedef short s16x8 __attribute__((ext_vector_type(8)));
typedef float f32x4 __attribute__((ext_vector_type(4)));
typedef float f32x2 __attribute__((ext_vector_type(2)));
typedef unsigned u32x4 __attribute__((ext_vector_type(4)));
typedef unsigned u32x2 __attribute__((ext_vector_type(2)));
typedef GAS unsigned gu32;
typedef GAS unsigned long long gu64;
#define RLX_AGENT __ATOMIC_RELAXED, __HIP_MEMORY_SCOPE_AGENT

namespace tabs {
constexpr double PI = 3.14159265358979323846264338327950288;
constexpr double cos_q(double x) {
    double x2 = x * x, t = 1.0, s = 1.0;
    for (int i = 1; i <= 14; ++i) { t *= -x2 / ((2 * i - 1) * (2 * i)); s += t; }
    return s;
}
constexpr uint16_t f2bf(float f) {
    uint32_t u = __builtin_bit_cast(uint32_t, f);
    u += 0x7FFFu + ((u >> 16) & 1u);
    return (uint16_t)(u >> 16);
}
struct Base { uint16_t v[512];
    constexpr Base() : v{} {
        for (int m = 0; m <= 128; ++m) {
            double c = (m == 128) ? 0.0 : cos_q(2.0 * PI * m / 512.0);
            uint16_t b = f2bf((float)c);
            uint16_t nb = (m == 128) ? b : (uint16_t)(b ^ 0x8000u);
            v[m] = b; v[(512 - m) & 511] = b;
            v[256 - m] = nb; v[(256 + m) & 511] = nb;
        }
    }
};
constexpr Base base{};
constexpr uint16_t cosb(int m) { return base.v[m & 511]; }
constexpr uint16_t nsinb(int m) { return base.v[(m + 128) & 511]; }
constexpr uint16_t sinb(int m) { return base.v[(m + 384) & 511]; }
struct Chunk { uint16_t v[16384]; };
constexpr Chunk mk_fwd(int c) {
    Chunk r{};
    for (int d = 0; d < 512; ++d)
        for (int n = 0; n < 32; ++n) { const int k = 16 * c + (n & 15);
            r.v[d * 32 + n] = base.v[((n >> 4) ? (k == 0 ? d * 256 : d * k + 128) : d * k) & 511]; }
    return r;
}
constexpr Chunk mk_inv(int blk) {
    Chunk r{};
    const int cc = blk >> 1;
    for (int e = 0; e < 16384; e += 8) {
        const int l = (e >> 3) & 63, dt = (e >> 9) & 3, kg = (e >> 11) & 3, s = ((blk & 1) << 1) + (e >> 13);
        const int kt = 4 * kg + s, d = 64 * cc + 16 * dt + (l & 15), k0 = 16 * kt + 4 * (l >> 4);
        for (int j = 0; j < 4; ++j) { const int k = k0 + j;
            r.v[e + j] = base.v[(k * d) & 511];
            r.v[e + 4 + j] = base.v[(k == 0 ? d * 256 : k * d + 128) & 511]; }
    }
    return r;
}
template <int I> constexpr Chunk fwd_c = mk_fwd(I);
template <int I> constexpr Chunk inv_c = mk_inv(I);
struct Big { Chunk c[16]; };
}
#define C16(N) {{tabs::N<0>, tabs::N<1>, tabs::N<2>, tabs::N<3>, tabs::N<4>, tabs::N<5>, tabs::N<6>, tabs::N<7>, \
                 tabs::N<8>, tabs::N<9>, tabs::N<10>, tabs::N<11>, tabs::N<12>, tabs::N<13>, tabs::N<14>, tabs::N<15>}}
alignas(256) static __device__ const tabs::Big g_fwd = C16(fwd_c);
alignas(256) static __device__ const tabs::Big g_inv = C16(inv_c);

constexpr int S = 2048, D = 512, F = 1792, DF = 257, NT = 512;
constexpr size_t WS_CTRL = 0;
constexpr size_t WS_STAT = 98304;
constexpr size_t WS_AP = 131072;
constexpr size_t WS_AX = WS_AP + (2u << 20);
constexpr size_t WS_TX = WS_AX + 8192;
constexpr size_t WS_T = WS_AX + 65536;
constexpr size_t WS_H = WS_T + (2u << 20);
constexpr size_t WS_H2 = WS_H + (size_t)16 * 8 * 48 * 1024;
constexpr size_t WS_END = WS_H2 + (size_t)16 * 8 * 16 * 512;
#define FSTR 32
#define PSLEEP 8
constexpr int W_CT = 1024, W_TMO = 2048, W_FA = 4096, W_FH = W_FA + 256 * 32;
constexpr unsigned SPIN_CAP = 1u << 16;

struct Params {
    const uint16_t* x; const uint16_t* wr; const uint16_t* wi;
    const uint16_t* wg; const uint16_t* wu; const uint16_t* wd;
    uint16_t* out; unsigned char* ws;
};

template <int I> struct IC { static constexpr int v = I; };
template <int B, int E, class Fn> DI void static_for(Fn&& f) { if constexpr (B < E) { f(IC<B>{}); static_for<B + 1, E>(f); } }
DI f32x4 mfma(u32x4 a, u32x4 b, f32x4 c) {
    return __builtin_amdgcn_mfma_f32_16x16x32_bf16(__builtin_bit_cast(bf8, a), __builtin_bit_cast(bf8, b), c, 0, 0, 0);
}
DI unsigned pk_bf16(float lo, float hi) { bf2 p; p[0] = (__bf16)lo; p[1] = (__bf16)hi; return __builtin_bit_cast(unsigned, p); }
DI float bf_lo(unsigned u) { return __builtin_bit_cast(float, u << 16); }
DI float bf_hi(unsigned u) { return __builtin_bit_cast(float, u & 0xffff0000u); }
DI float bfr(float x) { return bf_lo(pk_bf16(x, x)); }
DI u32x4 lds128(LAS unsigned char* L, int off) { return *(LAS u32x4*)(L + off); }
DI u32x2 lds_tr(LAS unsigned char* L, int off) {
    return __builtin_bit_cast(u32x2, __builtin_amdgcn_ds_read_tr16_b64_v4i16((LAS s16x4*)(L + off)));
}
DI u32x4 lds_tr2(LAS unsigned char* L, int off0, int off1) {
    const u32x2 a = lds_tr(L, off0), b = lds_tr(L, off1);
    return u32x4{a.x, a.y, b.x, b.y};
}
DI u32x4 swap_neg(u32x4 v) { return u32x4{v.z, v.w, v.x ^ 0x80008000u, v.y ^ 0x80008000u}; }
DI u32x4 pack8(const float (&a)[4], const float (&b)[4]) { return u32x4{pk_bf16(a[0], a[1]), pk_bf16(a[2], a[3]), pk_bf16(b[0], b[1]), pk_bf16(b[2], b[3])}; }
DI u32x4 pack8(f32x4 a, f32x4 b) { return u32x4{pk_bf16(a[0], a[1]), pk_bf16(a[2], a[3]), pk_bf16(b[0], b[1]), pk_bf16(b[2], b[3])}; }
DI void glds16(const void* gsrc, unsigned lds_addr) {
    asm volatile("s_mov_b32 m0, %1\n\ts_nop 0\n\tglobal_load_lds_dwordx4 %0, off" :: "v"(gsrc), "s"(lds_addr) : "memory");
}
DI void glds16s(const void* sbase, unsigned voff, unsigned lds_addr) {
    asm volatile("s_mov_b32 m0, %2\n\ts_nop 0\n\tglobal_load_lds_dwordx4 %0, %1" :: "v"(voff), "s"(sbase), "s"(lds_addr) : "memory");
}
DI void glds4(const void* gsrc, unsigned lds_addr) {
    asm volatile("s_mov_b32 m0, %1\n\ts_nop 0\n\tglobal_load_lds_dword %0, off" :: "v"(gsrc), "s"(lds_addr) : "memory");
}
DI void gl128(u32x4& d, const void* p) { asm volatile("global_load_dwordx4 %0, %1, off" : "=v"(d) : "v"(p) : "memory"); }
DI void gl128_sc1(u32x4& d, const void* base, unsigned off) { asm volatile("global_load_dwordx4 %0, %1, %2 sc1" : "=v"(d) : "v"(off), "s"(base) : "memory"); }
DI void gl64_sc1(u32x2& d, const void* base, unsigned off) { asm volatile("global_load_dwordx2 %0, %1, %2 sc1" : "=v"(d) : "v"(off), "s"(base) : "memory"); }
DI void gl32_sc1(unsigned& d, const void* base, unsigned off) { asm volatile("global_load_dword %0, %1, %2 sc1" : "=v"(d) : "v"(off), "s"(base) : "memory"); }
DI void gl16(unsigned& d, const void* base, unsigned off) { asm volatile("global_load_ushort %0, %1, %2" : "=v"(d) : "v"(off), "s"(base) : "memory"); }
template <int N> DI void wait_vm() { static_assert(N >= 0 && N < 64, "vmcnt"); asm volatile("s_waitcnt vmcnt(%0)" :: "n"(N) : "memory"); }
template <class T> DI void pin(T& v) { asm volatile("" : "+v"(v)); }
#define WAIT_LGKM0() asm volatile("s_waitcnt lgkmcnt(0)" ::: "memory")
#define BAR() do { asm volatile("" ::: "memory"); __builtin_amdgcn_s_barrier(); asm volatile("" ::: "memory"); } while (0)
#define SB() __builtin_amdgcn_sched_barrier(0)
DI void st_sc1(__amdgpu_buffer_rsrc_t rs, int off, u32x4 v) { __builtin_amdgcn_raw_buffer_store_b128(v, rs, off, 0, 16); }
DI void st_sc1_64(__amdgpu_buffer_rsrc_t rs, int off, u32x2 v) { __builtin_amdgcn_raw_buffer_store_b64(v, rs, off, 0, 16); }
template <int CTRL> DI float dpp_add(float v) {
    return v + __builtin_bit_cast(float, __builtin_amdgcn_update_dpp(0, __builtin_bit_cast(int, v), CTRL, 0xf, 0xf, true));
}
DI float wave_sum(float v) {
    v = dpp_add<0xB1>(v);
    v = dpp_add<0x4E>(v);
    v = dpp_add<0x141>(v);
    v = dpp_add<0x140>(v);
    const int vi = __builtin_bit_cast(int, v);
    return (__builtin_bit_cast(float, __builtin_amdgcn_readlane(vi, 0)) + __builtin_bit_cast(float, __builtin_amdgcn_readlane(vi, 16))) +
           (__builtin_bit_cast(float, __builtin_amdgcn_readlane(vi, 32)) + __builtin_bit_cast(float, __builtin_amdgcn_readlane(vi, 48)));
}
DI void give_up(const unsigned char* ws, int lane) { if (lane == 0) __hip_atomic_store((gu32*)ws + W_TMO, 1u, RLX_AGENT); }
DI void poll16(const unsigned char* ws, int word, unsigned tag, int lane) {
#pragma unroll 1
    for (unsigned spins = 0;; ++spins) {
        unsigned f; gl32_sc1(f, ws, (unsigned)(word + (lane & 15) * FSTR) * 4u); wait_vm<0>(); pin(f);
        if (__all(f == tag)) break;
        if (spins > SPIN_CAP) { give_up(ws, lane); break; }
        __builtin_amdgcn_s_sleep(PSLEEP);
    }
}
DI void fx_load(unsigned (&w)[8], unsigned o0, unsigned o1, unsigned o2, unsigned o3, const void* wr, const void* wi, int k4) {
    asm volatile("s_cmp_lg_u32 %14, 0\n\ts_cbranch_scc1 .Lnofx%=\n\t"
                 "global_load_ushort %0, %8, %12\n\tglobal_load_ushort %1, %9, %12\n\tglobal_load_ushort %2, %10, %12\n\tglobal_load_ushort %3, %11, %12\n\t"
                 "global_load_ushort %4, %8, %13\n\tglobal_load_ushort %5, %9, %13\n\tglobal_load_ushort %6, %10, %13\n\tglobal_load_ushort %7, %11, %13\n"
                 ".Lnofx%=:" : "=&v"(w[0]), "=&v"(w[1]), "=&v"(w[2]), "=&v"(w[3]), "=&v"(w[4]), "=&v"(w[5]), "=&v"(w[6]), "=&v"(w[7])
                 : "v"(o0), "v"(o1), "v"(o2), "v"(o3), "s"(wr), "s"(wi), "s"(k4) : "memory", "scc");
}
DI float cosr(float x) { return __builtin_amdgcn_cosf(x); }
DI float sinr(float x) { return __builtin_amdgcn_sinf(x); }
DI u32x4 frag_cs(int a, int b0, int n) {
    float c[4], sn[4];
#pragma unroll
    for (int j = 0; j < 4; ++j) { const float th = (float)((a * (b0 + j)) & (n - 1)) * (1.0f / n); c[j] = cosr(th); sn[j] = -sinr(th); }
    return pack8(c, sn);
}

#define STAMP(i) do {} while (0)
#define TS(i) do {} while (0)

constexpr int P1_STAGE = 49152, P1_BOFF = 16384;
constexpr int L_MISC = 147456;
constexpr int L_XI = L_MISC;
constexpr int L_ST = L_MISC + 8704;
constexpr int L_YX = L_ST + 1024;
constexpr int L_SCR = L_YX + 4096;
constexpr int L_FLG = L_SCR + 256;
constexpr int L_END = L_FLG + 16;
static_assert(L_END <= 163840, "lds");
constexpr int LDS_BYTES = 163840;
constexpr int P3_SLOT = 16384;
DI constexpr int p3_slot(int s) { return (s % 9) * P3_SLOT; }

DI int panel_row(int r, int m) { return 64 * (m & 31) + 4 * r + (m >> 5); }
DI int p3_ks(int s, int kg) { const int n = 4 * s + kg; if (s >= 12) return 7 * (n - 48) + 3; const int p = n / 6, o = n - 6 * p; return 7 * p + o + (o >= 3); }

struct LdSrc { unsigned a[4]; const unsigned char* b[8]; long sB64; const unsigned char* t; long tadv; };
DI void p1_issue(LdSrc& s, const unsigned char* x, unsigned Lb, int lw, int j) {
    const unsigned sb = Lb + (j % 3) * P1_STAGE;
#pragma unroll
    for (int i = 0; i < 4; ++i) glds16s(x + j * 128, s.a[i], sb + (4 * lw + i) * 1024);
#pragma unroll
    for (int u = 0; u < 8; ++u) { const int i = (lw & 1) + 2 * (u & 1) + 4 * (lw >> 1) + 8 * ((u >> 1) & 1) + 16 * (u >> 2);
        glds16(s.b[u], sb + P1_BOFF + i * 1024); s.b[u] += s.sB64; }
}
DI void p1_touch(LdSrc& s, unsigned Lb) { glds4(s.t, Lb + L_SCR); s.t += s.tadv; }
DI void p3_issue(const Params& p, unsigned Lb, int lw, int cc, int s, unsigned wv, unsigned fv) {
    const unsigned sb = Lb + p3_slot(s) + lw * 4096;
    if (s < 14) { const unsigned char* base = (const unsigned char*)p.wd + (size_t)p3_ks(s, lw) * 32768;
#pragma unroll
        for (int i = 0; i < 4; ++i) glds16s(base + i * 8192, wv, sb + i * 1024); }
    else { const unsigned char* base = (const unsigned char*)&g_inv + cc * 65536 + (s - 14) * 16384 + lw * 4096;
#pragma unroll
        for (int i = 0; i < 4; ++i) glds16s(base + i * 1024, fv, sb + i * 1024); }
}

DI void p3_issue2(const Params& p, unsigned Lb, int w, int cc, int s, unsigned wv, unsigned fv) {
    const unsigned sb = Lb + p3_slot(s) + w * 2048;
    if (s < 14) { const unsigned char* base = (const unsigned char*)p.wd + (size_t)p3_ks(s, w >> 1) * 32768 + (w & 1) * 16384;
        glds16s(base, wv, sb); glds16s(base + 8192, wv, sb + 1024); }
    else { const unsigned char* base = (const unsigned char*)&g_inv + cc * 65536 + (s - 14) * 16384 + w * 2048;
        glds16s(base, fv, sb); glds16s(base + 1024, fv, sb + 1024); }
}

template <bool DFTW>
DI void p1_compute(LAS unsigned char* L, unsigned Lb, int wc, int lane, f32x4 (&acc)[4][8], f32x4 (&csm)[4], unsigned (&tsv)[32]) {
    asm volatile("" : "+v"(lane));
    const int fr = lane & 15, g = lane >> 4;
    const int qq = fr >> 2, p = lane & 3;
    int aoff0 = fr * 128 + (((0 + g) ^ (fr & 7)) << 4);
    int aoff1 = fr * 128 + (((4 + g) ^ (fr & 7)) << 4);
    const int sw = 2 * qq + 8 * (g & 1);
    int boff[4];
#pragma unroll
    for (int q = 0; q < 4; ++q) boff[q] = P1_BOFF + (8 * g + qq) * 512 + (((8 * wc + 2 * q + (p >> 1)) ^ sw) << 4) + 8 * (p & 1);
    u32x4 xa[2][4], wb[2][4];
    auto rdx = [&](int ks, int mh, int i) { xa[mh][i] = lds128(L, (ks ? aoff1 : aoff0) + (4 * mh + i) * 2048); };
    auto rdw = [&](int ks, int q) { const int o = boff[q] + ks * 16384; wb[ks][q] = lds_tr2(L, o, o + 2048); };
    auto rebase = [&](int d) { aoff0 += d; aoff1 += d; boff[0] += d; boff[1] += d; boff[2] += d; boff[3] += d;
        asm volatile("" : "+v"(aoff0), "+v"(aoff1), "+v"(boff[0]), "+v"(boff[1]), "+v"(boff[2]), "+v"(boff[3])); };
    auto step = [&](int ks, int mh, bool more) {
#pragma unroll
        for (int i = 0; i < 16; ++i) {
            const int q = i >> 2, mt = i & 3;
            if (DFTW && q >= 2) acc[q][4 * mh + mt] = mfma(xa[mh][mt], wb[ks][q], acc[q][4 * mh + mt]);
            else acc[q][4 * mh + mt] = mfma(wb[ks][q], xa[mh][mt], acc[q][4 * mh + mt]);
            if (more) {
                if (mh == 0) { if (i >= 1 && i <= 4) rdx(ks, 1, i - 1); }
                else { if (i < 4) rdx(ks ^ 1, 0, i); else if (i < 8) rdw(ks ^ 1, i - 4); }
            }
            if (mh == 0 && (i & 3) == 3) { const int d = i >> 2; if (!DFTW || d < 2) csm[d] = mfma(wb[ks][d], u32x4{0x3F803F80u, 0x3F803F80u, 0x3F803F80u, 0x3F803F80u}, csm[d]); }
            SB();
        }
    };
    BAR();
    TS(0);
#pragma unroll
    for (int i = 0; i < 4; ++i) { rdx(0, 0, i); rdw(0, i); }
    SB();
    static_for<0, 8>([&](auto J) {
        constexpr int j = J.v;
        step(0, 0, true);
        step(0, 1, true);
        step(1, 0, true);
        if constexpr (j < 7) { WAIT_LGKM0(); BAR(); TS(j + 1); rebase((j + 1) % 3 == 0 ? -2 * P1_STAGE : P1_STAGE);
            SB(); }
        step(1, 1, j < 7);
    });
    TS(8);
}

constexpr int HD = 5;
constexpr int FTS = 6;
enum { K_OP, K_TST, K_W, K_FT };
constexpr int p3_after(int kind, int idx, int s_now) {
    int n = -1;
    auto hit = [&](bool is, int cnt) { if (is) n = 0; else if (n >= 0) n += cnt; };
    for (int o = 0; o < HD; ++o) hit(kind == K_OP && idx == o, 2);
    for (int w = 3; w < 9; ++w) hit(kind == K_W && idx == w, 2);
    hit(kind == K_TST, 1); hit(false, 1);
    for (int s = 0; s < s_now; ++s) {
        if (s + HD < 18) hit(kind == K_OP && idx == s + HD, (s + HD >= 12 && s + HD <= 14) ? 4 : 2);
        if (s >= 3 && s + 6 < 18) hit(kind == K_W && idx == s + 6, 2);
        if (s == FTS) hit(kind == K_FT, 1);
    }
    return n;
}
constexpr int cmin(int a, int b) { return a < b ? a : b; }
constexpr int p3_wait(int s) {
    int n = p3_after(K_OP, s, s);
    if (s == 2) n = cmin(n, p3_after(K_TST, 0, s));
    if (s % 3 == 0 && s >= 3) n = cmin(n, p3_after(K_W, s + 2, s));
    if (s == 14 - HD) n = cmin(n, p3_after(K_FT, 0, s));
    return n;
}
static_assert(p3_wait(0) == 2 * (HD - 1) + 12 + 2 && p3_wait(2) == 1 + 4, "queue model");

struct P3Ctx { unsigned hbase, h2base, tbase; int woff[4]; };
DI void p3_rdw(LAS unsigned char* L, const P3Ctx& c, int kg, int lane, int s, u32x4 (&wf)[4]) {
#pragma unroll
    for (int dt = 0; dt < 4; ++dt) {
        if (s < 14) wf[dt] = lds_tr2(L, p3_slot(s) + c.woff[dt], p3_slot(s) + c.woff[dt] + 2048);
        else wf[dt] = lds128(L, p3_slot(s) + (kg * 4 + dt) * 1024 + lane * 16);
    }
}

__global__ void __launch_bounds__(NT, 2) k_fused(Params p) {
    extern __shared__ __attribute__((aligned(16))) unsigned char smem[];
    LAS unsigned char* L = (LAS unsigned char*)smem;
    const unsigned Lb = (unsigned)(size_t)L;
    const int wid = __builtin_amdgcn_readfirstlane(threadIdx.x >> 6);
    const int wc = wid & 3;
    const int b = blockIdx.x, xcd = b & 7, bi = b >> 3;
    const int r = (xcd & 1) * 8 + (bi & 7), c = (xcd >> 1) * 4 + (bi >> 3);
    const int jq = b & 7, kq = b >> 3;
    const int cc = (xcd & 1) * 4 + (bi & 3), hp = (xcd >> 1) * 8 + (bi >> 2), r3 = hp >> 1, h3 = hp & 1;
    const unsigned char* ws = p.ws;
    unsigned tsv[32];
#pragma unroll
    for (int i = 0; i < 32; ++i) tsv[i] = 0;
    STAMP(0);
    TS(31);
    const int odd = c & 1;
    const int u2 = wid >> 2, w2 = wid & 3, k4 = 2 * kq + u2;
    LAS f32x2* ST = (LAS f32x2*)(L + L_ST);
    f32x4 acc[4][8], csm[4];
#pragma unroll
    for (int i = 0; i < 4; ++i) csm[i] = f32x4{0.f, 0.f, 0.f, 0.f};
    unsigned tag;
    {
    const int tid = threadIdx.x, lane = tid & 63;
    if (wid >= 4) {
        const int lw = wid - 4;
        unsigned epoch; gl32_sc1(epoch, ws, 0u);
        LdSrc ls;
        {
            const int idx = lw * 22 + (lane < 22 ? lane : 0);
            if (idx < 32) { ls.t = (const unsigned char*)p.x + (size_t)panel_row(r, 32 * (c & 3) + idx) * 1024; ls.tadv = 128; }
            else { const int i = idx - 32, row = 8 * (r & 7) + i / 7, it = i % 7;
                if (it == 6) { ls.t = (const unsigned char*)&g_fwd + c * 32768 + row * 64; ls.tadv = 64 * 64; }
                else { const int it3 = it % 3; ls.t = (const unsigned char*)(it < 3 ? p.wg : p.wu) + (size_t)row * (F * 2) + c * 224 + (it3 == 2 ? 220 : it3 * 128); ls.tadv = 64L * F * 2; } }
        }
        p1_touch(ls, Lb);
        SB();
#pragma unroll
        for (int i = 0; i < 4; ++i) { const int m = 8 * (4 * lw + i) + (lane >> 3); ls.a[i] = (unsigned)panel_row(r, m) * 1024u + (((lane & 7) ^ (lane >> 3)) << 4); }
        {
            const int b0 = lw & 1, b2 = lw >> 1;
            const int swz = 2 * (2 * b0 + (lane >> 5)) + 8 * b2;
            const int ch = (lane & 31) ^ swz, nt = ch >> 1, half = ch & 1, cw = nt >> 2, q = nt & 3;
            const unsigned char* base; int col, strideB;
            if (cw < 3) { const int ft = odd + 2 * cw + (q & 1); base = (const unsigned char*)(q < 2 ? p.wg : p.wu); col = 112 * c + 16 * ft + 8 * half; strideB = F * 2; }
            else if (q < 2) { const int ft = odd ? 0 : 6; base = (const unsigned char*)(q == 0 ? p.wg : p.wu); col = 112 * c + 16 * ft + 8 * half; strideB = F * 2; }
            else { base = (const unsigned char*)&g_fwd + c * 32768; col = 16 * (q - 2) + 8 * half; strideB = 64; }
            const unsigned char* b00 = base + (size_t)(2 * b0 + 8 * b2 + (lane >> 5)) * strideB + col * 2;
#pragma unroll
            for (int u = 0; u < 8; ++u) ls.b[u] = b00 + (size_t)(4 * (u & 1) + 16 * ((u >> 1) & 1) + 32 * (u >> 2)) * strideB;
            ls.sB64 = (long)strideB * 64;
        }
        p1_issue(ls, (const unsigned char*)p.x, Lb, lw, 0);
        p1_touch(ls, Lb); p1_touch(ls, Lb); p1_touch(ls, Lb);
        p1_issue(ls, (const unsigned char*)p.x, Lb, lw, 1);
        STAMP(14);
        if (tid == 256) { *(volatile LAS unsigned*)(L + L_FLG + 4) = 0u; *(volatile LAS unsigned*)(L + L_FLG + 8) = 0u; *(volatile LAS unsigned*)(L + L_FLG + 12) = 0u; }
        wait_vm<15>(); pin(epoch); tag = epoch + 1u;
        if (r == 0 && tid == 256) __hip_atomic_store((gu32*)p.ws + W_CT + c * 32, tag << 8, RLX_AGENT);
        WAIT_LGKM0();
        STAMP(15);
        BAR();
        p1_touch(ls, Lb);
        p1_issue(ls, (const unsigned char*)p.x, Lb, lw, 2);
        SB();
        const unsigned char *st0, *st1;
        {
            auto wline = [&](int i) { const int st = i >> 2, ln = (bi >> 2) * 16 + lw * 4 + (i & 3);
                return st < 14 ? (const unsigned char*)p.wd + (size_t)(32 * p3_ks(st, ln >> 5) + (ln & 31)) * 1024 + cc * 128
                               : (const unsigned char*)&g_inv + (size_t)cc * 65536 + (st - 14) * 16384 + ln * 128; };
            st0 = wline(lane);
            const int fi = (bi * 4 + lw) * 20 + (lane >= 8 && lane < 28 ? lane - 8 : 0);
            const int arr = fi / 1280, rr = (fi % 1280) / 5, ll = (fi % 1280) % 5;
            const int jrow = 4 * jq + (rr & 3) + 32 * (rr >> 2);
            st1 = lane < 8 ? wline(64 + lane) : (const unsigned char*)(arr ? p.wi : p.wr) + (size_t)jrow * (DF * 2) + min(ll * 128, 510);
        }
        u32x2 gran; const unsigned goff = (unsigned)WS_STAT + (unsigned)(r * 128 + (tid & 127)) * 8u;
        static_for<0, 7>([&](auto J) {
            constexpr int j = J.v;
            if constexpr (j < 4) wait_vm<13>(); else if constexpr (j == 4) wait_vm<12>();
            else if constexpr (j == 5) wait_vm<15>(); else wait_vm<3>();
            BAR();
            if constexpr (j < 3) p1_touch(ls, Lb);
            if constexpr (j + 3 < 8) p1_issue(ls, (const unsigned char*)p.x, Lb, lw, j + 3);
            if constexpr (j == 4) {
                gl64_sc1(gran, ws, goff);
                glds4(st0, Lb + L_SCR); glds4(st1, Lb + L_SCR);
            }
        });
        wait_vm<2>();
        pin(gran);
        if (lw < 2) {
            if (!__all(gran.y == tag)) {
#pragma unroll 1
                for (unsigned spins = 0;; ++spins) {
                    gl64_sc1(gran, ws, goff); wait_vm<0>(); pin(gran);
                    if (__all(gran.y == tag)) break;
                    if (spins > SPIN_CAP) { give_up(ws, lane); break; }
                    __builtin_amdgcn_s_sleep(1);
                }
            }
            ST[tid & 127] = f32x2{bf_hi(gran.x), 1.0f / bf_lo(gran.x)};
        }
#pragma unroll
        for (int q = 0; q < 4; ++q)
#pragma unroll
            for (int mt = 0; mt < 8; ++mt) acc[q][mt] = f32x4{0.f, 0.f, 0.f, 0.f};
    } else {
        unsigned epoch; gl32_sc1(epoch, ws, 0u);
        u32x4 xrow[2];
#pragma unroll
        for (int i = 0; i < 2; ++i) gl128(xrow[i], (const unsigned char*)p.x + (size_t)panel_row(r, 8 * c + 2 * wid + i) * 1024 + lane * 16);
        wait_vm<0>(); pin(epoch); pin(xrow[0]); pin(xrow[1]);
        tag = epoch + 1u;
#pragma unroll
        for (int i = 0; i < 2; ++i) {
            float v[8];
#pragma unroll
            for (int e = 0; e < 4; ++e) { v[2 * e] = bf_lo(xrow[i][e]); v[2 * e + 1] = bf_hi(xrow[i][e]); }
            float s = ((v[0] + v[1]) + (v[2] + v[3])) + ((v[4] + v[5]) + (v[6] + v[7]));
            s = wave_sum(s);
            const float mean = s * (1.0f / 512.0f);
            float q2 = 0.f;
#pragma unroll
            for (int e = 0; e < 8; ++e) { const float d = v[e] - mean; q2 += d * d; }
            q2 = wave_sum(q2);
            const float var = q2 * (1.0f / 512.0f);
            const float m_bf = bfr(mean), v_bf = bfr(var), s_bf = bfr(sqrtf(bfr(v_bf + 1e-5f)));
            const unsigned val = (__builtin_bit_cast(unsigned, m_bf) & 0xffff0000u) | (__builtin_bit_cast(unsigned, s_bf) >> 16);
            if (lane == 0) __hip_atomic_store((gu64*)(p.ws + WS_STAT) + (r * 128 + 8 * c + 2 * wid + i), ((unsigned long long)tag << 32) | val, RLX_AGENT);
        }
#pragma unroll
        for (int q = 0; q < 4; ++q)
#pragma unroll
            for (int mt = 0; mt < 8; ++mt) acc[q][mt] = f32x4{0.f, 0.f, 0.f, 0.f};
        STAMP(1);
        if (wid < 3) p1_compute<false>(L, Lb, wc, lane, acc, csm, tsv); else p1_compute<true>(L, Lb, wc, lane, acc, csm, tsv);
        STAMP(2);
    }
    }
    int tz = threadIdx.x; asm volatile("" : "+v"(tz));
    const int tid = tz, lane = tz & 63, fr = lane & 15, g = lane >> 4;
    const __amdgpu_buffer_rsrc_t rs = __builtin_amdgcn_make_buffer_rsrc((void*)p.ws, 0, (int)WS_END, 0x00020000);
    const unsigned wv = (unsigned)(lane >> 3) * 1024u + cc * 128u + (unsigned)(((lane & 7) ^ (2 * ((lane >> 4) & 3))) << 4), fv = lane * 16u;
    volatile LAS unsigned* flg = (volatile LAS unsigned*)(L + L_FLG);
    WAIT_LGKM0(); BAR();
    STAMP(3);
    const int fj1 = 4 * jq + (fr & 3), fk = 4 * k4 + (fr >> 2);
    unsigned fw[4], fwi[4];
    auto signal = [&](int cnt, int word) {
        if (lane == 0) { const unsigned old = __hip_atomic_fetch_add((LAS unsigned*)(L + L_FLG) + cnt, 1u, __ATOMIC_RELAXED, __HIP_MEMORY_SCOPE_WORKGROUP);
            if (old == 3u) __hip_atomic_store((gu32*)p.ws + word, tag, RLX_AGENT); }
    };
    if (wid < 3) {
        const f32x4 cg0 = csm[0], cg1 = csm[1], cu0 = csm[2], cu1 = csm[3];
#pragma unroll
        for (int mt = 0; mt < 8; ++mt) {
            const f32x2 st = ST[16 * mt + fr];
            const float r2 = st.y * st.y;
            const f32x4 h0 = (acc[0][mt] - st.x * cg0) * (acc[2][mt] - st.x * cu0) * r2;
            const f32x4 h1 = (acc[1][mt] - st.x * cg1) * (acc[3][mt] - st.x * cu1) * r2;
            st_sc1(rs, (int)WS_H + ((((r * 8 + mt) * 48 + 3 * c + wc) * 64 + lane) << 4), pack8(h0, h1));
        }
    } else if (wid == 3) {
        const float csf = (c == 0 && fr == 0) ? 512.0f : 0.0f;
#pragma unroll
        for (int mt = 0; mt < 8; ++mt) {
            const int m0 = 16 * mt + 4 * g;
            const f32x4 s01 = *(LAS f32x4*)(ST + m0), s23 = *(LAS f32x4*)(ST + m0 + 2);
            const int s2l = mt >> 1, s1 = 16 * (mt & 1) + 4 * g;
            *(LAS u32x2*)(L + L_XI + fr * 272 + s2l * 64 + s1 * 2) =
                u32x2{pk_bf16((acc[2][mt][0] - s01[0] * csf) * s01[1], (acc[2][mt][1] - s01[2] * csf) * s01[3]),
                      pk_bf16((acc[2][mt][2] - s23[0] * csf) * s23[1], (acc[2][mt][3] - s23[2] * csf) * s23[3])};
            *(LAS u32x2*)(L + L_XI + (16 + fr) * 272 + s2l * 64 + s1 * 2) =
                u32x2{pk_bf16(acc[3][mt][0] * s01[1], acc[3][mt][1] * s01[3]), pk_bf16(acc[3][mt][2] * s23[1], acc[3][mt][3] * s23[3])};
        }
        WAIT_LGKM0();
        flg[0] = tag;
        const f32x4 cg0 = csm[0], cu0 = csm[1];
#pragma unroll
        for (int mt = 0; mt < 8; ++mt) {
            const f32x2 st = ST[16 * mt + fr];
            const float r2 = st.y * st.y;
            const f32x4 h0 = (acc[0][mt] - st.x * cg0) * (acc[1][mt] - st.x * cu0) * r2;
            st_sc1_64(rs, (int)WS_H2 + ((((r * 8 + mt) * 16 + c) * 64 + lane) << 3), u32x2{pk_bf16(h0[0], h0[1]), pk_bf16(h0[2], h0[3])});
        }
    } else {
        const int mt = wid - 4;
        u32x4 eaC[2], eaS[2];
        float tc[2][4], tsn[2][4];
#pragma unroll
        for (int jt = 0; jt < 2; ++jt) {
            float cv[8], sv[8];
#pragma unroll
            for (int j = 0; j < 8; ++j) { const float th = (float)(((16 * jt + fr) * (8 * g + j)) & 31) * (1.0f / 32.0f); cv[j] = cosr(th); sv[j] = sinr(th); }
            eaC[jt] = u32x4{pk_bf16(cv[0], cv[1]), pk_bf16(cv[2], cv[3]), pk_bf16(cv[4], cv[5]), pk_bf16(cv[6], cv[7])};
            eaS[jt] = u32x4{pk_bf16(sv[0], sv[1]), pk_bf16(sv[2], sv[3]), pk_bf16(sv[4], sv[5]), pk_bf16(sv[6], sv[7])};
#pragma unroll
            for (int e = 0; e < 4; ++e) { const float th = (float)((16 * jt + fr) * (4 * r + e)) * (1.0f / 2048.0f); tc[jt][e] = cosr(th); tsn[jt][e] = sinr(th); }
        }
        SB();
#pragma unroll 1
        for (unsigned spins = 0; flg[0] != tag && spins < (1u << 20); ++spins) __builtin_amdgcn_s_sleep(1);
        asm volatile("" ::: "memory");
        const int kq_ = fr >> 2, s2l = fr & 3;
        const u32x4 are = lds128(L, L_XI + (4 * mt + kq_) * 272 + s2l * 64 + g * 16);
        const u32x4 aim = lds128(L, L_XI + (16 + 4 * mt + kq_) * 272 + s2l * 64 + g * 16);
        const f32x4 z = {0.f, 0.f, 0.f, 0.f};
        const bool special = (c == 0 && mt == 0 && g == 0);
#pragma unroll
        for (int jt = 0; jt < 2; ++jt) {
            const f32x4 Prc = mfma(are, eaC[jt], z), Pis = mfma(aim, eaS[jt], z), Prs = mfma(are, eaS[jt], z), Pic = mfma(aim, eaC[jt], z);
            const int j1 = 16 * jt + fr;
            float pr[4], qi[4], pr2[4], qi2[4];
#pragma unroll
            for (int e = 0; e < 4; ++e) {
                const float cw = tc[jt][e], sw = tsn[jt][e];
                const float ar = special ? Prc[e] : Prc[e] + Pis[e], ai = special ? -Prs[e] : Pic[e] - Prs[e];
                pr[e] = ar * cw + ai * sw; qi[e] = -(ai * cw - ar * sw);
                const float br = Pic[e], bi2 = -Pis[e];
                pr2[e] = br * cw + bi2 * sw; qi2[e] = -(bi2 * cw - br * sw);
            }
            const int U = (j1 >> 2) * 64 + 4 * c + mt, lp = 16 * (r & 3) + 4 * g + (j1 & 3);
            st_sc1(rs, (int)WS_AP + (((U * 4 + (r >> 2)) * 64 + lp) << 4), pack8(pr, qi));
            if (special) st_sc1(rs, (int)WS_AX + (((((j1 >> 2) * 4 + (r >> 2)) * 4 + (r & 3)) * 4 + (j1 & 3)) << 4), pack8(pr2, qi2));
        }
#pragma unroll
        for (int st = 0; st < 3; ++st) p3_issue(p, Lb, mt, cc, st, wv, fv);
        wait_vm<12>();
        signal(1, W_FA + (c * 16 + r) * FSTR);
    }
#pragma unroll
    for (int e = 0; e < 4; ++e) { const unsigned o = (unsigned)((fj1 + 32 * (16 * w2 + 4 * g + e)) * DF + fk) * 2u; gl16(fw[e], p.wr, o); gl16(fwi[e], p.wi, o); }
    unsigned fx[8];
    {
        const unsigned o0 = (unsigned)((fj1 + 32 * (16 * w2 + 4 * g)) * DF + 256) * 2u;
        fx_load(fx, o0, o0 + 32u * DF * 2u, o0 + 64u * DF * 2u, o0 + 96u * DF * 2u, p.wr, p.wi, k4);
    }
    if (wid < 4) { if (k4 == 0) wait_vm<16>(); else wait_vm<8>(); signal(2, W_FH + (r * 16 + c) * FSTR); }
    STAMP(5);
    {
        const int kg = wid & 3, rh = wid >> 2;
        u32x4 E1[4], E2[4];
#pragma unroll
        for (int t = 0; t < 4; ++t) E1[t] = frag_cs(16 * w2 + fr, 16 * t + 4 * g, 64);
#pragma unroll
        for (int i = 0; i < 4; ++i) E2[i] = frag_cs(16 * (i >> 1) + fr, 16 * (i & 1) + 4 * g, 32);
        float tc[4], tsn[4];
#pragma unroll
        for (int e = 0; e < 4; ++e) { const float th = (float)((4 * jq + e) * (16 * w2 + fr)) * (1.0f / 2048.0f); tc[e] = cosr(th); tsn[e] = sinr(th); }
        P3Ctx cx;
        {
            const int qq = fr >> 2, pp = lane & 3, row = 32 * kg + 4 * g + qq, sw = 2 * ((row >> 1) & 3);
#pragma unroll
            for (int dt = 0; dt < 4; ++dt) cx.woff[dt] = row * 128 + (((2 * dt + (pp >> 1)) ^ sw) << 4) + 8 * (pp & 1);
            cx.hbase = (unsigned)WS_H + (unsigned)(((r3 * 8 + 4 * h3 + 2 * rh) * 48 + kg) * 64 + lane) * 16u;
            cx.h2base = (unsigned)WS_H2 + (unsigned)(((r3 * 8 + 4 * h3 + 2 * rh) * 16 + 2 * kg) * 64 + lane) * 8u;
            cx.tbase = (unsigned)WS_T + (unsigned)((((4 * r3 + 2 * h3 + rh) * 16 + 4 * kg) * 2) * 64 + lane) * 16u;
        }
        SB();
        STAMP(6);
        if (wid == 0) poll16(ws, W_FA + (kq >> 1) * 16 * FSTR, tag, lane);
        if (wid == 1) poll16(ws, W_FH + r3 * 16 * FSTR, tag, lane);
        BAR();
        STAMP(7);
        const int U = jq * 64 + k4;
        u32x4 Bf[4], Bx[4];
        u32x4 hb[HD][2]; u32x2 hh[2][4];
#pragma unroll
        for (int t = 0; t < 4; ++t) gl128_sc1(Bf[t], ws, (unsigned)WS_AP + (unsigned)((U * 4 + t) * 64 + lane) * 16u);
        {
            const unsigned xo = (unsigned)WS_AX + (unsigned)((jq * 16 + g) * 4 + (fr & 3)) * 16u;
            asm volatile("s_cmp_lg_u32 %6, 0\n\ts_cbranch_scc1 .Lnox%=\n\t"
                         "global_load_dwordx4 %0, %4, %5 sc1\n\tglobal_load_dwordx4 %1, %4, %5 offset:256 sc1\n\t"
                         "global_load_dwordx4 %2, %4, %5 offset:512 sc1\n\tglobal_load_dwordx4 %3, %4, %5 offset:768 sc1\n"
                         ".Lnox%=:" : "=&v"(Bx[0]), "=&v"(Bx[1]), "=&v"(Bx[2]), "=&v"(Bx[3]) : "v"(xo), "s"(ws), "s"(k4) : "memory", "scc");
        }
        static_for<0, HD>([&](auto Sx) { constexpr int o = Sx.v;
            gl128_sc1(hb[o][0], ws, cx.hbase + o * 4096u); gl128_sc1(hb[o][1], ws, cx.hbase + 48u * 1024u + o * 4096u); });
#pragma unroll
        for (int st = 3; st < 9; ++st) p3_issue2(p, Lb, wid, cc, st, wv, fv);
        wait_vm<2 * HD + 12>();
        pin(Bf[0]); pin(Bf[1]); pin(Bf[2]); pin(Bf[3]); pin(Bx[0]); pin(Bx[1]); pin(Bx[2]); pin(Bx[3]);
        pin(fw[0]); pin(fw[1]); pin(fw[2]); pin(fw[3]); pin(fwi[0]); pin(fwi[1]); pin(fwi[2]); pin(fwi[3]);
        pin(fx[0]); pin(fx[1]); pin(fx[2]); pin(fx[3]); pin(fx[4]); pin(fx[5]); pin(fx[6]); pin(fx[7]);
        STAMP(8);
        {
            f32x4 a1 = {0.f, 0.f, 0.f, 0.f}, a2 = a1;
#pragma unroll
            for (int t = 0; t < 4; ++t) { a1 = mfma(E1[t], Bf[t], a1); a2 = mfma(E1[t], swap_neg(Bf[t]), a2); }
            constexpr float SC = 2.0f / (2048.0f * 512.0f);
            const float sc = (k4 == 0 && (fr >> 2) == 0) ? 0.5f * SC : SC;
            float yr[4], yi[4];
#pragma unroll
            for (int e = 0; e < 4; ++e) { const float xr = a1[e], xi = -a2[e], wre = bf_lo(fw[e]), wim = bf_lo(fwi[e]);
                yr[e] = (xr * wre - xi * wim) * sc; yi[e] = (xr * wim + xi * wre) * sc; }
            if (k4 == 0) {
                f32x4 n1 = {0.f, 0.f, 0.f, 0.f}, n2 = n1;
#pragma unroll
                for (int t = 0; t < 4; ++t) { n1 = mfma(E1[t], Bx[t], n1); n2 = mfma(E1[t], swap_neg(Bx[t]), n2); }
                float xr2[4], xi2[4];
#pragma unroll
                for (int e = 0; e < 4; ++e) { const float xr = n1[e], xi = -n2[e], wre = bf_lo(fx[e]), wim = bf_lo(fx[4 + e]);
                    xr2[e] = (xr * wre - xi * wim) * (0.5f * SC); xi2[e] = (xr * wim + xi * wre) * (0.5f * SC); }
                *(LAS u32x4*)(L + L_YX + (w2 * 64 + lane) * 16) = pack8(xr2, xi2);
            }
            *(LAS u32x4*)(L + L_XI + ((u2 * 4 + w2) * 64 + lane) * 16) = pack8(yr, yi);
        }
        WAIT_LGKM0(); BAR();
        u32x4 wf[2][4];
        p3_rdw(L, cx, kg, lane, 0, wf[0]);
        {
            f32x4 t1 = {0.f, 0.f, 0.f, 0.f}, t2 = t1;
#pragma unroll
            for (int t = 0; t < 4; ++t) { const u32x4 y = lds128(L, L_XI + ((u2 * 4 + t) * 64 + lane) * 16);
                t1 = mfma(y, E1[t], t1); t2 = mfma(swap_neg(y), E1[t], t2); }
            const int s2 = 16 * w2 + fr, ko = 4 * k4 + g;
            float tr[4], ti[4];
#pragma unroll
            for (int e = 0; e < 4; ++e) { tr[e] = t1[e] * tc[e] - t2[e] * tsn[e]; ti[e] = t2[e] * tc[e] + t1[e] * tsn[e]; }
            st_sc1(rs, (int)WS_T + ((((s2 * 16 + (ko >> 4)) * 2 + (jq >> 2)) * 64 + (jq & 3) * 16 + (ko & 15)) << 4), pack8(tr, ti));
            if (k4 == 0) {
                f32x4 x1 = {0.f, 0.f, 0.f, 0.f}, x2 = x1;
#pragma unroll
                for (int t = 0; t < 4; ++t) { const u32x4 y = lds128(L, L_YX + (t * 64 + lane) * 16);
                    x1 = mfma(y, E1[t], x1); x2 = mfma(swap_neg(y), E1[t], x2); }
#pragma unroll
                for (int e = 0; e < 4; ++e) { tr[e] = x1[e] * tc[e] - x2[e] * tsn[e]; ti[e] = x2[e] * tc[e] + x1[e] * tsn[e]; }
                if (g == 0) st_sc1(rs, (int)WS_TX + ((s2 * 8 + jq) << 4), pack8(tr, ti));
            }
        }
        u32x4 xres; gl128(xres, (const unsigned char*)p.x + ((size_t)panel_row(r3, 64 * h3 + (tid >> 3)) * 512 + 64 * cc + 8 * (tid & 7)) * 2);
        STAMP(9);
        f32x4 acc3[4][2];
#pragma unroll
        for (int dt = 0; dt < 4; ++dt) { acc3[dt][0] = f32x4{0.f, 0.f, 0.f, 0.f}; acc3[dt][1] = acc3[dt][0]; }
        unsigned ftf; u32x4 tx[2];
        static_for<0, 18>([&](auto Sx) {
            constexpr int s = Sx.v;
            wait_vm<p3_wait(s)>();
            if constexpr (s == 12 || s == 13) { pin(hh[s - 12][0]); pin(hh[s - 12][1]); pin(hh[s - 12][2]); pin(hh[s - 12][3]); }
            else { pin(hb[s % HD][0]); pin(hb[s % HD][1]); }
            if constexpr (s == 14) { pin(tx[0]); pin(tx[1]); }
            if constexpr (s == 2) STAMP(4);
            if constexpr (s == 2) {
                if (lane == 0) { const unsigned old = __hip_atomic_fetch_add((LAS unsigned*)(L + L_FLG) + 3, 1u, __ATOMIC_RELAXED, __HIP_MEMORY_SCOPE_WORKGROUP);
                    if (old == 7u) __hip_atomic_fetch_add((gu32*)p.ws + W_CT + (kq >> 1) * 32, 1u, RLX_AGENT); }
            }
            if constexpr (s == 14 - HD) {
                STAMP(12);
                pin(ftf);
                if (wid == 0) {
                    if (!__all(ftf == (tag << 8) + 16u)) {
#pragma unroll 1
                        for (unsigned spins = 0;; ++spins) {
                            __builtin_amdgcn_s_sleep(PSLEEP);
                            unsigned f2; gl32_sc1(f2, ws, (unsigned)(W_CT + (lane & 15) * 32) * 4u); wait_vm<0>(); pin(f2);
                            if (__all(f2 == (tag << 8) + 16u)) break;
                            if (spins > SPIN_CAP) { give_up(ws, lane); break; }
                        }
                    }
                    if (b == 0 && lane == 0) __hip_atomic_store((gu32*)p.ws, tag, RLX_AGENT);
                }
                STAMP(13);
            }
            if constexpr (s % 3 == 0 && s > 0) {
                WAIT_LGKM0(); BAR();
                p3_rdw(L, cx, kg, lane, s, wf[s & 1]);
            }
            TS(10 + s);
            if constexpr (s + 1 < 18 && (s + 1) % 3 != 0) p3_rdw(L, cx, kg, lane, s + 1, wf[(s + 1) & 1]);
            u32x4 b0, b1;
            if constexpr (s == 12 || s == 13) { b0 = u32x4{hh[s - 12][0].x, hh[s - 12][0].y, hh[s - 12][1].x, hh[s - 12][1].y}; b1 = u32x4{hh[s - 12][2].x, hh[s - 12][2].y, hh[s - 12][3].x, hh[s - 12][3].y}; }
            else { b0 = hb[s % HD][0]; b1 = hb[s % HD][1]; }
            if constexpr (s >= 14) {
                const u32x4 T0 = b0, T1 = b1;
                u32x4 T0s = swap_neg(T0), T1s = swap_neg(T1);
                if constexpr (s == 14) {
                    if (kg == 0 && fr == 0) { T0s = tx[0]; T1s = tx[1]; }
                }
                u32x4 zf[2];
#pragma unroll
                for (int st = 0; st < 2; ++st) {
                    f32x4 zr = {0.f, 0.f, 0.f, 0.f}, zi = zr;
                    zr = mfma(T0, E2[st * 2 + 0], zr); zr = mfma(T1, E2[st * 2 + 1], zr);
                    zi = mfma(T0s, E2[st * 2 + 0], zi); zi = mfma(T1s, E2[st * 2 + 1], zi);
                    zf[st] = pack8(zr, zi);
                }
                b0 = zf[0]; b1 = zf[1];
            }
#pragma unroll
            for (int dt = 0; dt < 4; ++dt) { acc3[dt][0] = mfma(wf[s & 1][dt], b0, acc3[dt][0]); acc3[dt][1] = mfma(wf[s & 1][dt], b1, acc3[dt][1]); }
            SB();
            if constexpr (s + HD < 18) {
                constexpr int o = s + HD;
                if constexpr (o < 12) { gl128_sc1(hb[o % HD][0], ws, cx.hbase + o * 4096u); gl128_sc1(hb[o % HD][1], ws, cx.hbase + 48u * 1024u + o * 4096u); }
                else if constexpr (o < 14) {
                    gl64_sc1(hh[o - 12][0], ws, cx.h2base + (o - 12) * 4096u); gl64_sc1(hh[o - 12][1], ws, cx.h2base + (o - 12) * 4096u + 512u);
                    gl64_sc1(hh[o - 12][2], ws, cx.h2base + 8192u + (o - 12) * 4096u); gl64_sc1(hh[o - 12][3], ws, cx.h2base + 8192u + (o - 12) * 4096u + 512u); }
                else { gl128_sc1(hb[o % HD][0], ws, cx.tbase + (o - 14) * 2048u); gl128_sc1(hb[o % HD][1], ws, cx.tbase + (o - 14) * 2048u + 1024u);
                    if constexpr (o == 14) { const unsigned txo = (unsigned)WS_TX + (unsigned)((4 * r3 + 2 * h3 + rh) * 8 + g) * 16u; gl128_sc1(tx[0], ws, txo); gl128_sc1(tx[1], ws, txo + 64u); } }
            }
            if constexpr (s >= 3 && s + 6 < 18) p3_issue2(p, Lb, wid, cc, s + 6, wv, fv);
            if constexpr (s == FTS) gl32_sc1(ftf, ws, wid == 0 ? (unsigned)(W_CT + (lane & 15) * 32) * 4u : cx.hbase - lane * 16u);
            SB();
        });
        STAMP(10);
        TS(28);
        wait_vm<0>(); pin(xres);
        WAIT_LGKM0(); BAR();
        constexpr int RP = 272;
#pragma unroll
        for (int dt = 0; dt < 4; ++dt)
#pragma unroll
            for (int st = 0; st < 2; ++st)
                *(LAS f32x4*)(L + (kg * 64 + 32 * rh + 16 * st + fr) * RP + (16 * dt + 4 * g) * 4) = acc3[dt][st];
        WAIT_LGKM0(); BAR();
        const int mm = tid >> 3, cg = tid & 7;
        f32x4 v0 = {0.f, 0.f, 0.f, 0.f}, v1 = v0;
#pragma unroll
        for (int q = 0; q < 4; ++q) { v0 += *(LAS f32x4*)(L + (q * 64 + mm) * RP + cg * 32); v1 += *(LAS f32x4*)(L + (q * 64 + mm) * RP + cg * 32 + 16); }
        const size_t o = (size_t)panel_row(r3, 64 * h3 + mm) * 512 + 64 * cc + 8 * cg;
        *(GAS u32x4*)(p.out + o) = u32x4{pk_bf16(bf_lo(xres.x) + v0[0], bf_hi(xres.x) + v0[1]), pk_bf16(bf_lo(xres.y) + v0[2], bf_hi(xres.y) + v0[3]),
                                         pk_bf16(bf_lo(xres.z) + v1[0], bf_hi(xres.z) + v1[1]), pk_bf16(bf_lo(xres.w) + v1[2], bf_hi(xres.w) + v1[3])};
    }
    STAMP(11);
}

extern "C" void kernel_launch(void* const* d_in, const int* in_sizes, int n_in, void* d_out, int out_size, void* d_ws, size_t ws_size,
                              hipStream_t stream) {
    Params p;
    p.x = (const uint16_t*)d_in[0]; p.wr = (const uint16_t*)d_in[1]; p.wi = (const uint16_t*)d_in[2];
    p.wg = (const uint16_t*)d_in[4]; p.wu = (const uint16_t*)d_in[5]; p.wd = (const uint16_t*)d_in[6];
    p.out = (uint16_t*)d_out; p.ws = (unsigned char*)d_ws;
    (void)hipFuncSetAttribute((const void*)k_fused, hipFuncAttributeMaxDynamicSharedMemorySize, LDS_BYTES);
    k_fused<<<256, NT, LDS_BYTES, stream>>>(p);
}
```

```cpp
#include <hip/hip_runtime.h>
#include <stdint.h>

#define LAS __attribute__((address_space(3)))
#define GAS __attribute__((address_space(1)))
#define DI __device__ __forceinline__
typedef __bf16 bf8 __attribute__((ext_vector_type(8)));
typedef __bf16 bf2 __attribute__((ext_vector_type(2)));
typedef short s16x4 __attribute__((ext_vector_type(4)));
typedef short s16x8 __attribute__((ext_vector_type(8)));
typedef float f32x4 __attribute__((ext_vector_type(4)));
typedef float f32x2 __attribute__((ext_vector_type(2)));
typedef unsigned u32x4 __attribute__((ext_vector_type(4)));
typedef unsigned u32x2 __attribute__((ext_vector_type(2)));
typedef GAS unsigned gu32;
typedef GAS unsigned long long gu64;
#define RLX_AGENT __ATOMIC_RELAXED, __HIP_MEMORY_SCOPE_AGENT

namespace tabs {
constexpr double PI = 3.14159265358979323846264338327950288;
constexpr double cos_q(double x) {
    double x2 = x * x, t = 1.0, s = 1.0;
    for (int i = 1; i <= 14; ++i) { t *= -x2 / ((2 * i - 1) * (2 * i)); s += t; }
    return s;
}
constexpr uint16_t f2bf(float f) {
    uint32_t u = __builtin_bit_cast(uint32_t, f);
    u += 0x7FFFu + ((u >> 16) & 1u);
    return (uint16_t)(u >> 16);
}
struct Base { uint16_t v[512];
    constexpr Base() : v{} {
        for (int m = 0; m <= 128; ++m) {
            double c = (m == 128) ? 0.0 : cos_q(2.0 * PI * m / 512.0);
            uint16_t b = f2bf((float)c);
            uint16_t nb = (m == 128) ? b : (uint16_t)(b ^ 0x8000u);
            v[m] = b; v[(512 - m) & 511] = b;
            v[256 - m] = nb; v[(256 + m) & 511] = nb;
        }
    }
};
constexpr Base base{};
constexpr uint16_t cosb(int m) { return base.v[m & 511]; }
constexpr uint16_t nsinb(int m) { return base.v[(m + 128) & 511]; }
constexpr uint16_t sinb(int m) { return base.v[(m + 384) & 511]; }
struct Chunk { uint16_t v[16384]; };
constexpr Chunk mk_fwd(int c) {
    Chunk r{};
    for (int d = 0; d < 512; ++d)
        for (int n = 0; n < 32; ++n) { const int k = 16 * c + (n & 15);
            r.v[d * 32 + n] = base.v[((n >> 4) ? (k == 0 ? d * 256 : d * k + 128) : d * k) & 511]; }
    return r;
}
constexpr Chunk mk_inv(int blk) {
    Chunk r{};
    const int cc = blk >> 1;
    for (int e = 0; e < 16384; e += 8) {
        const int l = (e >> 3) & 63, dt = (e >> 9) & 3, kg = (e >> 11) & 3, s = ((blk & 1) << 1) + (e >> 13);
        const int kt = 4 * kg + s, d = 64 * cc + 16 * dt + (l & 15), k0 = 16 * kt + 4 * (l >> 4);
        for (int j = 0; j < 4; ++j) { const int k = k0 + j;
            r.v[e + j] = base.v[(k * d) & 511];
            r.v[e + 4 + j] = base.v[(k == 0 ? d * 256 : k * d + 128) & 511]; }
    }
    return r;
}
template <int I> constexpr Chunk fwd_c = mk_fwd(I);
template <int I> constexpr Chunk inv_c = mk_inv(I);
struct Big { Chunk c[16]; };
}
#define C16(N) {{tabs::N<0>, tabs::N<1>, tabs::N<2>, tabs::N<3>, tabs::N<4>, tabs::N<5>, tabs::N<6>, tabs::N<7>, \
                 tabs::N<8>, tabs::N<9>, tabs::N<10>, tabs::N<11>, tabs::N<12>, tabs::N<13>, tabs::N<14>, tabs::N<15>}}
alignas(256) static __device__ const tabs::Big g_fwd = C16(fwd_c);
alignas(256) static __device__ const tabs::Big g_inv = C16(inv_c);

constexpr int S = 2048, D = 512, F = 1792, DF = 257, NT = 512;
constexpr size_t WS_CTRL = 0;
constexpr size_t WS_STAT = 98304;
constexpr size_t WS_AP = 131072;
constexpr size_t WS_AX = WS_AP + (2u << 20);
constexpr size_t WS_TX = WS_AX + 8192;
constexpr size_t WS_T = WS_AX + 65536;
constexpr size_t WS_H = WS_T + (2u << 20);
constexpr size_t WS_H2 = WS_H + (size_t)16 * 8 * 48 * 1024;
constexpr size_t WS_END = WS_H2 + (size_t)16 * 8 * 16 * 512;
#define FSTR 32
#define PSLEEP 8
constexpr int W_CT = 1024, W_TMO = 2048, W_FA = 4096, W_FH = W_FA + 256 * 32;
constexpr unsigned SPIN_CAP = 1u << 16;
constexpr int CODE_PF_BYTES = 39 * 1024;

struct Params {
    const uint16_t* x; const uint16_t* wr; const uint16_t* wi;
    const uint16_t* wg; const uint16_t* wu; const uint16_t* wd;
    uint16_t* out; unsigned char* ws;
};

template <int I> struct IC { static constexpr int v = I; };
template <int B, int E, class Fn> DI void static_for(Fn&& f) { if constexpr (B < E) { f(IC<B>{}); static_for<B + 1, E>(f); } }
DI f32x4 mfma(u32x4 a, u32x4 b, f32x4 c) {
    return __builtin_amdgcn_mfma_f32_16x16x32_bf16(__builtin_bit_cast(bf8, a), __builtin_bit_cast(bf8, b), c, 0, 0, 0);
}
DI unsigned pk_bf16(float lo, float hi) { bf2 p; p[0] = (__bf16)lo; p[1] = (__bf16)hi; return __builtin_bit_cast(unsigned, p); }
DI float bf_lo(unsigned u) { return __builtin_bit_cast(float, u << 16); }
DI float bf_hi(unsigned u) { return __builtin_bit_cast(float, u & 0xffff0000u); }
DI float bfr(float x) { return bf_lo(pk_bf16(x, x)); }
DI u32x4 lds128(LAS unsigned char* L, int off) { return *(LAS u32x4*)(L + off); }
DI u32x2 lds_tr(LAS unsigned char* L, int off) {
    return __builtin_bit_cast(u32x2, __builtin_amdgcn_ds_read_tr16_b64_v4i16((LAS s16x4*)(L + off)));
}
DI u32x4 lds_tr2(LAS unsigned char* L, int off0, int off1) {
    const u32x2 a = lds_tr(L, off0), b = lds_tr(L, off1);
    return u32x4{a.x, a.y, b.x, b.y};
}
DI u32x4 swap_neg(u32x4 v) { return u32x4{v.z, v.w, v.x ^ 0x80008000u, v.y ^ 0x80008000u}; }
DI u32x4 pack8(const float (&a)[4], const float (&b)[4]) { return u32x4{pk_bf16(a[0], a[1]), pk_bf16(a[2], a[3]), pk_bf16(b[0], b[1]), pk_bf16(b[2], b[3])}; }
DI u32x4 pack8(f32x4 a, f32x4 b) { return u32x4{pk_bf16(a[0], a[1]), pk_bf16(a[2], a[3]), pk_bf16(b[0], b[1]), pk_bf16(b[2], b[3])}; }
DI void glds16(const void* gsrc, unsigned lds_addr) {
    asm volatile("s_mov_b32 m0, %1\n\ts_nop 0\n\tglobal_load_lds_dwordx4 %0, off" :: "v"(gsrc), "s"(lds_addr) : "memory");
}
DI void glds16s(const void* sbase, unsigned voff, unsigned lds_addr) {
    asm volatile("s_mov_b32 m0, %2\n\ts_nop 0\n\tglobal_load_lds_dwordx4 %0, %1" :: "v"(voff), "s"(sbase), "s"(lds_addr) : "memory");
}
DI void glds4(const void* gsrc, unsigned lds_addr) {
    asm volatile("s_mov_b32 m0, %1\n\ts_nop 0\n\tglobal_load_lds_dword %0, off" :: "v"(gsrc), "s"(lds_addr) : "memory");
}
DI void gl128(u32x4& d, const void* p) { asm volatile("global_load_dwordx4 %0, %1, off" : "=v"(d) : "v"(p) : "memory"); }
DI void gl128_sc1(u32x4& d, const void* base, unsigned off) { asm volatile("global_load_dwordx4 %0, %1, %2 sc1" : "=v"(d) : "v"(off), "s"(base) : "memory"); }
DI void gl64_sc1(u32x2& d, const void* base, unsigned off) { asm volatile("global_load_dwordx2 %0, %1, %2 sc1" : "=v"(d) : "v"(off), "s"(base) : "memory"); }
DI void gl32_sc1(unsigned& d, const void* base, unsigned off) { asm volatile("global_load_dword %0, %1, %2 sc1" : "=v"(d) : "v"(off), "s"(base) : "memory"); }
DI void gl16(unsigned& d, const void* base, unsigned off) { asm volatile("global_load_ushort %0, %1, %2" : "=v"(d) : "v"(off), "s"(base) : "memory"); }
template <int N> DI void wait_vm() { static_assert(N >= 0 && N < 64, "vmcnt"); asm volatile("s_waitcnt vmcnt(%0)" :: "n"(N) : "memory"); }
template <class T> DI void pin(T& v) { asm volatile("" : "+v"(v)); }
#define WAIT_LGKM0() asm volatile("s_waitcnt lgkmcnt(0)" ::: "memory")
#define BAR() do { asm volatile("" ::: "memory"); __builtin_amdgcn_s_barrier(); asm volatile("" ::: "memory"); } while (0)
#define SB() __builtin_amdgcn_sched_barrier(0)
DI void st_sc1(__amdgpu_buffer_rsrc_t rs, int off, u32x4 v) { __builtin_amdgcn_raw_buffer_store_b128(v, rs, off, 0, 16); }
DI void st_sc1_64(__amdgpu_buffer_rsrc_t rs, int off, u32x2 v) { __builtin_amdgcn_raw_buffer_store_b64(v, rs, off, 0, 16); }
template <int CTRL> DI float dpp_add(float v) {
    return v + __builtin_bit_cast(float, __builtin_amdgcn_update_dpp(0, __builtin_bit_cast(int, v), CTRL, 0xf, 0xf, true));
}
DI float wave_sum(float v) {
    v = dpp_add<0xB1>(v);
    v = dpp_add<0x4E>(v);
    v = dpp_add<0x141>(v);
    v = dpp_add<0x140>(v);
    const int vi = __builtin_bit_cast(int, v);
    return (__builtin_bit_cast(float, __builtin_amdgcn_readlane(vi, 0)) + __builtin_bit_cast(float, __builtin_amdgcn_readlane(vi, 16))) +
           (__builtin_bit_cast(float, __builtin_amdgcn_readlane(vi, 32)) + __builtin_bit_cast(float, __builtin_amdgcn_readlane(vi, 48)));
}
DI void give_up(const unsigned char* ws, int lane) { if (lane == 0) __hip_atomic_store((gu32*)ws + W_TMO, 1u, RLX_AGENT); }
DI void poll16(const unsigned char* ws, int word, unsigned tag, int lane) {
#pragma unroll 1
    for (unsigned spins = 0;; ++spins) {
        unsigned f; gl32_sc1(f, ws, (unsigned)(word + (lane & 15) * FSTR) * 4u); wait_vm<0>(); pin(f);
        if (__all(f == tag)) break;
        if (spins > SPIN_CAP) { give_up(ws, lane); break; }
        __builtin_amdgcn_s_sleep(PSLEEP);
    }
}
DI void fx_load(unsigned (&w)[8], unsigned o0, unsigned o1, unsigned o2, unsigned o3, const void* wr, const void* wi, int k4) {
    asm volatile("s_cmp_lg_u32 %14, 0\n\ts_cbranch_scc1 .Lnofx%=\n\t"
                 "global_load_ushort %0, %8, %12\n\tglobal_load_ushort %1, %9, %12\n\tglobal_load_ushort %2, %10, %12\n\tglobal_load_ushort %3, %11, %12\n\t"
                 "global_load_ushort %4, %8, %13\n\tglobal_load_ushort %5, %9, %13\n\tglobal_load_ushort %6, %10, %13\n\tglobal_load_ushort %7, %11, %13\n"
                 ".Lnofx%=:" : "=&v"(w[0]), "=&v"(w[1]), "=&v"(w[2]), "=&v"(w[3]), "=&v"(w[4]), "=&v"(w[5]), "=&v"(w[6]), "=&v"(w[7])
                 : "v"(o0), "v"(o1), "v"(o2), "v"(o3), "s"(wr), "s"(wi), "s"(k4) : "memory", "scc");
}
DI float cosr(float x) { return __builtin_amdgcn_cosf(x); }
DI float sinr(float x) { return __builtin_amdgcn_sinf(x); }
DI u32x4 frag_cs(int a, int b0, int n) {
    float c[4], sn[4];
#pragma unroll
    for (int j = 0; j < 4; ++j) { const float th = (float)((a * (b0 + j)) & (n - 1)) * (1.0f / n); c[j] = cosr(th); sn[j] = -sinr(th); }
    return pack8(c, sn);
}

#define STAMP(i) do {} while (0)
#define TS(i) do {} while (0)

constexpr int P1_STAGE = 49152, P1_BOFF = 16384;
constexpr int L_MISC = 147456;
constexpr int L_XI = L_MISC;
constexpr int L_ST = L_MISC + 8704;
constexpr int L_YX = L_ST + 1024;
constexpr int L_SCR = L_YX + 4096;
constexpr int L_FLG = L_SCR + 256;
constexpr int L_END = L_FLG + 16;
static_assert(L_END <= 163840, "lds");
constexpr int LDS_BYTES = 163840;
constexpr int P3_SLOT = 16384;
DI constexpr int p3_slot(int s) { return (s % 9) * P3_SLOT; }

DI int panel_row(int r, int m) { return 64 * (m & 31) + 4 * r + (m >> 5); }
DI int p3_ks(int s, int kg) { const int n = 4 * s + kg; if (s >= 12) return 7 * (n - 48) + 3; const int p = n / 6, o = n - 6 * p; return 7 * p + o + (o >= 3); }

struct LdSrc { unsigned a[4]; const unsigned char* b[8]; long sB64; const unsigned char* t; long tadv; };
DI void p1_issue(LdSrc& s, const unsigned char* x, unsigned Lb, int lw, int j) {
    const unsigned sb = Lb + (j % 3) * P1_STAGE;
#pragma unroll
    for (int i = 0; i < 4; ++i) glds16s(x + j * 128, s.a[i], sb + (4 * lw + i) * 1024);
#pragma unroll
    for (int u = 0; u < 8; ++u) { const int i = (lw & 1) + 2 * (u & 1) + 4 * (lw >> 1) + 8 * ((u >> 1) & 1) + 16 * (u >> 2);
        glds16(s.b[u], sb + P1_BOFF + i * 1024); s.b[u] += s.sB64; }
}
DI void p1_touch(LdSrc& s, unsigned Lb) { glds4(s.t, Lb + L_SCR); s.t += s.tadv; }
DI void p3_issue(const Params& p, unsigned Lb, int lw, int cc, int s, unsigned wv, unsigned fv) {
    const unsigned sb = Lb + p3_slot(s) + lw * 4096;
    if (s < 14) { const unsigned char* base = (const unsigned char*)p.wd + (size_t)p3_ks(s, lw) * 32768;
#pragma unroll
        for (int i = 0; i < 4; ++i) glds16s(base + i * 8192, wv, sb + i * 1024); }
    else { const unsigned char* base = (const unsigned char*)&g_inv + cc * 65536 + (s - 14) * 16384 + lw * 4096;
#pragma unroll
        for (int i = 0; i < 4; ++i) glds16s(base + i * 1024, fv, sb + i * 1024); }
}

DI void p3_issue2(const Params& p, unsigned Lb, int w, int cc, int s, unsigned wv, unsigned fv) {
    const unsigned sb = Lb + p3_slot(s) + w * 2048;
    if (s < 14) { const unsigned char* base = (const unsigned char*)p.wd + (size_t)p3_ks(s, w >> 1) * 32768 + (w & 1) * 16384;
        glds16s(base, wv, sb); glds16s(base + 8192, wv, sb + 1024); }
    else { const unsigned char* base = (const unsigned char*)&g_inv + cc * 65536 + (s - 14) * 16384 + w * 2048;
        glds16s(base, fv, sb); glds16s(base + 1024, fv, sb + 1024); }
}

template <bool DFTW>
DI void p1_compute(LAS unsigned char* L, unsigned Lb, int wc, int lane, f32x4 (&acc)[4][8], f32x4 (&csm)[4], unsigned (&tsv)[32]) {
    asm volatile("" : "+v"(lane));
    const int fr = lane & 15, g = lane >> 4;
    const int qq = fr >> 2, p = lane & 3;
    int aoff0 = fr * 128 + (((0 + g) ^ (fr & 7)) << 4);
    int aoff1 = fr * 128 + (((4 + g) ^ (fr & 7)) << 4);
    const int sw = 2 * qq + 8 * (g & 1);
    int boff[4];
#pragma unroll
    for (int q = 0; q < 4; ++q) boff[q] = P1_BOFF + (8 * g + qq) * 512 + (((8 * wc + 2 * q + (p >> 1)) ^ sw) << 4) + 8 * (p & 1);
    u32x4 xa[2][4], wb[2][4];
    auto rdx = [&](int ks, int mh, int i) { xa[mh][i] = lds128(L, (ks ? aoff1 : aoff0) + (4 * mh + i) * 2048); };
    auto rdw = [&](int ks, int q) { const int o = boff[q] + ks * 16384; wb[ks][q] = lds_tr2(L, o, o + 2048); };
    auto rebase = [&](int d) { aoff0 += d; aoff1 += d; boff[0] += d; boff[1] += d; boff[2] += d; boff[3] += d;
        asm volatile("" : "+v"(aoff0), "+v"(aoff1), "+v"(boff[0]), "+v"(boff[1]), "+v"(boff[2]), "+v"(boff[3])); };
    auto step = [&](int ks, int mh, bool more) {
#pragma unroll
        for (int i = 0; i < 16; ++i) {
            const int q = i >> 2, mt = i & 3;
            if (DFTW && q >= 2) acc[q][4 * mh + mt] = mfma(xa[mh][mt], wb[ks][q], acc[q][4 * mh + mt]);
            else acc[q][4 * mh + mt] = mfma(wb[ks][q], xa[mh][mt], acc[q][4 * mh + mt]);
            if (more) {
                if (mh == 0) { if (i >= 1 && i <= 4) rdx(ks, 1, i - 1); }
                else { if (i < 4) rdx(ks ^ 1, 0, i); else if (i < 8) rdw(ks ^ 1, i - 4); }
            }
            if (mh == 0 && (i & 3) == 3) { const int d = i >> 2; if (!DFTW || d < 2) csm[d] = mfma(wb[ks][d], u32x4{0x3F803F80u, 0x3F803F80u, 0x3F803F80u, 0x3F803F80u}, csm[d]); }
            SB();
        }
    };
    BAR();
    TS(0);
#pragma unroll
    for (int i = 0; i < 4; ++i) { rdx(0, 0, i); rdw(0, i); }
    SB();
    static_for<0, 8>([&](auto J) {
        constexpr int j = J.v;
        step(0, 0, true);
        step(0, 1, true);
        step(1, 0, true);
        if constexpr (j < 7) { WAIT_LGKM0(); BAR(); TS(j + 1); rebase((j + 1) % 3 == 0 ? -2 * P1_STAGE : P1_STAGE);
            SB(); }
        step(1, 1, j < 7);
    });
    TS(8);
}

constexpr int HD = 5;
constexpr int FTS = 6;
enum { K_OP, K_TST, K_W, K_FT };
constexpr int p3_after(int kind, int idx, int s_now) {
    int n = -1;
    auto hit = [&](bool is, int cnt) { if (is) n = 0; else if (n >= 0) n += cnt; };
    for (int o = 0; o < HD; ++o) hit(kind == K_OP && idx == o, 2);
    for (int w = 3; w < 9; ++w) hit(kind == K_W && idx == w, 2);
    hit(kind == K_TST, 1); hit(false, 1);
    for (int s = 0; s < s_now; ++s) {
        if (s + HD < 18) hit(kind == K_OP && idx == s + HD, (s + HD >= 12 && s + HD <= 14) ? 4 : 2);
        if (s >= 3 && s + 6 < 18) hit(kind == K_W && idx == s + 6, 2);
        if (s == FTS) hit(kind == K_FT, 1);
    }
    return n;
}
constexpr int cmin(int a, int b) { return a < b ? a : b; }
constexpr int p3_wait(int s) {
    int n = p3_after(K_OP, s, s);
    if (s == 2) n = cmin(n, p3_after(K_TST, 0, s));
    if (s % 3 == 0 && s >= 3) n = cmin(n, p3_after(K_W, s + 2, s));
    if (s == 14 - HD) n = cmin(n, p3_after(K_FT, 0, s));
    return n;
}
static_assert(p3_wait(0) == 2 * (HD - 1) + 12 + 2 && p3_wait(2) == 1 + 4, "queue model");

struct P3Ctx { unsigned hbase, h2base, tbase; int woff[4]; };
DI void p3_rdw(LAS unsigned char* L, const P3Ctx& c, int kg, int lane, int s, u32x4 (&wf)[4]) {
#pragma unroll
    for (int dt = 0; dt < 4; ++dt) {
        if (s < 14) wf[dt] = lds_tr2(L, p3_slot(s) + c.woff[dt], p3_slot(s) + c.woff[dt] + 2048);
        else wf[dt] = lds128(L, p3_slot(s) + (kg * 4 + dt) * 1024 + lane * 16);
    }
}

__global__ void __launch_bounds__(NT, 2) k_fused(Params p) {
    extern __shared__ __attribute__((aligned(16))) unsigned char smem[];
    LAS unsigned char* L = (LAS unsigned char*)smem;
    const unsigned Lb = (unsigned)(size_t)L;
    const int wid = __builtin_amdgcn_readfirstlane(threadIdx.x >> 6);
    const int wc = wid & 3;
    const int b = blockIdx.x, xcd = b & 7, bi = b >> 3;
    const int r = (xcd & 1) * 8 + (bi & 7), c = (xcd >> 1) * 4 + (bi >> 3);
    const int jq = b & 7, kq = b >> 3;
    const int cc = (xcd & 1) * 4 + (bi & 3), hp = (xcd >> 1) * 8 + (bi >> 2), r3 = hp >> 1, h3 = hp & 1;
    const unsigned char* ws = p.ws;
    unsigned tsv[32];
#pragma unroll
    for (int i = 0; i < 32; ++i) tsv[i] = 0;
    STAMP(0);
    TS(31);
    const int odd = c & 1;
    const int u2 = wid >> 2, w2 = wid & 3, k4 = 2 * kq + u2;
    LAS f32x2* ST = (LAS f32x2*)(L + L_ST);
    f32x4 acc[4][8], csm[4];
#pragma unroll
    for (int i = 0; i < 4; ++i) csm[i] = f32x4{0.f, 0.f, 0.f, 0.f};
    unsigned tag;
    {
    const int tid = threadIdx.x, lane = tid & 63;
    if (wid >= 4) {
        const int lw = wid - 4;
        unsigned epoch; gl32_sc1(epoch, ws, 0u);
        LdSrc ls;
        {
            const int idx = lw * 22 + (lane < 22 ? lane : 0);
            if (idx < 32) { ls.t = (const unsigned char*)p.x + (size_t)panel_row(r, 32 * (c & 3) + idx) * 1024; ls.tadv = 128; }
            else { const int i = idx - 32, row = 8 * (r & 7) + i / 7, it = i % 7;
                if (it == 6) { ls.t = (const unsigned char*)&g_fwd + c * 32768 + row * 64; ls.tadv = 64 * 64; }
                else { const int it3 = it % 3; ls.t = (const unsigned char*)(it < 3 ? p.wg : p.wu) + (size_t)row * (F * 2) + c * 224 + (it3 == 2 ? 220 : it3 * 128); ls.tadv = 64L * F * 2; } }
        }
        p1_touch(ls, Lb);
        SB();
#pragma unroll
        for (int i = 0; i < 4; ++i) { const int m = 8 * (4 * lw + i) + (lane >> 3); ls.a[i] = (unsigned)panel_row(r, m) * 1024u + (((lane & 7) ^ (lane >> 3)) << 4); }
        {
            const int b0 = lw & 1, b2 = lw >> 1;
            const int swz = 2 * (2 * b0 + (lane >> 5)) + 8 * b2;
            const int ch = (lane & 31) ^ swz, nt = ch >> 1, half = ch & 1, cw = nt >> 2, q = nt & 3;
            const unsigned char* base; int col, strideB;
            if (cw < 3) { const int ft = odd + 2 * cw + (q & 1); base = (const unsigned char*)(q < 2 ? p.wg : p.wu); col = 112 * c + 16 * ft + 8 * half; strideB = F * 2; }
            else if (q < 2) { const int ft = odd ? 0 : 6; base = (const unsigned char*)(q == 0 ? p.wg : p.wu); col = 112 * c + 16 * ft + 8 * half; strideB = F * 2; }
            else { base = (const unsigned char*)&g_fwd + c * 32768; col = 16 * (q - 2) + 8 * half; strideB = 64; }
            const unsigned char* b00 = base + (size_t)(2 * b0 + 8 * b2 + (lane >> 5)) * strideB + col * 2;
#pragma unroll
            for (int u = 0; u < 8; ++u) ls.b[u] = b00 + (size_t)(4 * (u & 1) + 16 * ((u >> 1) & 1) + 32 * (u >> 2)) * strideB;
            ls.sB64 = (long)strideB * 64;
        }
        p1_issue(ls, (const unsigned char*)p.x, Lb, lw, 0);
        p1_touch(ls, Lb); p1_touch(ls, Lb); p1_touch(ls, Lb);
        p1_issue(ls, (const unsigned char*)p.x, Lb, lw, 1);
        STAMP(14);
        if (tid == 256) { *(volatile LAS unsigned*)(L + L_FLG + 4) = 0u; *(volatile LAS unsigned*)(L + L_FLG + 8) = 0u; *(volatile LAS unsigned*)(L + L_FLG + 12) = 0u; }
        wait_vm<15>(); pin(epoch); tag = epoch + 1u;
        if (r == 0 && tid == 256) __hip_atomic_store((gu32*)p.ws + W_CT + c * 32, tag << 8, RLX_AGENT);
        WAIT_LGKM0();
        STAMP(15);
        BAR();
        p1_touch(ls, Lb);
        p1_issue(ls, (const unsigned char*)p.x, Lb, lw, 2);
        SB();
        const unsigned char *st0, *st1;
        {
            auto wline = [&](int i) { const int st = i >> 2, ln = (bi >> 2) * 16 + lw * 4 + (i & 3);
                return st < 14 ? (const unsigned char*)p.wd + (size_t)(32 * p3_ks(st, ln >> 5) + (ln & 31)) * 1024 + cc * 128
                               : (const unsigned char*)&g_inv + (size_t)cc * 65536 + (st - 14) * 16384 + ln * 128; };
            st0 = wline(lane);
            const int fi = (bi * 4 + lw) * 20 + (lane >= 8 && lane < 28 ? lane - 8 : 0);
            const int arr = fi / 1280, rr = (fi % 1280) / 5, ll = (fi % 1280) % 5;
            const int jrow = 4 * jq + (rr & 3) + 32 * (rr >> 2);
            st1 = lane < 8 ? wline(64 + lane) : (const unsigned char*)(arr ? p.wi : p.wr) + (size_t)jrow * (DF * 2) + min(ll * 128, 510);
        }
        u32x2 gran; const unsigned goff = (unsigned)WS_STAT + (unsigned)(r * 128 + (tid & 127)) * 8u;
        static_for<0, 7>([&](auto J) {
            constexpr int j = J.v;
            if constexpr (j < 4) wait_vm<13>(); else if constexpr (j == 4) wait_vm<12>();
            else if constexpr (j == 5) wait_vm<15>(); else wait_vm<3>();
            BAR();
            if constexpr (j < 3) p1_touch(ls, Lb);
            if constexpr (j + 3 < 8) p1_issue(ls, (const unsigned char*)p.x, Lb, lw, j + 3);
            if constexpr (j == 4) {
                gl64_sc1(gran, ws, goff);
                glds4(st0, Lb + L_SCR); glds4(st1, Lb + L_SCR);
            }
        });
        wait_vm<2>();
        pin(gran);
        if (lw < 2) {
            if (!__all(gran.y == tag)) {
#pragma unroll 1
                for (unsigned spins = 0;; ++spins) {
                    gl64_sc1(gran, ws, goff); wait_vm<0>(); pin(gran);
                    if (__all(gran.y == tag)) break;
                    if (spins > SPIN_CAP) { give_up(ws, lane); break; }
                    __builtin_amdgcn_s_sleep(1);
                }
            }
            ST[tid & 127] = f32x2{bf_hi(gran.x), 1.0f / bf_lo(gran.x)};
        }
#pragma unroll
        for (int q = 0; q < 4; ++q)
#pragma unroll
            for (int mt = 0; mt < 8; ++mt) acc[q][mt] = f32x4{0.f, 0.f, 0.f, 0.f};
    } else {
        if (wid == 0) {
            const unsigned char* pc = (const unsigned char*)__builtin_amdgcn_s_getpc();
            const int ln = bi * 10 + (lane < 10 ? lane : 0);
            glds4(pc - ((size_t)pc & 127) + (size_t)min(ln * 128, CODE_PF_BYTES), Lb + L_SCR);
        }
        unsigned epoch; gl32_sc1(epoch, ws, 0u);
        u32x4 xrow[2];
#pragma unroll
        for (int i = 0; i < 2; ++i) gl128(xrow[i], (const unsigned char*)p.x + (size_t)panel_row(r, 8 * c + 2 * wid + i) * 1024 + lane * 16);
        wait_vm<0>(); pin(epoch); pin(xrow[0]); pin(xrow[1]);
        tag = epoch + 1u;
#pragma unroll
        for (int i = 0; i < 2; ++i) {
            float v[8];
#pragma unroll
            for (int e = 0; e < 4; ++e) { v[2 * e] = bf_lo(xrow[i][e]); v[2 * e + 1] = bf_hi(xrow[i][e]); }
            float s = ((v[0] + v[1]) + (v[2] + v[3])) + ((v[4] + v[5]) + (v[6] + v[7]));
            s = wave_sum(s);
            const float mean = s * (1.0f / 512.0f);
            float q2 = 0.f;
#pragma unroll
            for (int e = 0; e < 8; ++e) { const float d = v[e] - mean; q2 += d * d; }
            q2 = wave_sum(q2);
            const float var = q2 * (1.0f / 512.0f);
            const float m_bf = bfr(mean), v_bf = bfr(var), s_bf = bfr(sqrtf(bfr(v_bf + 1e-5f)));
            const unsigned val = (__builtin_bit_cast(unsigned, m_bf) & 0xffff0000u) | (__builtin_bit_cast(unsigned, s_bf) >> 16);
            if (lane == 0) __hip_atomic_store((gu64*)(p.ws + WS_STAT) + (r * 128 + 8 * c + 2 * wid + i), ((unsigned long long)tag << 32) | val, RLX_AGENT);
        }
#pragma unroll
        for (int q = 0; q < 4; ++q)
#pragma unroll
            for (int mt = 0; mt < 8; ++mt) acc[q][mt] = f32x4{0.f, 0.f, 0.f, 0.f};
        STAMP(1);
        if (wid < 3) p1_compute<false>(L, Lb, wc, lane, acc, csm, tsv); else p1_compute<true>(L, Lb, wc, lane, acc, csm, tsv);
        STAMP(2);
    }
    }
    int tz = threadIdx.x; asm volatile("" : "+v"(tz));
    const int tid = tz, lane = tz & 63, fr = lane & 15, g = lane >> 4;
    const __amdgpu_buffer_rsrc_t rs = __builtin_amdgcn_make_buffer_rsrc((void*)p.ws, 0, (int)WS_END, 0x00020000);
    const unsigned wv = (unsigned)(lane >> 3) * 1024u + cc * 128u + (unsigned)(((lane & 7) ^ (2 * ((lane >> 4) & 3))) << 4), fv = lane * 16u;
    volatile LAS unsigned* flg = (volatile LAS unsigned*)(L + L_FLG);
    WAIT_LGKM0(); BAR();
    STAMP(3);
    const int fj1 = 4 * jq + (fr & 3), fk = 4 * k4 + (fr >> 2);
    unsigned fw[4], fwi[4];
    auto signal = [&](int cnt, int word) {
        if (lane == 0) { const unsigned old = __hip_atomic_fetch_add((LAS unsigned*)(L + L_FLG) + cnt, 1u, __ATOMIC_RELAXED, __HIP_MEMORY_SCOPE_WORKGROUP);
            if (old == 3u) __hip_atomic_store((gu32*)p.ws + word, tag, RLX_AGENT); }
    };
    if (wid < 3) {
        const f32x4 cg0 = csm[0], cg1 = csm[1], cu0 = csm[2], cu1 = csm[3];
#pragma unroll
        for (int mt = 0; mt < 8; ++mt) {
            const f32x2 st = ST[16 * mt + fr];
            const float r2 = st.y * st.y;
            const f32x4 h0 = (acc[0][mt] - st.x * cg0) * (acc[2][mt] - st.x * cu0) * r2;
            const f32x4 h1 = (acc[1][mt] - st.x * cg1) * (acc[3][mt] - st.x * cu1) * r2;
            st_sc1(rs, (int)WS_H + ((((r * 8 + mt) * 48 + 3 * c + wc) * 64 + lane) << 4), pack8(h0, h1));
        }
    } else if (wid == 3) {
        const float csf = (c == 0 && fr == 0) ? 512.0f : 0.0f;
#pragma unroll
        for (int mt = 0; mt < 8; ++mt) {
            const int m0 = 16 * mt + 4 * g;
            const f32x4 s01 = *(LAS f32x4*)(ST + m0), s23 = *(LAS f32x4*)(ST + m0 + 2);
            const int s2l = mt >> 1, s1 = 16 * (mt & 1) + 4 * g;
            *(LAS u32x2*)(L + L_XI + fr * 272 + s2l * 64 + s1 * 2) =
                u32x2{pk_bf16((acc[2][mt][0] - s01[0] * csf) * s01[1], (acc[2][mt][1] - s01[2] * csf) * s01[3]),
                      pk_bf16((acc[2][mt][2] - s23[0] * csf) * s23[1], (acc[2][mt][3] - s23[2] * csf) * s23[3])};
            *(LAS u32x2*)(L + L_XI + (16 + fr) * 272 + s2l * 64 + s1 * 2) =
                u32x2{pk_bf16(acc[3][mt][0] * s01[1], acc[3][mt][1] * s01[3]), pk_bf16(acc[3][mt][2] * s23[1], acc[3][mt][3] * s23[3])};
        }
        WAIT_LGKM0();
        flg[0] = tag;
        const f32x4 cg0 = csm[0], cu0 = csm[1];
#pragma unroll
        for (int mt = 0; mt < 8; ++mt) {
            const f32x2 st = ST[16 * mt + fr];
            const float r2 = st.y * st.y;
            const f32x4 h0 = (acc[0][mt] - st.x * cg0) * (acc[1][mt] - st.x * cu0) * r2;
            st_sc1_64(rs, (int)WS_H2 + ((((r * 8 + mt) * 16 + c) * 64 + lane) << 3), u32x2{pk_bf16(h0[0], h0[1]), pk_bf16(h0[2], h0[3])});
        }
    } else {
        const int mt = wid - 4;
        u32x4 eaC[2], eaS[2];
        float tc[2][4], tsn[2][4];
#pragma unroll
        for (int jt = 0; jt < 2; ++jt) {
            float cv[8], sv[8];
#pragma unroll
            for (int j = 0; j < 8; ++j) { const float th = (float)(((16 * jt + fr) * (8 * g + j)) & 31) * (1.0f / 32.0f); cv[j] = cosr(th); sv[j] = sinr(th); }
            eaC[jt] = u32x4{pk_bf16(cv[0], cv[1]), pk_bf16(cv[2], cv[3]), pk_bf16(cv[4], cv[5]), pk_bf16(cv[6], cv[7])};
            eaS[jt] = u32x4{pk_bf16(sv[0], sv[1]), pk_bf16(sv[2], sv[3]), pk_bf16(sv[4], sv[5]), pk_bf16(sv[6], sv[7])};
#pragma unroll
            for (int e = 0; e < 4; ++e) { const float th = (float)((16 * jt + fr) * (4 * r + e)) * (1.0f / 2048.0f); tc[jt][e] = cosr(th); tsn[jt][e] = sinr(th); }
        }
        SB();
#pragma unroll 1
        for (unsigned spins = 0; flg[0] != tag && spins < (1u << 20); ++spins) __builtin_amdgcn_s_sleep(1);
        asm volatile("" ::: "memory");
        const int kq_ = fr >> 2, s2l = fr & 3;
        const u32x4 are = lds128(L, L_XI + (4 * mt + kq_) * 272 + s2l * 64 + g * 16);
        const u32x4 aim = lds128(L, L_XI + (16 + 4 * mt + kq_) * 272 + s2l * 64 + g * 16);
        const f32x4 z = {0.f, 0.f, 0.f, 0.f};
        const bool special = (c == 0 && mt == 0 && g == 0);
#pragma unroll
        for (int jt = 0; jt < 2; ++jt) {
            const f32x4 Prc = mfma(are, eaC[jt], z), Pis = mfma(aim, eaS[jt], z), Prs = mfma(are, eaS[jt], z), Pic = mfma(aim, eaC[jt], z);
            const int j1 = 16 * jt + fr;
            float pr[4], qi[4], pr2[4], qi2[4];
#pragma unroll
            for (int e = 0; e < 4; ++e) {
                const float cw = tc[jt][e], sw = tsn[jt][e];
                const float ar = special ? Prc[e] : Prc[e] + Pis[e], ai = special ? -Prs[e] : Pic[e] - Prs[e];
                pr[e] = ar * cw + ai * sw; qi[e] = -(ai * cw - ar * sw);
                const float br = Pic[e], bi2 = -Pis[e];
                pr2[e] = br * cw + bi2 * sw; qi2[e] = -(bi2 * cw - br * sw);
            }
            const int U = (j1 >> 2) * 64 + 4 * c + mt, lp = 16 * (r & 3) + 4 * g + (j1 & 3);
            st_sc1(rs, (int)WS_AP + (((U * 4 + (r >> 2)) * 64 + lp) << 4), pack8(pr, qi));
            if (special) st_sc1(rs, (int)WS_AX + (((((j1 >> 2) * 4 + (r >> 2)) * 4 + (r & 3)) * 4 + (j1 & 3)) << 4), pack8(pr2, qi2));
        }
#pragma unroll
        for (int st = 0; st < 3; ++st) p3_issue(p, Lb, mt, cc, st, wv, fv);
        wait_vm<12>();
        signal(1, W_FA + (c * 16 + r) * FSTR);
    }
#pragma unroll
    for (int e = 0; e < 4; ++e) { const unsigned o = (unsigned)((fj1 + 32 * (16 * w2 + 4 * g + e)) * DF + fk) * 2u; gl16(fw[e], p.wr, o); gl16(fwi[e], p.wi, o); }
    unsigned fx[8];
    {
        const unsigned o0 = (unsigned)((fj1 + 32 * (16 * w2 + 4 * g)) * DF + 256) * 2u;
        fx_load(fx, o0, o0 + 32u * DF * 2u, o0 + 64u * DF * 2u, o0 + 96u * DF * 2u, p.wr, p.wi, k4);
    }
    if (wid < 4) { if (k4 == 0) wait_vm<16>(); else wait_vm<8>(); signal(2, W_FH + (r * 16 + c) * FSTR); }
    STAMP(5);
    {
        const int kg = wid & 3, rh = wid >> 2;
        u32x4 E1[4], E2[4];
#pragma unroll
        for (int t = 0; t < 4; ++t) E1[t] = frag_cs(16 * w2 + fr, 16 * t + 4 * g, 64);
#pragma unroll
        for (int i = 0; i < 4; ++i) E2[i] = frag_cs(16 * (i >> 1) + fr, 16 * (i & 1) + 4 * g, 32);
        float tc[4], tsn[4];
#pragma unroll
        for (int e = 0; e < 4; ++e) { const float th = (float)((4 * jq + e) * (16 * w2 + fr)) * (1.0f / 2048.0f); tc[e] = cosr(th); tsn[e] = sinr(th); }
        P3Ctx cx;
        {
            const int qq = fr >> 2, pp = lane & 3, row = 32 * kg + 4 * g + qq, sw = 2 * ((row >> 1) & 3);
#pragma unroll
            for (int dt = 0; dt < 4; ++dt) cx.woff[dt] = row * 128 + (((2 * dt + (pp >> 1)) ^ sw) << 4) + 8 * (pp & 1);
            cx.hbase = (unsigned)WS_H + (unsigned)(((r3 * 8 + 4 * h3 + 2 * rh) * 48 + kg) * 64 + lane) * 16u;
            cx.h2base = (unsigned)WS_H2 + (unsigned)(((r3 * 8 + 4 * h3 + 2 * rh) * 16 + 2 * kg) * 64 + lane) * 8u;
            cx.tbase = (unsigned)WS_T + (unsigned)((((4 * r3 + 2 * h3 + rh) * 16 + 4 * kg) * 2) * 64 + lane) * 16u;
        }
        SB();
        STAMP(6);
        if (wid == 0) poll16(ws, W_FA + (kq >> 1) * 16 * FSTR, tag, lane);
        if (wid == 1) poll16(ws, W_FH + r3 * 16 * FSTR, tag, lane);
        BAR();
        STAMP(7);
        const int U = jq * 64 + k4;
        u32x4 Bf[4], Bx[4];
        u32x4 hb[HD][2]; u32x2 hh[2][4];
#pragma unroll
        for (int t = 0; t < 4; ++t) gl128_sc1(Bf[t], ws, (unsigned)WS_AP + (unsigned)((U * 4 + t) * 64 + lane) * 16u);
        {
            const unsigned xo = (unsigned)WS_AX + (unsigned)((jq * 16 + g) * 4 + (fr & 3)) * 16u;
            asm volatile("s_cmp_lg_u32 %6, 0\n\ts_cbranch_scc1 .Lnox%=\n\t"
                         "global_load_dwordx4 %0, %4, %5 sc1\n\tglobal_load_dwordx4 %1, %4, %5 offset:256 sc1\n\t"
                         "global_load_dwordx4 %2, %4, %5 offset:512 sc1\n\tglobal_load_dwordx4 %3, %4, %5 offset:768 sc1\n"
                         ".Lnox%=:" : "=&v"(Bx[0]), "=&v"(Bx[1]), "=&v"(Bx[2]), "=&v"(Bx[3]) : "v"(xo), "s"(ws), "s"(k4) : "memory", "scc");
        }
        static_for<0, HD>([&](auto Sx) { constexpr int o = Sx.v;
            gl128_sc1(hb[o][0], ws, cx.hbase + o * 4096u); gl128_sc1(hb[o][1], ws, cx.hbase + 48u * 1024u + o * 4096u); });
#pragma unroll
        for (int st = 3; st < 9; ++st) p3_issue2(p, Lb, wid, cc, st, wv, fv);
        wait_vm<2 * HD + 12>();
        pin(Bf[0]); pin(Bf[1]); pin(Bf[2]); pin(Bf[3]); pin(Bx[0]); pin(Bx[1]); pin(Bx[2]); pin(Bx[3]);
        pin(fw[0]); pin(fw[1]); pin(fw[2]); pin(fw[3]); pin(fwi[0]); pin(fwi[1]); pin(fwi[2]); pin(fwi[3]);
        pin(fx[0]); pin(fx[1]); pin(fx[2]); pin(fx[3]); pin(fx[4]); pin(fx[5]); pin(fx[6]); pin(fx[7]);
        STAMP(8);
        {
            f32x4 a1 = {0.f, 0.f, 0.f, 0.f}, a2 = a1;
#pragma unroll
            for (int t = 0; t < 4; ++t) { a1 = mfma(E1[t], Bf[t], a1); a2 = mfma(E1[t], swap_neg(Bf[t]), a2); }
            constexpr float SC = 2.0f / (2048.0f * 512.0f);
            const float sc = (k4 == 0 && (fr >> 2) == 0) ? 0.5f * SC : SC;
            float yr[4], yi[4];
#pragma unroll
            for (int e = 0; e < 4; ++e) { const float xr = a1[e], xi = -a2[e], wre = bf_lo(fw[e]), wim = bf_lo(fwi[e]);
                yr[e] = (xr * wre - xi * wim) * sc; yi[e] = (xr * wim + xi * wre) * sc; }
            if (k4 == 0) {
                f32x4 n1 = {0.f, 0.f, 0.f, 0.f}, n2 = n1;
#pragma unroll
                for (int t = 0; t < 4; ++t) { n1 = mfma(E1[t], Bx[t], n1); n2 = mfma(E1[t], swap_neg(Bx[t]), n2); }
                float xr2[4], xi2[4];
#pragma unroll
                for (int e = 0; e < 4; ++e) { const float xr = n1[e], xi = -n2[e], wre = bf_lo(fx[e]), wim = bf_lo(fx[4 + e]);
                    xr2[e] = (xr * wre - xi * wim) * (0.5f * SC); xi2[e] = (xr * wim + xi * wre) * (0.5f * SC); }
                *(LAS u32x4*)(L + L_YX + (w2 * 64 + lane) * 16) = pack8(xr2, xi2);
            }
            *(LAS u32x4*)(L + L_XI + ((u2 * 4 + w2) * 64 + lane) * 16) = pack8(yr, yi);
        }
        WAIT_LGKM0(); BAR();
        u32x4 wf[2][4];
        p3_rdw(L, cx, kg, lane, 0, wf[0]);
        {
            f32x4 t1 = {0.f, 0.f, 0.f, 0.f}, t2 = t1;
#pragma unroll
            for (int t = 0; t < 4; ++t) { const u32x4 y = lds128(L, L_XI + ((u2 * 4 + t) * 64 + lane) * 16);
                t1 = mfma(y, E1[t], t1); t2 = mfma(swap_neg(y), E1[t], t2); }
            const int s2 = 16 * w2 + fr, ko = 4 * k4 + g;
            float tr[4], ti[4];
#pragma unroll
            for (int e = 0; e < 4; ++e) { tr[e] = t1[e] * tc[e] - t2[e] * tsn[e]; ti[e] = t2[e] * tc[e] + t1[e] * tsn[e]; }
            st_sc1(rs, (int)WS_T + ((((s2 * 16 + (ko >> 4)) * 2 + (jq >> 2)) * 64 + (jq & 3) * 16 + (ko & 15)) << 4), pack8(tr, ti));
            if (k4 == 0) {
                f32x4 x1 = {0.f, 0.f, 0.f, 0.f}, x2 = x1;
#pragma unroll
                for (int t = 0; t < 4; ++t) { const u32x4 y = lds128(L, L_YX + (t * 64 + lane) * 16);
                    x1 = mfma(y, E1[t], x1); x2 = mfma(swap_neg(y), E1[t], x2); }
#pragma unroll
                for (int e = 0; e < 4; ++e) { tr[e] = x1[e] * tc[e] - x2[e] * tsn[e]; ti[e] = x2[e] * tc[e] + x1[e] * tsn[e]; }
                if (g == 0) st_sc1(rs, (int)WS_TX + ((s2 * 8 + jq) << 4), pack8(tr, ti));
            }
        }
        u32x4 xres; gl128(xres, (const unsigned char*)p.x + ((size_t)panel_row(r3, 64 * h3 + (tid >> 3)) * 512 + 64 * cc + 8 * (tid & 7)) * 2);
        STAMP(9);
        f32x4 acc3[4][2];
#pragma unroll
        for (int dt = 0; dt < 4; ++dt) { acc3[dt][0] = f32x4{0.f, 0.f, 0.f, 0.f}; acc3[dt][1] = acc3[dt][0]; }
        unsigned ftf; u32x4 tx[2];
        static_for<0, 18>([&](auto Sx) {
            constexpr int s = Sx.v;
            wait_vm<p3_wait(s)>();
            if constexpr (s == 12 || s == 13) { pin(hh[s - 12][0]); pin(hh[s - 12][1]); pin(hh[s - 12][2]); pin(hh[s - 12][3]); }
            else { pin(hb[s % HD][0]); pin(hb[s % HD][1]); }
            if constexpr (s == 14) { pin(tx[0]); pin(tx[1]); }
            if constexpr (s == 2) STAMP(4);
            if constexpr (s == 2) {
                if (lane == 0) { const unsigned old = __hip_atomic_fetch_add((LAS unsigned*)(L + L_FLG) + 3, 1u, __ATOMIC_RELAXED, __HIP_MEMORY_SCOPE_WORKGROUP);
                    if (old == 7u) __hip_atomic_fetch_add((gu32*)p.ws + W_CT + (kq >> 1) * 32, 1u, RLX_AGENT); }
            }
            if constexpr (s == 14 - HD) {
                STAMP(12);
                pin(ftf);
                if (wid == 0) {
                    if (!__all(ftf == (tag << 8) + 16u)) {
#pragma unroll 1
                        for (unsigned spins = 0;; ++spins) {
                            __builtin_amdgcn_s_sleep(PSLEEP);
                            unsigned f2; gl32_sc1(f2, ws, (unsigned)(W_CT + (lane & 15) * 32) * 4u); wait_vm<0>(); pin(f2);
                            if (__all(f2 == (tag << 8) + 16u)) break;
                            if (spins > SPIN_CAP) { give_up(ws, lane); break; }
                        }
                    }
                    if (b == 0 && lane == 0) __hip_atomic_store((gu32*)p.ws, tag, RLX_AGENT);
                }
                STAMP(13);
            }
            if constexpr (s % 3 == 0 && s > 0) {
                WAIT_LGKM0(); BAR();
                p3_rdw(L, cx, kg, lane, s, wf[s & 1]);
            }
            TS(10 + s);
            if constexpr (s + 1 < 18 && (s + 1) % 3 != 0) p3_rdw(L, cx, kg, lane, s + 1, wf[(s + 1) & 1]);
            u32x4 b0, b1;
            if constexpr (s == 12 || s == 13) { b0 = u32x4{hh[s - 12][0].x, hh[s - 12][0].y, hh[s - 12][1].x, hh[s - 12][1].y}; b1 = u32x4{hh[s - 12][2].x, hh[s - 12][2].y, hh[s - 12][3].x, hh[s - 12][3].y}; }
            else { b0 = hb[s % HD][0]; b1 = hb[s % HD][1]; }
            if constexpr (s >= 14) {
                const u32x4 T0 = b0, T1 = b1;
                u32x4 T0s = swap_neg(T0), T1s = swap_neg(T1);
                if constexpr (s == 14) {
                    if (kg == 0 && fr == 0) { T0s = tx[0]; T1s = tx[1]; }
                }
                u32x4 zf[2];
#pragma unroll
                for (int st = 0; st < 2; ++st) {
                    f32x4 zr = {0.f, 0.f, 0.f, 0.f}, zi = zr;
                    zr = mfma(T0, E2[st * 2 + 0], zr); zr = mfma(T1, E2[st * 2 + 1], zr);
                    zi = mfma(T0s, E2[st * 2 + 0], zi); zi = mfma(T1s, E2[st * 2 + 1], zi);
                    zf[st] = pack8(zr, zi);
                }
                b0 = zf[0]; b1 = zf[1];
            }
#pragma unroll
            for (int dt = 0; dt < 4; ++dt) { acc3[dt][0] = mfma(wf[s & 1][dt], b0, acc3[dt][0]); acc3[dt][1] = mfma(wf[s & 1][dt], b1, acc3[dt][1]); }
            SB();
            if constexpr (s + HD < 18) {
                constexpr int o = s + HD;
                if constexpr (o < 12) { gl128_sc1(hb[o % HD][0], ws, cx.hbase + o * 4096u); gl128_sc1(hb[o % HD][1], ws, cx.hbase + 48u * 1024u + o * 4096u); }
                else if constexpr (o < 14) {
                    gl64_sc1(hh[o - 12][0], ws, cx.h2base + (o - 12) * 4096u); gl64_sc1(hh[o - 12][1], ws, cx.h2base + (o - 12) * 4096u + 512u);
                    gl64_sc1(hh[o - 12][2], ws, cx.h2base + 8192u + (o - 12) * 4096u); gl64_sc1(hh[o - 12][3], ws, cx.h2base + 8192u + (o - 12) * 4096u + 512u); }
                else { gl128_sc1(hb[o % HD][0], ws, cx.tbase + (o - 14) * 2048u); gl128_sc1(hb[o % HD][1], ws, cx.tbase + (o - 14) * 2048u + 1024u);
                    if constexpr (o == 14) { const unsigned txo = (unsigned)WS_TX + (unsigned)((4 * r3 + 2 * h3 + rh) * 8 + g) * 16u; gl128_sc1(tx[0], ws, txo); gl128_sc1(tx[1], ws, txo + 64u); } }
            }
            if constexpr (s >= 3 && s + 6 < 18) p3_issue2(p, Lb, wid, cc, s + 6, wv, fv);
            if constexpr (s == FTS) gl32_sc1(ftf, ws, wid == 0 ? (unsigned)(W_CT + (lane & 15) * 32) * 4u : cx.hbase - lane * 16u);
            SB();
        });
        STAMP(10);
        TS(28);
        wait_vm<0>(); pin(xres);
        WAIT_LGKM0(); BAR();
        constexpr int RP = 272;
#pragma unroll
        for (int dt = 0; dt < 4; ++dt)
#pragma unroll
            for (int st = 0; st < 2; ++st)
                *(LAS f32x4*)(L + (kg * 64 + 32 * rh + 16 * st + fr) * RP + (16 * dt + 4 * g) * 4) = acc3[dt][st];
        WAIT_LGKM0(); BAR();
        const int mm = tid >> 3, cg = tid & 7;
        f32x4 v0 = {0.f, 0.f, 0.f, 0.f}, v1 = v0;
#pragma unroll
        for (int q = 0; q < 4; ++q) { v0 += *(LAS f32x4*)(L + (q * 64 + mm) * RP + cg * 32); v1 += *(LAS f32x4*)(L + (q * 64 + mm) * RP + cg * 32 + 16); }
        const size_t o = (size_t)panel_row(r3, 64 * h3 + mm) * 512 + 64 * cc + 8 * cg;
        *(GAS u32x4*)(p.out + o) = u32x4{pk_bf16(bf_lo(xres.x) + v0[0], bf_hi(xres.x) + v0[1]), pk_bf16(bf_lo(xres.y) + v0[2], bf_hi(xres.y) + v0[3]),
                                         pk_bf16(bf_lo(xres.z) + v1[0], bf_hi(xres.z) + v1[1]), pk_bf16(bf_lo(xres.w) + v1[2], bf_hi(xres.w) + v1[3])};
    }
    STAMP(11);
}

extern "C" void kernel_launch(void* const* d_in, const int* in_sizes, int n_in, void* d_out, int out_size, void* d_ws, size_t ws_size,
                              hipStream_t stream) {
    Params p;
    p.x = (const uint16_t*)d_in[0]; p.wr = (const uint16_t*)d_in[1]; p.wi = (const uint16_t*)d_in[2];
    p.wg = (const uint16_t*)d_in[4]; p.wu = (const uint16_t*)d_in[5]; p.wd = (const uint16_t*)d_in[6];
    p.out = (uint16_t*)d_out; p.ws = (unsigned char*)d_ws;
    (void)hipFuncSetAttribute((const void*)k_fused, hipFuncAttributeMaxDynamicSharedMemorySize, LDS_BYTES);
    k_fused<<<256, NT, LDS_BYTES, stream>>>(p);
}
```

```cpp
#define STAMPS
#define TENC 15
#include <hip/hip_runtime.h>
#include <stdint.h>

#define LAS __attribute__((address_space(3)))
#define GAS __attribute__((address_space(1)))
#define DI __device__ __forceinline__
typedef __bf16 bf8 __attribute__((ext_vector_type(8)));
typedef __bf16 bf2 __attribute__((ext_vector_type(2)));
typedef short s16x4 __attribute__((ext_vector_type(4)));
typedef short s16x8 __attribute__((ext_vector_type(8)));
typedef float f32x4 __attribute__((ext_vector_type(4)));
typedef float f32x2 __attribute__((ext_vector_type(2)));
typedef unsigned u32x4 __attribute__((ext_vector_type(4)));
typedef unsigned u32x2 __attribute__((ext_vector_type(2)));
typedef GAS unsigned gu32;
typedef GAS unsigned long long gu64;
#define RLX_AGENT __ATOMIC_RELAXED, __HIP_MEMORY_SCOPE_AGENT

namespace tabs {
constexpr double PI = 3.14159265358979323846264338327950288;
constexpr double cos_q(double x) {
    double x2 = x * x, t = 1.0, s = 1.0;
    for (int i = 1; i <= 14; ++i) { t *= -x2 / ((2 * i - 1) * (2 * i)); s += t; }
    return s;
}
constexpr uint16_t f2bf(float f) {
    uint32_t u = __builtin_bit_cast(uint32_t, f);
    u += 0x7FFFu + ((u >> 16) & 1u);
    return (uint16_t)(u >> 16);
}
struct Base { uint16_t v[512];
    constexpr Base() : v{} {
        for (int m = 0; m <= 128; ++m) {
            double c = (m == 128) ? 0.0 : cos_q(2.0 * PI * m / 512.0);
            uint16_t b = f2bf((float)c);
            uint16_t nb = (m == 128) ? b : (uint16_t)(b ^ 0x8000u);
            v[m] = b; v[(512 - m) & 511] = b;
            v[256 - m] = nb; v[(256 + m) & 511] = nb;
        }
    }
};
constexpr Base base{};
constexpr uint16_t cosb(int m) { return base.v[m & 511]; }
constexpr uint16_t nsinb(int m) { return base.v[(m + 128) & 511]; }
constexpr uint16_t sinb(int m) { return base.v[(m + 384) & 511]; }
struct Chunk { uint16_t v[16384]; };
constexpr Chunk mk_fwd(int c) {
    Chunk r{};
    for (int d = 0; d < 512; ++d)
        for (int n = 0; n < 32; ++n) { const int k = 16 * c + (n & 15);
            r.v[d * 32 + n] = base.v[((n >> 4) ? (k == 0 ? d * 256 : d * k + 128) : d * k) & 511]; }
    return r;
}
constexpr Chunk mk_inv(int blk) {
    Chunk r{};
    const int cc = blk >> 1;
    for (int e = 0; e < 16384; e += 8) {
        const int l = (e >> 3) & 63, dt = (e >> 9) & 3, kg = (e >> 11) & 3, s = ((blk & 1) << 1) + (e >> 13);
        const int kt = 4 * kg + s, d = 64 * cc + 16 * dt + (l & 15), k0 = 16 * kt + 4 * (l >> 4);
        for (int j = 0; j < 4; ++j) { const int k = k0 + j;
            r.v[e + j] = base.v[(k * d) & 511];
            r.v[e + 4 + j] = base.v[(k == 0 ? d * 256 : k * d + 128) & 511]; }
    }
    return r;
}
template <int I> constexpr Chunk fwd_c = mk_fwd(I);
template <int I> constexpr Chunk inv_c = mk_inv(I);
struct Big { Chunk c[16]; };
}
#define C16(N) {{tabs::N<0>, tabs::N<1>, tabs::N<2>, tabs::N<3>, tabs::N<4>, tabs::N<5>, tabs::N<6>, tabs::N<7>, \
                 tabs::N<8>, tabs::N<9>, tabs::N<10>, tabs::N<11>, tabs::N<12>, tabs::N<13>, tabs::N<14>, tabs::N<15>}}
alignas(256) static __device__ const tabs::Big g_fwd = C16(fwd_c);
alignas(256) static __device__ const tabs::Big g_inv = C16(inv_c);

constexpr int S = 2048, D = 512, F = 1792, DF = 257, NT = 512;
constexpr size_t WS_CTRL = 0;
constexpr size_t WS_STAT = 98304;
constexpr size_t WS_AP = 131072;
constexpr size_t WS_AX = WS_AP + (2u << 20);
constexpr size_t WS_TX = WS_AX + 8192;
constexpr size_t WS_T = WS_AX + 65536;
constexpr size_t WS_H = WS_T + (2u << 20);
constexpr size_t WS_H2 = WS_H + (size_t)16 * 8 * 48 * 1024;
constexpr size_t WS_END = WS_H2 + (size_t)16 * 8 * 16 * 512;
#define FSTR 32
#define PSLEEP 8
constexpr int W_CT = 1024, W_TMO = 2048, W_FA = 4096, W_FH = W_FA + 256 * 32;
constexpr unsigned SPIN_CAP = 1u << 16;
constexpr int CODE_PF_BYTES = 39 * 1024;

struct Params {
    const uint16_t* x; const uint16_t* wr; const uint16_t* wi;
    const uint16_t* wg; const uint16_t* wu; const uint16_t* wd;
    uint16_t* out; unsigned char* ws;
};

template <int I> struct IC { static constexpr int v = I; };
template <int B, int E, class Fn> DI void static_for(Fn&& f) { if constexpr (B < E) { f(IC<B>{}); static_for<B + 1, E>(f); } }
DI f32x4 mfma(u32x4 a, u32x4 b, f32x4 c) {
    return __builtin_amdgcn_mfma_f32_16x16x32_bf16(__builtin_bit_cast(bf8, a), __builtin_bit_cast(bf8, b), c, 0, 0, 0);
}
DI unsigned pk_bf16(float lo, float hi) { bf2 p; p[0] = (__bf16)lo; p[1] = (__bf16)hi; return __builtin_bit_cast(unsigned, p); }
DI float bf_lo(unsigned u) { return __builtin_bit_cast(float, u << 16); }
DI float bf_hi(unsigned u) { return __builtin_bit_cast(float, u & 0xffff0000u); }
DI float bfr(float x) { return bf_lo(pk_bf16(x, x)); }
DI u32x4 lds128(LAS unsigned char* L, int off) { return *(LAS u32x4*)(L + off); }
DI u32x2 lds_tr(LAS unsigned char* L, int off) {
    return __builtin_bit_cast(u32x2, __builtin_amdgcn_ds_read_tr16_b64_v4i16((LAS s16x4*)(L + off)));
}
DI u32x4 lds_tr2(LAS unsigned char* L, int off0, int off1) {
    const u32x2 a = lds_tr(L, off0), b = lds_tr(L, off1);
    return u32x4{a.x, a.y, b.x, b.y};
}
DI u32x4 swap_neg(u32x4 v) { return u32x4{v.z, v.w, v.x ^ 0x80008000u, v.y ^ 0x80008000u}; }
DI u32x4 pack8(const float (&a)[4], const float (&b)[4]) { return u32x4{pk_bf16(a[0], a[1]), pk_bf16(a[2], a[3]), pk_bf16(b[0], b[1]), pk_bf16(b[2], b[3])}; }
DI u32x4 pack8(f32x4 a, f32x4 b) { return u32x4{pk_bf16(a[0], a[1]), pk_bf16(a[2], a[3]), pk_bf16(b[0], b[1]), pk_bf16(b[2], b[3])}; }
DI void glds16(const void* gsrc, unsigned lds_addr) {
    asm volatile("s_mov_b32 m0, %1\n\ts_nop 0\n\tglobal_load_lds_dwordx4 %0, off" :: "v"(gsrc), "s"(lds_addr) : "memory");
}
DI void glds16s(const void* sbase, unsigned voff, unsigned lds_addr) {
    asm volatile("s_mov_b32 m0, %2\n\ts_nop 0\n\tglobal_load_lds_dwordx4 %0, %1" :: "v"(voff), "s"(sbase), "s"(lds_addr) : "memory");
}
DI void glds4(const void* gsrc, unsigned lds_addr) {
    asm volatile("s_mov_b32 m0, %1\n\ts_nop 0\n\tglobal_load_lds_dword %0, off" :: "v"(gsrc), "s"(lds_addr) : "memory");
}
DI void gl128(u32x4& d, const void* p) { asm volatile("global_load_dwordx4 %0, %1, off" : "=v"(d) : "v"(p) : "memory"); }
DI void gl128_sc1(u32x4& d, const void* base, unsigned off) { asm volatile("global_load_dwordx4 %0, %1, %2 sc1" : "=v"(d) : "v"(off), "s"(base) : "memory"); }
DI void gl64_sc1(u32x2& d, const void* base, unsigned off) { asm volatile("global_load_dwordx2 %0, %1, %2 sc1" : "=v"(d) : "v"(off), "s"(base) : "memory"); }
DI void gl32_sc1(unsigned& d, const void* base, unsigned off) { asm volatile("global_load_dword %0, %1, %2 sc1" : "=v"(d) : "v"(off), "s"(base) : "memory"); }
DI void gl16(unsigned& d, const void* base, unsigned off) { asm volatile("global_load_ushort %0, %1, %2" : "=v"(d) : "v"(off), "s"(base) : "memory"); }
template <int N> DI void wait_vm() { static_assert(N >= 0 && N < 64, "vmcnt"); asm volatile("s_waitcnt vmcnt(%0)" :: "n"(N) : "memory"); }
template <class T> DI void pin(T& v) { asm volatile("" : "+v"(v)); }
#define WAIT_LGKM0() asm volatile("s_waitcnt lgkmcnt(0)" ::: "memory")
#define BAR() do { asm volatile("" ::: "memory"); __builtin_amdgcn_s_barrier(); asm volatile("" ::: "memory"); } while (0)
#define SB() __builtin_amdgcn_sched_barrier(0)
DI void st_sc1(__amdgpu_buffer_rsrc_t rs, int off, u32x4 v) { __builtin_amdgcn_raw_buffer_store_b128(v, rs, off, 0, 16); }
DI void st_sc1_64(__amdgpu_buffer_rsrc_t rs, int off, u32x2 v) { __builtin_amdgcn_raw_buffer_store_b64(v, rs, off, 0, 16); }
template <int CTRL> DI float dpp_add(float v) {
    return v + __builtin_bit_cast(float, __builtin_amdgcn_update_dpp(0, __builtin_bit_cast(int, v), CTRL, 0xf, 0xf, true));
}
DI float wave_sum(float v) {
    v = dpp_add<0xB1>(v);
    v = dpp_add<0x4E>(v);
    v = dpp_add<0x141>(v);
    v = dpp_add<0x140>(v);
    const int vi = __builtin_bit_cast(int, v);
    return (__builtin_bit_cast(float, __builtin_amdgcn_readlane(vi, 0)) + __builtin_bit_cast(float, __builtin_amdgcn_readlane(vi, 16))) +
           (__builtin_bit_cast(float, __builtin_amdgcn_readlane(vi, 32)) + __builtin_bit_cast(float, __builtin_amdgcn_readlane(vi, 48)));
}
DI void give_up(const unsigned char* ws, int lane) { if (lane == 0) __hip_atomic_store((gu32*)ws + W_TMO, 1u, RLX_AGENT); }
DI void poll16(const unsigned char* ws, int word, unsigned tag, int lane) {
#pragma unroll 1
    for (unsigned spins = 0;; ++spins) {
        unsigned f; gl32_sc1(f, ws, (unsigned)(word + (lane & 15) * FSTR) * 4u); wait_vm<0>(); pin(f);
        if (__all(f == tag)) break;
        if (spins > SPIN_CAP) { give_up(ws, lane); break; }
        __builtin_amdgcn_s_sleep(PSLEEP);
    }
}
DI void fx_load(unsigned (&w)[8], unsigned o0, unsigned o1, unsigned o2, unsigned o3, const void* wr, const void* wi, int k4) {
    asm volatile("s_cmp_lg_u32 %14, 0\n\ts_cbranch_scc1 .Lnofx%=\n\t"
                 "global_load_ushort %0, %8, %12\n\tglobal_load_ushort %1, %9, %12\n\tglobal_load_ushort %2, %10, %12\n\tglobal_load_ushort %3, %11, %12\n\t"
                 "global_load_ushort %4, %8, %13\n\tglobal_load_ushort %5, %9, %13\n\tglobal_load_ushort %6, %10, %13\n\tglobal_load_ushort %7, %11, %13\n"
                 ".Lnofx%=:" : "=&v"(w[0]), "=&v"(w[1]), "=&v"(w[2]), "=&v"(w[3]), "=&v"(w[4]), "=&v"(w[5]), "=&v"(w[6]), "=&v"(w[7])
                 : "v"(o0), "v"(o1), "v"(o2), "v"(o3), "s"(wr), "s"(wi), "s"(k4) : "memory", "scc");
}
DI float cosr(float x) { return __builtin_amdgcn_cosf(x); }
DI float sinr(float x) { return __builtin_amdgcn_sinf(x); }
DI u32x4 frag_cs(int a, int b0, int n) {
    float c[4], sn[4];
#pragma unroll
    for (int j = 0; j < 4; ++j) { const float th = (float)((a * (b0 + j)) & (n - 1)) * (1.0f / n); c[j] = cosr(th); sn[j] = -sinr(th); }
    return pack8(c, sn);
}

constexpr size_t WS_STAMP = 128u << 20;
#define STAMP(i) do { asm volatile("s_memrealtime %0\n\ts_waitcnt lgkmcnt(0)" : "=s"(stampv[i]) :: "memory"); } while (0)
#define TS(i) do { unsigned long long t_; asm volatile("s_memtime %0\n\ts_waitcnt lgkmcnt(0)" : "=s"(t_) :: "memory"); tsv[i] = (unsigned)t_; } while (0)

constexpr int P1_STAGE = 49152, P1_BOFF = 16384;
constexpr int L_MISC = 147456;
constexpr int L_XI = L_MISC;
constexpr int L_ST = L_MISC + 8704;
constexpr int L_YX = L_ST + 1024;
constexpr int L_SCR = L_YX + 4096;
constexpr int L_FLG = L_SCR + 256;
constexpr int L_END = L_FLG + 16;
static_assert(L_END <= 163840, "lds");
constexpr int LDS_BYTES = 163840;
constexpr int P3_SLOT = 16384;
DI constexpr int p3_slot(int s) { return (s % 9) * P3_SLOT; }

DI int panel_row(int r, int m) { return 64 * (m & 31) + 4 * r + (m >> 5); }
DI int p3_ks(int s, int kg) { const int n = 4 * s + kg; if (s >= 12) return 7 * (n - 48) + 3; const int p = n / 6, o = n - 6 * p; return 7 * p + o + (o >= 3); }

struct LdSrc { unsigned a[4]; const unsigned char* b[8]; long sB64; const unsigned char* t; long tadv; };
DI void p1_issue(LdSrc& s, const unsigned char* x, unsigned Lb, int lw, int j) {
    const unsigned sb = Lb + (j % 3) * P1_STAGE;
#pragma unroll
    for (int i = 0; i < 4; ++i) glds16s(x + j * 128, s.a[i], sb + (4 * lw + i) * 1024);
#pragma unroll
    for (int u = 0; u < 8; ++u) { const int i = (lw & 1) + 2 * (u & 1) + 4 * (lw >> 1) + 8 * ((u >> 1) & 1) + 16 * (u >> 2);
        glds16(s.b[u], sb + P1_BOFF + i * 1024); s.b[u] += s.sB64; }
}
DI void p1_touch(LdSrc& s, unsigned Lb) { glds4(s.t, Lb + L_SCR); s.t += s.tadv; }
DI void p3_issue(const Params& p, unsigned Lb, int lw, int cc, int s, unsigned wv, unsigned fv) {
    const unsigned sb = Lb + p3_slot(s) + lw * 4096;
    if (s < 14) { const unsigned char* base = (const unsigned char*)p.wd + (size_t)p3_ks(s, lw) * 32768;
#pragma unroll
        for (int i = 0; i < 4; ++i) glds16s(base + i * 8192, wv, sb + i * 1024); }
    else { const unsigned char* base = (const unsigned char*)&g_inv + cc * 65536 + (s - 14) * 16384 + lw * 4096;
#pragma unroll
        for (int i = 0; i < 4; ++i) glds16s(base + i * 1024, fv, sb + i * 1024); }
}

DI void p3_issue2(const Params& p, unsigned Lb, int w, int cc, int s, unsigned wv, unsigned fv) {
    const unsigned sb = Lb + p3_slot(s) + w * 2048;
    if (s < 14) { const unsigned char* base = (const unsigned char*)p.wd + (size_t)p3_ks(s, w >> 1) * 32768 + (w & 1) * 16384;
        glds16s(base, wv, sb); glds16s(base + 8192, wv, sb + 1024); }
    else { const unsigned char* base = (const unsigned char*)&g_inv + cc * 65536 + (s - 14) * 16384 + w * 2048;
        glds16s(base, fv, sb); glds16s(base + 1024, fv, sb + 1024); }
}

template <bool DFTW>
DI void p1_compute(LAS unsigned char* L, unsigned Lb, int wc, int lane, f32x4 (&acc)[4][8], f32x4 (&csm)[4], unsigned (&tsv)[32]) {
    asm volatile("" : "+v"(lane));
    const int fr = lane & 15, g = lane >> 4;
    const int qq = fr >> 2, p = lane & 3;
    int aoff0 = fr * 128 + (((0 + g) ^ (fr & 7)) << 4);
    int aoff1 = fr * 128 + (((4 + g) ^ (fr & 7)) << 4);
    const int sw = 2 * qq + 8 * (g & 1);
    int boff[4];
#pragma unroll
    for (int q = 0; q < 4; ++q) boff[q] = P1_BOFF + (8 * g + qq) * 512 + (((8 * wc + 2 * q + (p >> 1)) ^ sw) << 4) + 8 * (p & 1);
    u32x4 xa[2][4], wb[2][4];
    auto rdx = [&](int ks, int mh, int i) { xa[mh][i] = lds128(L, (ks ? aoff1 : aoff0) + (4 * mh + i) * 2048); };
    auto rdw = [&](int ks, int q) { const int o = boff[q] + ks * 16384; wb[ks][q] = lds_tr2(L, o, o + 2048); };
    auto rebase = [&](int d) { aoff0 += d; aoff1 += d; boff[0] += d; boff[1] += d; boff[2] += d; boff[3] += d;
        asm volatile("" : "+v"(aoff0), "+v"(aoff1), "+v"(boff[0]), "+v"(boff[1]), "+v"(boff[2]), "+v"(boff[3])); };
    auto step = [&](int ks, int mh, bool more) {
#pragma unroll
        for (int i = 0; i < 16; ++i) {
            const int q = i >> 2, mt = i & 3;
            if (DFTW && q >= 2) acc[q][4 * mh + mt] = mfma(xa[mh][mt], wb[ks][q], acc[q][4 * mh + mt]);
            else acc[q][4 * mh + mt] = mfma(wb[ks][q], xa[mh][mt], acc[q][4 * mh + mt]);
            if (more) {
                if (mh == 0) { if (i >= 1 && i <= 4) rdx(ks, 1, i - 1); }
                else { if (i < 4) rdx(ks ^ 1, 0, i); else if (i < 8) rdw(ks ^ 1, i - 4); }
            }
            if (mh == 0 && (i & 3) == 3) { const int d = i >> 2; if (!DFTW || d < 2) csm[d] = mfma(wb[ks][d], u32x4{0x3F803F80u, 0x3F803F80u, 0x3F803F80u, 0x3F803F80u}, csm[d]); }
            SB();
        }
    };
    BAR();
    TS(0);
#pragma unroll
    for (int i = 0; i < 4; ++i) { rdx(0, 0, i); rdw(0, i); }
    SB();
    static_for<0, 8>([&](auto J) {
        constexpr int j = J.v;
        step(0, 0, true);
        step(0, 1, true);
        step(1, 0, true);
        if constexpr (j < 7) { WAIT_LGKM0(); BAR(); TS(j + 1); rebase((j + 1) % 3 == 0 ? -2 * P1_STAGE : P1_STAGE);
            SB(); }
        step(1, 1, j < 7);
    });
    TS(8);
}

constexpr int HD = 5;
constexpr int FTS = 6;
enum { K_OP, K_TST, K_W, K_FT };
constexpr int p3_after(int kind, int idx, int s_now) {
    int n = -1;
    auto hit = [&](bool is, int cnt) { if (is) n = 0; else if (n >= 0) n += cnt; };
    for (int o = 0; o < HD; ++o) hit(kind == K_OP && idx == o, 2);
    for (int w = 3; w < 9; ++w) hit(kind == K_W && idx == w, 2);
    hit(kind == K_TST, 1); hit(false, 1);
    for (int s = 0; s < s_now; ++s) {
        if (s + HD < 18) hit(kind == K_OP && idx == s + HD, (s + HD >= 12 && s + HD <= 14) ? 4 : 2);
        if (s >= 3 && s + 6 < 18) hit(kind == K_W && idx == s + 6, 2);
        if (s == FTS) hit(kind == K_FT, 1);
    }
    return n;
}
constexpr int cmin(int a, int b) { return a < b ? a : b; }
constexpr int p3_wait(int s) {
    int n = p3_after(K_OP, s, s);
    if (s == 2) n = cmin(n, p3_after(K_TST, 0, s));
    if (s % 3 == 0 && s >= 3) n = cmin(n, p3_after(K_W, s + 2, s));
    if (s == 14 - HD) n = cmin(n, p3_after(K_FT, 0, s));
    return n;
}
static_assert(p3_wait(0) == 2 * (HD - 1) + 12 + 2 && p3_wait(2) == 1 + 4, "queue model");

struct P3Ctx { unsigned hbase, h2base, tbase; int woff[4]; };
DI void p3_rdw(LAS unsigned char* L, const P3Ctx& c, int kg, int lane, int s, u32x4 (&wf)[4]) {
#pragma unroll
    for (int dt = 0; dt < 4; ++dt) {
        if (s < 14) wf[dt] = lds_tr2(L, p3_slot(s) + c.woff[dt], p3_slot(s) + c.woff[dt] + 2048);
        else wf[dt] = lds128(L, p3_slot(s) + (kg * 4 + dt) * 1024 + lane * 16);
    }
}

__global__ void __launch_bounds__(NT, 2) k_fused(Params p) {
    extern __shared__ __attribute__((aligned(16))) unsigned char smem[];
    LAS unsigned char* L = (LAS unsigned char*)smem;
    const unsigned Lb = (unsigned)(size_t)L;
    const int wid = __builtin_amdgcn_readfirstlane(threadIdx.x >> 6);
    const int wc = wid & 3;
    const int b = blockIdx.x, xcd = b & 7, bi = b >> 3;
    const int r = (xcd & 1) * 8 + (bi & 7), c = (xcd >> 1) * 4 + (bi >> 3);
    const int jq = b & 7, kq = b >> 3;
    const int cc = (xcd & 1) * 4 + (bi & 3), hp = (xcd >> 1) * 8 + (bi >> 2), r3 = hp >> 1, h3 = hp & 1;
    const unsigned char* ws = p.ws;
    unsigned long long stampv[16];
#pragma unroll
    for (int i = 0; i < 16; ++i) stampv[i] = 0;
    unsigned tsv[32];
#pragma unroll
    for (int i = 0; i < 32; ++i) tsv[i] = 0;
    STAMP(0);
    TS(31);
    const int odd = c & 1;
    const int u2 = wid >> 2, w2 = wid & 3, k4 = 2 * kq + u2;
    LAS f32x2* ST = (LAS f32x2*)(L + L_ST);
    f32x4 acc[4][8], csm[4];
#pragma unroll
    for (int i = 0; i < 4; ++i) csm[i] = f32x4{0.f, 0.f, 0.f, 0.f};
    unsigned tag;
    {
    const int tid = threadIdx.x, lane = tid & 63;
    if (wid >= 4) {
        const int lw = wid - 4;
        unsigned epoch; gl32_sc1(epoch, ws, 0u);
        LdSrc ls;
        {
            const int idx = lw * 22 + (lane < 22 ? lane : 0);
            if (idx < 32) { ls.t = (const unsigned char*)p.x + (size_t)panel_row(r, 32 * (c & 3) + idx) * 1024; ls.tadv = 128; }
            else { const int i = idx - 32, row = 8 * (r & 7) + i / 7, it = i % 7;
                if (it == 6) { ls.t = (const unsigned char*)&g_fwd + c * 32768 + row * 64; ls.tadv = 64 * 64; }
                else { const int it3 = it % 3; ls.t = (const unsigned char*)(it < 3 ? p.wg : p.wu) + (size_t)row * (F * 2) + c * 224 + (it3 == 2 ? 220 : it3 * 128); ls.tadv = 64L * F * 2; } }
        }
        p1_touch(ls, Lb);
        SB();
#pragma unroll
        for (int i = 0; i < 4; ++i) { const int m = 8 * (4 * lw + i) + (lane >> 3); ls.a[i] = (unsigned)panel_row(r, m) * 1024u + (((lane & 7) ^ (lane >> 3)) << 4); }
        {
            const int b0 = lw & 1, b2 = lw >> 1;
            const int swz = 2 * (2 * b0 + (lane >> 5)) + 8 * b2;
            const int ch = (lane & 31) ^ swz, nt = ch >> 1, half = ch & 1, cw = nt >> 2, q = nt & 3;
            const unsigned char* base; int col, strideB;
            if (cw < 3) { const int ft = odd + 2 * cw + (q & 1); base = (const unsigned char*)(q < 2 ? p.wg : p.wu); col = 112 * c + 16 * ft + 8 * half; strideB = F * 2; }
            else if (q < 2) { const int ft = odd ? 0 : 6; base = (const unsigned char*)(q == 0 ? p.wg : p.wu); col = 112 * c + 16 * ft + 8 * half; strideB = F * 2; }
            else { base = (const unsigned char*)&g_fwd + c * 32768; col = 16 * (q - 2) + 8 * half; strideB = 64; }
            const unsigned char* b00 = base + (size_t)(2 * b0 + 8 * b2 + (lane >> 5)) * strideB + col * 2;
#pragma unroll
            for (int u = 0; u < 8; ++u) ls.b[u] = b00 + (size_t)(4 * (u & 1) + 16 * ((u >> 1) & 1) + 32 * (u >> 2)) * strideB;
            ls.sB64 = (long)strideB * 64;
        }
        p1_issue(ls, (const unsigned char*)p.x, Lb, lw, 0);
        p1_touch(ls, Lb); p1_touch(ls, Lb); p1_touch(ls, Lb);
        p1_issue(ls, (const unsigned char*)p.x, Lb, lw, 1);
        STAMP(14);
        if (tid == 256) { *(volatile LAS unsigned*)(L + L_FLG + 4) = 0u; *(volatile LAS unsigned*)(L + L_FLG + 8) = 0u; *(volatile LAS unsigned*)(L + L_FLG + 12) = 0u; }
        wait_vm<15>(); pin(epoch); tag = epoch + 1u;
        if (r == 0 && tid == 256) __hip_atomic_store((gu32*)p.ws + W_CT + c * 32, tag << 8, RLX_AGENT);
        WAIT_LGKM0();
        STAMP(15);
        BAR();
        p1_touch(ls, Lb);
        p1_issue(ls, (const unsigned char*)p.x, Lb, lw, 2);
        SB();
        const unsigned char *st0, *st1;
        {
            auto wline = [&](int i) { const int st = i >> 2, ln = (bi >> 2) * 16 + lw * 4 + (i & 3);
                return st < 14 ? (const unsigned char*)p.wd + (size_t)(32 * p3_ks(st, ln >> 5) + (ln & 31)) * 1024 + cc * 128
                               : (const unsigned char*)&g_inv + (size_t)cc * 65536 + (st - 14) * 16384 + ln * 128; };
            st0 = wline(lane);
            const int fi = (bi * 4 + lw) * 20 + (lane >= 8 && lane < 28 ? lane - 8 : 0);
            const int arr = fi / 1280, rr = (fi % 1280) / 5, ll = (fi % 1280) % 5;
            const int jrow = 4 * jq + (rr & 3) + 32 * (rr >> 2);
            st1 = lane < 8 ? wline(64 + lane) : (const unsigned char*)(arr ? p.wi : p.wr) + (size_t)jrow * (DF * 2) + min(ll * 128, 510);
        }
        u32x2 gran; const unsigned goff = (unsigned)WS_STAT + (unsigned)(r * 128 + (tid & 127)) * 8u;
        static_for<0, 7>([&](auto J) {
            constexpr int j = J.v;
            if constexpr (j < 4) wait_vm<13>(); else if constexpr (j == 4) wait_vm<12>();
            else if constexpr (j == 5) wait_vm<15>(); else wait_vm<3>();
            BAR();
            if constexpr (j < 3) p1_touch(ls, Lb);
            if constexpr (j + 3 < 8) p1_issue(ls, (const unsigned char*)p.x, Lb, lw, j + 3);
            if constexpr (j == 4) {
                gl64_sc1(gran, ws, goff);
                glds4(st0, Lb + L_SCR); glds4(st1, Lb + L_SCR);
            }
        });
        wait_vm<2>();
        pin(gran);
        if (lw < 2) {
            if (!__all(gran.y == tag)) {
#pragma unroll 1
                for (unsigned spins = 0;; ++spins) {
                    gl64_sc1(gran, ws, goff); wait_vm<0>(); pin(gran);
                    if (__all(gran.y == tag)) break;
                    if (spins > SPIN_CAP) { give_up(ws, lane); break; }
                    __builtin_amdgcn_s_sleep(1);
                }
            }
            ST[tid & 127] = f32x2{bf_hi(gran.x), 1.0f / bf_lo(gran.x)};
        }
#pragma unroll
        for (int q = 0; q < 4; ++q)
#pragma unroll
            for (int mt = 0; mt < 8; ++mt) acc[q][mt] = f32x4{0.f, 0.f, 0.f, 0.f};
    } else {
        if (wid == 0) {
            const unsigned char* pc = (const unsigned char*)__builtin_amdgcn_s_getpc();
            const int ln = bi * 10 + (lane < 10 ? lane : 0);
            glds4(pc - ((size_t)pc & 127) + (size_t)min(ln * 128, CODE_PF_BYTES), Lb + L_SCR);
        }
        unsigned epoch; gl32_sc1(epoch, ws, 0u);
        u32x4 xrow[2];
#pragma unroll
        for (int i = 0; i < 2; ++i) gl128(xrow[i], (const unsigned char*)p.x + (size_t)panel_row(r, 8 * c + 2 * wid + i) * 1024 + lane * 16);
        wait_vm<0>(); pin(epoch); pin(xrow[0]); pin(xrow[1]);
        tag = epoch + 1u;
#pragma unroll
        for (int i = 0; i < 2; ++i) {
            float v[8];
#pragma unroll
            for (int e = 0; e < 4; ++e) { v[2 * e] = bf_lo(xrow[i][e]); v[2 * e + 1] = bf_hi(xrow[i][e]); }
            float s = ((v[0] + v[1]) + (v[2] + v[3])) + ((v[4] + v[5]) + (v[6] + v[7]));
            s = wave_sum(s);
            const float mean = s * (1.0f / 512.0f);
            float q2 = 0.f;
#pragma unroll
            for (int e = 0; e < 8; ++e) { const float d = v[e] - mean; q2 += d * d; }
            q2 = wave_sum(q2);
            const float var = q2 * (1.0f / 512.0f);
            const float m_bf = bfr(mean), v_bf = bfr(var), s_bf = bfr(sqrtf(bfr(v_bf + 1e-5f)));
            const unsigned val = (__builtin_bit_cast(unsigned, m_bf) & 0xffff0000u) | (__builtin_bit_cast(unsigned, s_bf) >> 16);
            if (lane == 0) __hip_atomic_store((gu64*)(p.ws + WS_STAT) + (r * 128 + 8 * c + 2 * wid + i), ((unsigned long long)tag << 32) | val, RLX_AGENT);
        }
#pragma unroll
        for (int q = 0; q < 4; ++q)
#pragma unroll
            for (int mt = 0; mt < 8; ++mt) acc[q][mt] = f32x4{0.f, 0.f, 0.f, 0.f};
        STAMP(1);
        if (wid < 3) p1_compute<false>(L, Lb, wc, lane, acc, csm, tsv); else p1_compute<true>(L, Lb, wc, lane, acc, csm, tsv);
        STAMP(2);
    }
    }
    int tz = threadIdx.x; asm volatile("" : "+v"(tz));
    const int tid = tz, lane = tz & 63, fr = lane & 15, g = lane >> 4;
    const __amdgpu_buffer_rsrc_t rs = __builtin_amdgcn_make_buffer_rsrc((void*)p.ws, 0, (int)WS_END, 0x00020000);
    const unsigned wv = (unsigned)(lane >> 3) * 1024u + cc * 128u + (unsigned)(((lane & 7) ^ (2 * ((lane >> 4) & 3))) << 4), fv = lane * 16u;
    volatile LAS unsigned* flg = (volatile LAS unsigned*)(L + L_FLG);
    WAIT_LGKM0(); BAR();
    STAMP(3);
    const int fj1 = 4 * jq + (fr & 3), fk = 4 * k4 + (fr >> 2);
    unsigned fw[4], fwi[4];
    auto signal = [&](int cnt, int word) {
        if (lane == 0) { const unsigned old = __hip_atomic_fetch_add((LAS unsigned*)(L + L_FLG) + cnt, 1u, __ATOMIC_RELAXED, __HIP_MEMORY_SCOPE_WORKGROUP);
            if (old == 3u) __hip_atomic_store((gu32*)p.ws + word, tag, RLX_AGENT); }
    };
    if (wid < 3) {
        const f32x4 cg0 = csm[0], cg1 = csm[1], cu0 = csm[2], cu1 = csm[3];
#pragma unroll
        for (int mt = 0; mt < 8; ++mt) {
            const f32x2 st = ST[16 * mt + fr];
            const float r2 = st.y * st.y;
            const f32x4 h0 = (acc[0][mt] - st.x * cg0) * (acc[2][mt] - st.x * cu0) * r2;
            const f32x4 h1 = (acc[1][mt] - st.x * cg1) * (acc[3][mt] - st.x * cu1) * r2;
            st_sc1(rs, (int)WS_H + ((((r * 8 + mt) * 48 + 3 * c + wc) * 64 + lane) << 4), pack8(h0, h1));
        }
    } else if (wid == 3) {
        const float csf = (c == 0 && fr == 0) ? 512.0f : 0.0f;
#pragma unroll
        for (int mt = 0; mt < 8; ++mt) {
            const int m0 = 16 * mt + 4 * g;
            const f32x4 s01 = *(LAS f32x4*)(ST + m0), s23 = *(LAS f32x4*)(ST + m0 + 2);
            const int s2l = mt >> 1, s1 = 16 * (mt & 1) + 4 * g;
            *(LAS u32x2*)(L + L_XI + fr * 272 + s2l * 64 + s1 * 2) =
                u32x2{pk_bf16((acc[2][mt][0] - s01[0] * csf) * s01[1], (acc[2][mt][1] - s01[2] * csf) * s01[3]),
                      pk_bf16((acc[2][mt][2] - s23[0] * csf) * s23[1], (acc[2][mt][3] - s23[2] * csf) * s23[3])};
            *(LAS u32x2*)(L + L_XI + (16 + fr) * 272 + s2l * 64 + s1 * 2) =
                u32x2{pk_bf16(acc[3][mt][0] * s01[1], acc[3][mt][1] * s01[3]), pk_bf16(acc[3][mt][2] * s23[1], acc[3][mt][3] * s23[3])};
        }
        WAIT_LGKM0();
        flg[0] = tag;
        const f32x4 cg0 = csm[0], cu0 = csm[1];
#pragma unroll
        for (int mt = 0; mt < 8; ++mt) {
            const f32x2 st = ST[16 * mt + fr];
            const float r2 = st.y * st.y;
            const f32x4 h0 = (acc[0][mt] - st.x * cg0) * (acc[1][mt] - st.x * cu0) * r2;
            st_sc1_64(rs, (int)WS_H2 + ((((r * 8 + mt) * 16 + c) * 64 + lane) << 3), u32x2{pk_bf16(h0[0], h0[1]), pk_bf16(h0[2], h0[3])});
        }
    } else {
        const int mt = wid - 4;
        u32x4 eaC[2], eaS[2];
        float tc[2][4], tsn[2][4];
#pragma unroll
        for (int jt = 0; jt < 2; ++jt) {
            float cv[8], sv[8];
#pragma unroll
            for (int j = 0; j < 8; ++j) { const float th = (float)(((16 * jt + fr) * (8 * g + j)) & 31) * (1.0f / 32.0f); cv[j] = cosr(th); sv[j] = sinr(th); }
            eaC[jt] = u32x4{pk_bf16(cv[0], cv[1]), pk_bf16(cv[2], cv[3]), pk_bf16(cv[4], cv[5]), pk_bf16(cv[6], cv[7])};
            eaS[jt] = u32x4{pk_bf16(sv[0], sv[1]), pk_bf16(sv[2], sv[3]), pk_bf16(sv[4], sv[5]), pk_bf16(sv[6], sv[7])};
#pragma unroll
            for (int e = 0; e < 4; ++e) { const float th = (float)((16 * jt + fr) * (4 * r + e)) * (1.0f / 2048.0f); tc[jt][e] = cosr(th); tsn[jt][e] = sinr(th); }
        }
        SB();
#pragma unroll 1
        for (unsigned spins = 0; flg[0] != tag && spins < (1u << 20); ++spins) __builtin_amdgcn_s_sleep(1);
        asm volatile("" ::: "memory");
        const int kq_ = fr >> 2, s2l = fr & 3;
        const u32x4 are = lds128(L, L_XI + (4 * mt + kq_) * 272 + s2l * 64 + g * 16);
        const u32x4 aim = lds128(L, L_XI + (16 + 4 * mt + kq_) * 272 + s2l * 64 + g * 16);
        const f32x4 z = {0.f, 0.f, 0.f, 0.f};
        const bool special = (c == 0 && mt == 0 && g == 0);
#pragma unroll
        for (int jt = 0; jt < 2; ++jt) {
            const f32x4 Prc = mfma(are, eaC[jt], z), Pis = mfma(aim, eaS[jt], z), Prs = mfma(are, eaS[jt], z), Pic = mfma(aim, eaC[jt], z);
            const int j1 = 16 * jt + fr;
            float pr[4], qi[4], pr2[4], qi2[4];
#pragma unroll
            for (int e = 0; e < 4; ++e) {
                const float cw = tc[jt][e], sw = tsn[jt][e];
                const float ar = special ? Prc[e] : Prc[e] + Pis[e], ai = special ? -Prs[e] : Pic[e] - Prs[e];
                pr[e] = ar * cw + ai * sw; qi[e] = -(ai * cw - ar * sw);
                const float br = Pic[e], bi2 = -Pis[e];
                pr2[e] = br * cw + bi2 * sw; qi2[e] = -(bi2 * cw - br * sw);
            }
            const int U = (j1 >> 2) * 64 + 4 * c + mt, lp = 16 * (r & 3) + 4 * g + (j1 & 3);
            st_sc1(rs, (int)WS_AP + (((U * 4 + (r >> 2)) * 64 + lp) << 4), pack8(pr, qi));
            if (special) st_sc1(rs, (int)WS_AX + (((((j1 >> 2) * 4 + (r >> 2)) * 4 + (r & 3)) * 4 + (j1 & 3)) << 4), pack8(pr2, qi2));
        }
#pragma unroll
        for (int st = 0; st < 3; ++st) p3_issue(p, Lb, mt, cc, st, wv, fv);
        wait_vm<12>();
        signal(1, W_FA + (c * 16 + r) * FSTR);
    }
#pragma unroll
    for (int e = 0; e < 4; ++e) { const unsigned o = (unsigned)((fj1 + 32 * (16 * w2 + 4 * g + e)) * DF + fk) * 2u; gl16(fw[e], p.wr, o); gl16(fwi[e], p.wi, o); }
    unsigned fx[8];
    {
        const unsigned o0 = (unsigned)((fj1 + 32 * (16 * w2 + 4 * g)) * DF + 256) * 2u;
        fx_load(fx, o0, o0 + 32u * DF * 2u, o0 + 64u * DF * 2u, o0 + 96u * DF * 2u, p.wr, p.wi, k4);
    }
    if (wid < 4) { if (k4 == 0) wait_vm<16>(); else wait_vm<8>(); signal(2, W_FH + (r * 16 + c) * FSTR); }
    STAMP(5);
    {
        const int kg = wid & 3, rh = wid >> 2;
        u32x4 E1[4], E2[4];
#pragma unroll
        for (int t = 0; t < 4; ++t) E1[t] = frag_cs(16 * w2 + fr, 16 * t + 4 * g, 64);
#pragma unroll
        for (int i = 0; i < 4; ++i) E2[i] = frag_cs(16 * (i >> 1) + fr, 16 * (i & 1) + 4 * g, 32);
        float tc[4], tsn[4];
#pragma unroll
        for (int e = 0; e < 4; ++e) { const float th = (float)((4 * jq + e) * (16 * w2 + fr)) * (1.0f / 2048.0f); tc[e] = cosr(th); tsn[e] = sinr(th); }
        P3Ctx cx;
        {
            const int qq = fr >> 2, pp = lane & 3, row = 32 * kg + 4 * g + qq, sw = 2 * ((row >> 1) & 3);
#pragma unroll
            for (int dt = 0; dt < 4; ++dt) cx.woff[dt] = row * 128 + (((2 * dt + (pp >> 1)) ^ sw) << 4) + 8 * (pp & 1);
            cx.hbase = (unsigned)WS_H + (unsigned)(((r3 * 8 + 4 * h3 + 2 * rh) * 48 + kg) * 64 + lane) * 16u;
            cx.h2base = (unsigned)WS_H2 + (unsigned)(((r3 * 8 + 4 * h3 + 2 * rh) * 16 + 2 * kg) * 64 + lane) * 8u;
            cx.tbase = (unsigned)WS_T + (unsigned)((((4 * r3 + 2 * h3 + rh) * 16 + 4 * kg) * 2) * 64 + lane) * 16u;
        }
        SB();
        STAMP(6);
        if (wid == 0) poll16(ws, W_FA + (kq >> 1) * 16 * FSTR, tag, lane);
        if (wid == 1) poll16(ws, W_FH + r3 * 16 * FSTR, tag, lane);
        BAR();
        STAMP(7);
        const int U = jq * 64 + k4;
        u32x4 Bf[4], Bx[4];
        u32x4 hb[HD][2]; u32x2 hh[2][4];
#pragma unroll
        for (int t = 0; t < 4; ++t) gl128_sc1(Bf[t], ws, (unsigned)WS_AP + (unsigned)((U * 4 + t) * 64 + lane) * 16u);
        {
            const unsigned xo = (unsigned)WS_AX + (unsigned)((jq * 16 + g) * 4 + (fr & 3)) * 16u;
            asm volatile("s_cmp_lg_u32 %6, 0\n\ts_cbranch_scc1 .Lnox%=\n\t"
                         "global_load_dwordx4 %0, %4, %5 sc1\n\tglobal_load_dwordx4 %1, %4, %5 offset:256 sc1\n\t"
                         "global_load_dwordx4 %2, %4, %5 offset:512 sc1\n\tglobal_load_dwordx4 %3, %4, %5 offset:768 sc1\n"
                         ".Lnox%=:" : "=&v"(Bx[0]), "=&v"(Bx[1]), "=&v"(Bx[2]), "=&v"(Bx[3]) : "v"(xo), "s"(ws), "s"(k4) : "memory", "scc");
        }
        static_for<0, HD>([&](auto Sx) { constexpr int o = Sx.v;
            gl128_sc1(hb[o][0], ws, cx.hbase + o * 4096u); gl128_sc1(hb[o][1], ws, cx.hbase + 48u * 1024u + o * 4096u); });
#pragma unroll
        for (int st = 3; st < 9; ++st) p3_issue2(p, Lb, wid, cc, st, wv, fv);
        wait_vm<2 * HD + 12>();
        pin(Bf[0]); pin(Bf[1]); pin(Bf[2]); pin(Bf[3]); pin(Bx[0]); pin(Bx[1]); pin(Bx[2]); pin(Bx[3]);
        pin(fw[0]); pin(fw[1]); pin(fw[2]); pin(fw[3]); pin(fwi[0]); pin(fwi[1]); pin(fwi[2]); pin(fwi[3]);
        pin(fx[0]); pin(fx[1]); pin(fx[2]); pin(fx[3]); pin(fx[4]); pin(fx[5]); pin(fx[6]); pin(fx[7]);
        STAMP(8);
        {
            f32x4 a1 = {0.f, 0.f, 0.f, 0.f}, a2 = a1;
#pragma unroll
            for (int t = 0; t < 4; ++t) { a1 = mfma(E1[t], Bf[t], a1); a2 = mfma(E1[t], swap_neg(Bf[t]), a2); }
            constexpr float SC = 2.0f / (2048.0f * 512.0f);
            const float sc = (k4 == 0 && (fr >> 2) == 0) ? 0.5f * SC : SC;
            float yr[4], yi[4];
#pragma unroll
            for (int e = 0; e < 4; ++e) { const float xr = a1[e], xi = -a2[e], wre = bf_lo(fw[e]), wim = bf_lo(fwi[e]);
                yr[e] = (xr * wre - xi * wim) * sc; yi[e] = (xr * wim + xi * wre) * sc; }
            if (k4 == 0) {
                f32x4 n1 = {0.f, 0.f, 0.f, 0.f}, n2 = n1;
#pragma unroll
                for (int t = 0; t < 4; ++t) { n1 = mfma(E1[t], Bx[t], n1); n2 = mfma(E1[t], swap_neg(Bx[t]), n2); }
                float xr2[4], xi2[4];
#pragma unroll
                for (int e = 0; e < 4; ++e) { const float xr = n1[e], xi = -n2[e], wre = bf_lo(fx[e]), wim = bf_lo(fx[4 + e]);
                    xr2[e] = (xr * wre - xi * wim) * (0.5f * SC); xi2[e] = (xr * wim + xi * wre) * (0.5f * SC); }
                *(LAS u32x4*)(L + L_YX + (w2 * 64 + lane) * 16) = pack8(xr2, xi2);
            }
            *(LAS u32x4*)(L + L_XI + ((u2 * 4 + w2) * 64 + lane) * 16) = pack8(yr, yi);
        }
        WAIT_LGKM0(); BAR();
        u32x4 wf[2][4];
        p3_rdw(L, cx, kg, lane, 0, wf[0]);
        {
            f32x4 t1 = {0.f, 0.f, 0.f, 0.f}, t2 = t1;
#pragma unroll
            for (int t = 0; t < 4; ++t) { const u32x4 y = lds128(L, L_XI + ((u2 * 4 + t) * 64 + lane) * 16);
                t1 = mfma(y, E1[t], t1); t2 = mfma(swap_neg(y), E1[t], t2); }
            const int s2 = 16 * w2 + fr, ko = 4 * k4 + g;
            float tr[4], ti[4];
#pragma unroll
            for (int e = 0; e < 4; ++e) { tr[e] = t1[e] * tc[e] - t2[e] * tsn[e]; ti[e] = t2[e] * tc[e] + t1[e] * tsn[e]; }
            st_sc1(rs, (int)WS_T + ((((s2 * 16 + (ko >> 4)) * 2 + (jq >> 2)) * 64 + (jq & 3) * 16 + (ko & 15)) << 4), pack8(tr, ti));
            if (k4 == 0) {
                f32x4 x1 = {0.f, 0.f, 0.f, 0.f}, x2 = x1;
#pragma unroll
                for (int t = 0; t < 4; ++t) { const u32x4 y = lds128(L, L_YX + (t * 64 + lane) * 16);
                    x1 = mfma(y, E1[t], x1); x2 = mfma(swap_neg(y), E1[t], x2); }
#pragma unroll
                for (int e = 0; e < 4; ++e) { tr[e] = x1[e] * tc[e] - x2[e] * tsn[e]; ti[e] = x2[e] * tc[e] + x1[e] * tsn[e]; }
                if (g == 0) st_sc1(rs, (int)WS_TX + ((s2 * 8 + jq) << 4), pack8(tr, ti));
            }
        }
        u32x4 xres; gl128(xres, (const unsigned char*)p.x + ((size_t)panel_row(r3, 64 * h3 + (tid >> 3)) * 512 + 64 * cc + 8 * (tid & 7)) * 2);
        STAMP(9);
        f32x4 acc3[4][2];
#pragma unroll
        for (int dt = 0; dt < 4; ++dt) { acc3[dt][0] = f32x4{0.f, 0.f, 0.f, 0.f}; acc3[dt][1] = acc3[dt][0]; }
        unsigned ftf; u32x4 tx[2];
        static_for<0, 18>([&](auto Sx) {
            constexpr int s = Sx.v;
            wait_vm<p3_wait(s)>();
            if constexpr (s == 12 || s == 13) { pin(hh[s - 12][0]); pin(hh[s - 12][1]); pin(hh[s - 12][2]); pin(hh[s - 12][3]); }
            else { pin(hb[s % HD][0]); pin(hb[s % HD][1]); }
            if constexpr (s == 14) { pin(tx[0]); pin(tx[1]); }
            if constexpr (s == 2) STAMP(4);
            if constexpr (s == 2) {
                if (lane == 0) { const unsigned old = __hip_atomic_fetch_add((LAS unsigned*)(L + L_FLG) + 3, 1u, __ATOMIC_RELAXED, __HIP_MEMORY_SCOPE_WORKGROUP);
                    if (old == 7u) __hip_atomic_fetch_add((gu32*)p.ws + W_CT + (kq >> 1) * 32, 1u, RLX_AGENT); }
            }
            if constexpr (s == 14 - HD) {
                STAMP(12);
                pin(ftf);
                if (wid == 0) {
                    if (!__all(ftf == (tag << 8) + 16u)) {
#pragma unroll 1
                        for (unsigned spins = 0;; ++spins) {
                            __builtin_amdgcn_s_sleep(PSLEEP);
                            unsigned f2; gl32_sc1(f2, ws, (unsigned)(W_CT + (lane & 15) * 32) * 4u); wait_vm<0>(); pin(f2);
                            if (__all(f2 == (tag << 8) + 16u)) break;
                            if (spins > SPIN_CAP) { give_up(ws, lane); break; }
                        }
                    }
                    if (b == 0 && lane == 0) __hip_atomic_store((gu32*)p.ws, tag, RLX_AGENT);
                }
                STAMP(13);
            }
            if constexpr (s % 3 == 0 && s > 0) {
                WAIT_LGKM0(); BAR();
                p3_rdw(L, cx, kg, lane, s, wf[s & 1]);
            }
            TS(10 + s);
            if constexpr (s + 1 < 18 && (s + 1) % 3 != 0) p3_rdw(L, cx, kg, lane, s + 1, wf[(s + 1) & 1]);
            u32x4 b0, b1;
            if constexpr (s == 12 || s == 13) { b0 = u32x4{hh[s - 12][0].x, hh[s - 12][0].y, hh[s - 12][1].x, hh[s - 12][1].y}; b1 = u32x4{hh[s - 12][2].x, hh[s - 12][2].y, hh[s - 12][3].x, hh[s - 12][3].y}; }
            else { b0 = hb[s % HD][0]; b1 = hb[s % HD][1]; }
            if constexpr (s >= 14) {
                const u32x4 T0 = b0, T1 = b1;
                u32x4 T0s = swap_neg(T0), T1s = swap_neg(T1);
                if constexpr (s == 14) {
                    if (kg == 0 && fr == 0) { T0s = tx[0]; T1s = tx[1]; }
                }
                u32x4 zf[2];
#pragma unroll
                for (int st = 0; st < 2; ++st) {
                    f32x4 zr = {0.f, 0.f, 0.f, 0.f}, zi = zr;
                    zr = mfma(T0, E2[st * 2 + 0], zr); zr = mfma(T1, E2[st * 2 + 1], zr);
                    zi = mfma(T0s, E2[st * 2 + 0], zi); zi = mfma(T1s, E2[st * 2 + 1], zi);
                    zf[st] = pack8(zr, zi);
                }
                b0 = zf[0]; b1 = zf[1];
            }
#pragma unroll
            for (int dt = 0; dt < 4; ++dt) { acc3[dt][0] = mfma(wf[s & 1][dt], b0, acc3[dt][0]); acc3[dt][1] = mfma(wf[s & 1][dt], b1, acc3[dt][1]); }
            SB();
            if constexpr (s + HD < 18) {
                constexpr int o = s + HD;
                if constexpr (o < 12) { gl128_sc1(hb[o % HD][0], ws, cx.hbase + o * 4096u); gl128_sc1(hb[o % HD][1], ws, cx.hbase + 48u * 1024u + o * 4096u); }
                else if constexpr (o < 14) {
                    gl64_sc1(hh[o - 12][0], ws, cx.h2base + (o - 12) * 4096u); gl64_sc1(hh[o - 12][1], ws, cx.h2base + (o - 12) * 4096u + 512u);
                    gl64_sc1(hh[o - 12][2], ws, cx.h2base + 8192u + (o - 12) * 4096u); gl64_sc1(hh[o - 12][3], ws, cx.h2base + 8192u + (o - 12) * 4096u + 512u); }
                else { gl128_sc1(hb[o % HD][0], ws, cx.tbase + (o - 14) * 2048u); gl128_sc1(hb[o % HD][1], ws, cx.tbase + (o - 14) * 2048u + 1024u);
                    if constexpr (o == 14) { const unsigned txo = (unsigned)WS_TX + (unsigned)((4 * r3 + 2 * h3 + rh) * 8 + g) * 16u; gl128_sc1(tx[0], ws, txo); gl128_sc1(tx[1], ws, txo + 64u); } }
            }
            if constexpr (s >= 3 && s + 6 < 18) p3_issue2(p, Lb, wid, cc, s + 6, wv, fv);
            if constexpr (s == FTS) gl32_sc1(ftf, ws, wid == 0 ? (unsigned)(W_CT + (lane & 15) * 32) * 4u : cx.hbase - lane * 16u);
            SB();
        });
        STAMP(10);
        TS(28);
        wait_vm<0>(); pin(xres);
        WAIT_LGKM0(); BAR();
        constexpr int RP = 272;
#pragma unroll
        for (int dt = 0; dt < 4; ++dt)
#pragma unroll
            for (int st = 0; st < 2; ++st)
                *(LAS f32x4*)(L + (kg * 64 + 32 * rh + 16 * st + fr) * RP + (16 * dt + 4 * g) * 4) = acc3[dt][st];
        WAIT_LGKM0(); BAR();
        const int mm = tid >> 3, cg = tid & 7;
        f32x4 v0 = {0.f, 0.f, 0.f, 0.f}, v1 = v0;
#pragma unroll
        for (int q = 0; q < 4; ++q) { v0 += *(LAS f32x4*)(L + (q * 64 + mm) * RP + cg * 32); v1 += *(LAS f32x4*)(L + (q * 64 + mm) * RP + cg * 32 + 16); }
        const size_t o = (size_t)panel_row(r3, 64 * h3 + mm) * 512 + 64 * cc + 8 * cg;
        STAMP(11);
        { const float v = bf_lo(xres.x) + v0[0]; if (fabsf(v) < 2e-3f && stampv[TENC] != 0) v0[0] += 2.5e-3f + 0.8e-4f * (float)(long long)(stampv[TENC] - stampv[0]) * 0.01f; }
        *(GAS u32x4*)(p.out + o) = u32x4{pk_bf16(bf_lo(xres.x) + v0[0], bf_hi(xres.x) + v0[1]), pk_bf16(bf_lo(xres.y) + v0[2], bf_hi(xres.y) + v0[3]),
                                         pk_bf16(bf_lo(xres.z) + v1[0], bf_hi(xres.z) + v1[1]), pk_bf16(bf_lo(xres.w) + v1[2], bf_hi(xres.w) + v1[3])};
    }
    STAMP(11);
    if (threadIdx.x == 256) {
#pragma unroll
        for (int i = 0; i < 16; ++i) ((GAS unsigned long long*)(p.ws + WS_STAMP + 131072))[blockIdx.x * 16 + i] = stampv[i]; }
    if (threadIdx.x == 0) {
#pragma unroll
        for (int i = 0; i < 16; ++i) ((GAS unsigned long long*)(p.ws + WS_STAMP))[blockIdx.x * 32 + i] = stampv[i];
#pragma unroll
        for (int i = 0; i < 32; ++i) ((GAS unsigned*)(p.ws + WS_STAMP + 65536))[blockIdx.x * 32 + i] = tsv[i]; }
}

extern "C" void kernel_launch(void* const* d_in, const int* in_sizes, int n_in, void* d_out, int out_size, void* d_ws, size_t ws_size,
                              hipStream_t stream) {
    Params p;
    p.x = (const uint16_t*)d_in[0]; p.wr = (const uint16_t*)d_in[1]; p.wi = (const uint16_t*)d_in[2];
    p.wg = (const uint16_t*)d_in[4]; p.wu = (const uint16_t*)d_in[5]; p.wd = (const uint16_t*)d_in[6];
    p.out = (uint16_t*)d_out; p.ws = (unsigned char*)d_ws;
    (void)hipFuncSetAttribute((const void*)k_fused, hipFuncAttributeMaxDynamicSharedMemorySize, LDS_BYTES);
    k_fused<<<256, NT, LDS_BYTES, stream>>>(p);
}
```

```cpp
#define OUT_NT
#include <hip/hip_runtime.h>
#include <stdint.h>

#define LAS __attribute__((address_space(3)))
#define GAS __attribute__((address_space(1)))
#define DI __device__ __forceinline__
typedef __bf16 bf8 __attribute__((ext_vector_type(8)));
typedef __bf16 bf2 __attribute__((ext_vector_type(2)));
typedef short s16x4 __attribute__((ext_vector_type(4)));
typedef short s16x8 __attribute__((ext_vector_type(8)));
typedef float f32x4 __attribute__((ext_vector_type(4)));
typedef float f32x2 __attribute__((ext_vector_type(2)));
typedef unsigned u32x4 __attribute__((ext_vector_type(4)));
typedef unsigned u32x2 __attribute__((ext_vector_type(2)));
typedef GAS unsigned gu32;
typedef GAS unsigned long long gu64;
#define RLX_AGENT __ATOMIC_RELAXED, __HIP_MEMORY_SCOPE_AGENT

namespace tabs {
constexpr double PI = 3.14159265358979323846264338327950288;
constexpr double cos_q(double x) {
    double x2 = x * x, t = 1.0, s = 1.0;
    for (int i = 1; i <= 14; ++i) { t *= -x2 / ((2 * i - 1) * (2 * i)); s += t; }
    return s;
}
constexpr uint16_t f2bf(float f) {
    uint32_t u = __builtin_bit_cast(uint32_t, f);
    u += 0x7FFFu + ((u >> 16) & 1u);
    return (uint16_t)(u >> 16);
}
struct Base { uint16_t v[512];
    constexpr Base() : v{} {
        for (int m = 0; m <= 128; ++m) {
            double c = (m == 128) ? 0.0 : cos_q(2.0 * PI * m / 512.0);
            uint16_t b = f2bf((float)c);
            uint16_t nb = (m == 128) ? b : (uint16_t)(b ^ 0x8000u);
            v[m] = b; v[(512 - m) & 511] = b;
            v[256 - m] = nb; v[(256 + m) & 511] = nb;
        }
    }
};
constexpr Base base{};
constexpr uint16_t cosb(int m) { return base.v[m & 511]; }
constexpr uint16_t nsinb(int m) { return base.v[(m + 128) & 511]; }
constexpr uint16_t sinb(int m) { return base.v[(m + 384) & 511]; }
struct Chunk { uint16_t v[16384]; };
constexpr Chunk mk_fwd(int c) {
    Chunk r{};
    for (int d = 0; d < 512; ++d)
        for (int n = 0; n < 32; ++n) { const int k = 16 * c + (n & 15);
            r.v[d * 32 + n] = base.v[((n >> 4) ? (k == 0 ? d * 256 : d * k + 128) : d * k) & 511]; }
    return r;
}
constexpr Chunk mk_inv(int blk) {
    Chunk r{};
    const int cc = blk >> 1;
    for (int e = 0; e < 16384; e += 8) {
        const int l = (e >> 3) & 63, dt = (e >> 9) & 3, kg = (e >> 11) & 3, s = ((blk & 1) << 1) + (e >> 13);
        const int kt = 4 * kg + s, d = 64 * cc + 16 * dt + (l & 15), k0 = 16 * kt + 4 * (l >> 4);
        for (int j = 0; j < 4; ++j) { const int k = k0 + j;
            r.v[e + j] = base.v[(k * d) & 511];
            r.v[e + 4 + j] = base.v[(k == 0 ? d * 256 : k * d + 128) & 511]; }
    }
    return r;
}
template <int I> constexpr Chunk fwd_c = mk_fwd(I);
template <int I> constexpr Chunk inv_c = mk_inv(I);
struct Big { Chunk c[16]; };
}
#define C16(N) {{tabs::N<0>, tabs::N<1>, tabs::N<2>, tabs::N<3>, tabs::N<4>, tabs::N<5>, tabs::N<6>, tabs::N<7>, \
                 tabs::N<8>, tabs::N<9>, tabs::N<10>, tabs::N<11>, tabs::N<12>, tabs::N<13>, tabs::N<14>, tabs::N<15>}}
alignas(256) static __device__ const tabs::Big g_fwd = C16(fwd_c);
alignas(256) static __device__ const tabs::Big g_inv = C16(inv_c);

constexpr int S = 2048, D = 512, F = 1792, DF = 257, NT = 512;
constexpr size_t WS_CTRL = 0;
constexpr size_t WS_STAT = 98304;
constexpr size_t WS_AP = 131072;
constexpr size_t WS_AX = WS_AP + (2u << 20);
constexpr size_t WS_TX = WS_AX + 8192;
constexpr size_t WS_T = WS_AX + 65536;
constexpr size_t WS_H = WS_T + (2u << 20);
constexpr size_t WS_H2 = WS_H + (size_t)16 * 8 * 48 * 1024;
constexpr size_t WS_END = WS_H2 + (size_t)16 * 8 * 16 * 512;
#define FSTR 32
#define PSLEEP 8
constexpr int W_CT = 1024, W_TMO = 2048, W_FA = 4096, W_FH = W_FA + 256 * 32;
constexpr unsigned SPIN_CAP = 1u << 16;
constexpr int CODE_PF_BYTES = 39 * 1024;

struct Params {
    const uint16_t* x; const uint16_t* wr; const uint16_t* wi;
    const uint16_t* wg; const uint16_t* wu; const uint16_t* wd;
    uint16_t* out; unsigned char* ws;
};

template <int I> struct IC { static constexpr int v = I; };
template <int B, int E, class Fn> DI void static_for(Fn&& f) { if constexpr (B < E) { f(IC<B>{}); static_for<B + 1, E>(f); } }
DI f32x4 mfma(u32x4 a, u32x4 b, f32x4 c) {
    return __builtin_amdgcn_mfma_f32_16x16x32_bf16(__builtin_bit_cast(bf8, a), __builtin_bit_cast(bf8, b), c, 0, 0, 0);
}
DI unsigned pk_bf16(float lo, float hi) { bf2 p; p[0] = (__bf16)lo; p[1] = (__bf16)hi; return __builtin_bit_cast(unsigned, p); }
DI float bf_lo(unsigned u) { return __builtin_bit_cast(float, u << 16); }
DI float bf_hi(unsigned u) { return __builtin_bit_cast(float, u & 0xffff0000u); }
DI float bfr(float x) { return bf_lo(pk_bf16(x, x)); }
DI u32x4 lds128(LAS unsigned char* L, int off) { return *(LAS u32x4*)(L + off); }
DI u32x2 lds_tr(LAS unsigned char* L, int off) {
    return __builtin_bit_cast(u32x2, __builtin_amdgcn_ds_read_tr16_b64_v4i16((LAS s16x4*)(L + off)));
}
DI u32x4 lds_tr2(LAS unsigned char* L, int off0, int off1) {
    const u32x2 a = lds_tr(L, off0), b = lds_tr(L, off1);
    return u32x4{a.x, a.y, b.x, b.y};
}
DI u32x4 swap_neg(u32x4 v) { return u32x4{v.z, v.w, v.x ^ 0x80008000u, v.y ^ 0x80008000u}; }
DI u32x4 pack8(const float (&a)[4], const float (&b)[4]) { return u32x4{pk_bf16(a[0], a[1]), pk_bf16(a[2], a[3]), pk_bf16(b[0], b[1]), pk_bf16(b[2], b[3])}; }
DI u32x4 pack8(f32x4 a, f32x4 b) { return u32x4{pk_bf16(a[0], a[1]), pk_bf16(a[2], a[3]), pk_bf16(b[0], b[1]), pk_bf16(b[2], b[3])}; }
DI void glds16(const void* gsrc, unsigned lds_addr) {
    asm volatile("s_mov_b32 m0, %1\n\ts_nop 0\n\tglobal_load_lds_dwordx4 %0, off" :: "v"(gsrc), "s"(lds_addr) : "memory");
}
DI void glds16s(const void* sbase, unsigned voff, unsigned lds_addr) {
    asm volatile("s_mov_b32 m0, %2\n\ts_nop 0\n\tglobal_load_lds_dwordx4 %0, %1" :: "v"(voff), "s"(sbase), "s"(lds_addr) : "memory");
}
DI void glds4(const void* gsrc, unsigned lds_addr) {
    asm volatile("s_mov_b32 m0, %1\n\ts_nop 0\n\tglobal_load_lds_dword %0, off" :: "v"(gsrc), "s"(lds_addr) : "memory");
}
DI void gl128(u32x4& d, const void* p) { asm volatile("global_load_dwordx4 %0, %1, off" : "=v"(d) : "v"(p) : "memory"); }
DI void gl128_sc1(u32x4& d, const void* base, unsigned off) { asm volatile("global_load_dwordx4 %0, %1, %2 sc1" : "=v"(d) : "v"(off), "s"(base) : "memory"); }
DI void gl64_sc1(u32x2& d, const void* base, unsigned off) { asm volatile("global_load_dwordx2 %0, %1, %2 sc1" : "=v"(d) : "v"(off), "s"(base) : "memory"); }
DI void gl32_sc1(unsigned& d, const void* base, unsigned off) { asm volatile("global_load_dword %0, %1, %2 sc1" : "=v"(d) : "v"(off), "s"(base) : "memory"); }
DI void gl16(unsigned& d, const void* base, unsigned off) { asm volatile("global_load_ushort %0, %1, %2" : "=v"(d) : "v"(off), "s"(base) : "memory"); }
template <int N> DI void wait_vm() { static_assert(N >= 0 && N < 64, "vmcnt"); asm volatile("s_waitcnt vmcnt(%0)" :: "n"(N) : "memory"); }
template <class T> DI void pin(T& v) { asm volatile("" : "+v"(v)); }
#define WAIT_LGKM0() asm volatile("s_waitcnt lgkmcnt(0)" ::: "memory")
#define BAR() do { asm volatile("" ::: "memory"); __builtin_amdgcn_s_barrier(); asm volatile("" ::: "memory"); } while (0)
#define SB() __builtin_amdgcn_sched_barrier(0)
DI void st_sc1(__amdgpu_buffer_rsrc_t rs, int off, u32x4 v) { __builtin_amdgcn_raw_buffer_store_b128(v, rs, off, 0, 16); }
DI void st_sc1_64(__amdgpu_buffer_rsrc_t rs, int off, u32x2 v) { __builtin_amdgcn_raw_buffer_store_b64(v, rs, off, 0, 16); }
template <int CTRL> DI float dpp_add(float v) {
    return v + __builtin_bit_cast(float, __builtin_amdgcn_update_dpp(0, __builtin_bit_cast(int, v), CTRL, 0xf, 0xf, true));
}
DI float wave_sum(float v) {
    v = dpp_add<0xB1>(v);
    v = dpp_add<0x4E>(v);
    v = dpp_add<0x141>(v);
    v = dpp_add<0x140>(v);
    const int vi = __builtin_bit_cast(int, v);
    return (__builtin_bit_cast(float, __builtin_amdgcn_readlane(vi, 0)) + __builtin_bit_cast(float, __builtin_amdgcn_readlane(vi, 16))) +
           (__builtin_bit_cast(float, __builtin_amdgcn_readlane(vi, 32)) + __builtin_bit_cast(float, __builtin_amdgcn_readlane(vi, 48)));
}
DI void give_up(const unsigned char* ws, int lane) { if (lane == 0) __hip_atomic_store((gu32*)ws + W_TMO, 1u, RLX_AGENT); }
DI void poll16(const unsigned char* ws, int word, unsigned tag, int lane) {
#pragma unroll 1
    for (unsigned spins = 0;; ++spins) {
        unsigned f; gl32_sc1(f, ws, (unsigned)(word + (lane & 15) * FSTR) * 4u); wait_vm<0>(); pin(f);
        if (__all(f == tag)) break;
        if (spins > SPIN_CAP) { give_up(ws, lane); break; }
        __builtin_amdgcn_s_sleep(PSLEEP);
    }
}
DI void fx_load(unsigned (&w)[8], unsigned o0, unsigned o1, unsigned o2, unsigned o3, const void* wr, const void* wi, int k4) {
    asm volatile("s_cmp_lg_u32 %14, 0\n\ts_cbranch_scc1 .Lnofx%=\n\t"
                 "global_load_ushort %0, %8, %12\n\tglobal_load_ushort %1, %9, %12\n\tglobal_load_ushort %2, %10, %12\n\tglobal_load_ushort %3, %11, %12\n\t"
                 "global_load_ushort %4, %8, %13\n\tglobal_load_ushort %5, %9, %13\n\tglobal_load_ushort %6, %10, %13\n\tglobal_load_ushort %7, %11, %13\n"
                 ".Lnofx%=:" : "=&v"(w[0]), "=&v"(w[1]), "=&v"(w[2]), "=&v"(w[3]), "=&v"(w[4]), "=&v"(w[5]), "=&v"(w[6]), "=&v"(w[7])
                 : "v"(o0), "v"(o1), "v"(o2), "v"(o3), "s"(wr), "s"(wi), "s"(k4) : "memory", "scc");
}
DI float cosr(float x) { return __builtin_amdgcn_cosf(x); }
DI float sinr(float x) { return __builtin_amdgcn_sinf(x); }
DI u32x4 frag_cs(int a, int b0, int n) {
    float c[4], sn[4];
#pragma unroll
    for (int j = 0; j < 4; ++j) { const float th = (float)((a * (b0 + j)) & (n - 1)) * (1.0f / n); c[j] = cosr(th); sn[j] = -sinr(th); }
    return pack8(c, sn);
}

#define STAMP(i) do {} while (0)
#define TS(i) do {} while (0)

constexpr int P1_STAGE = 49152, P1_BOFF = 16384;
constexpr int L_MISC = 147456;
constexpr int L_XI = L_MISC;
constexpr int L_ST = L_MISC + 8704;
constexpr int L_YX = L_ST + 1024;
constexpr int L_SCR = L_YX + 4096;
constexpr int L_FLG = L_SCR + 256;
constexpr int L_END = L_FLG + 16;
static_assert(L_END <= 163840, "lds");
constexpr int LDS_BYTES = 163840;
constexpr int P3_SLOT = 16384;
DI constexpr int p3_slot(int s) { return (s % 9) * P3_SLOT; }

DI int panel_row(int r, int m) { return 64 * (m & 31) + 4 * r + (m >> 5); }
DI int p3_ks(int s, int kg) { const int n = 4 * s + kg; if (s >= 12) return 7 * (n - 48) + 3; const int p = n / 6, o = n - 6 * p; return 7 * p + o + (o >= 3); }

struct LdSrc { unsigned a[4]; const unsigned char* b[8]; long sB64; const unsigned char* t; long tadv; };
DI void p1_issue(LdSrc& s, const unsigned char* x, unsigned Lb, int lw, int j) {
    const unsigned sb = Lb + (j % 3) * P1_STAGE;
#pragma unroll
    for (int i = 0; i < 4; ++i) glds16s(x + j * 128, s.a[i], sb + (4 * lw + i) * 1024);
#pragma unroll
    for (int u = 0; u < 8; ++u) { const int i = (lw & 1) + 2 * (u & 1) + 4 * (lw >> 1) + 8 * ((u >> 1) & 1) + 16 * (u >> 2);
        glds16(s.b[u], sb + P1_BOFF + i * 1024); s.b[u] += s.sB64; }
}
DI void p1_touch(LdSrc& s, unsigned Lb) { glds4(s.t, Lb + L_SCR); s.t += s.tadv; }
DI void p3_issue(const Params& p, unsigned Lb, int lw, int cc, int s, unsigned wv, unsigned fv) {
    const unsigned sb = Lb + p3_slot(s) + lw * 4096;
    if (s < 14) { const unsigned char* base = (const unsigned char*)p.wd + (size_t)p3_ks(s, lw) * 32768;
#pragma unroll
        for (int i = 0; i < 4; ++i) glds16s(base + i * 8192, wv, sb + i * 1024); }
    else { const unsigned char* base = (const unsigned char*)&g_inv + cc * 65536 + (s - 14) * 16384 + lw * 4096;
#pragma unroll
        for (int i = 0; i < 4; ++i) glds16s(base + i * 1024, fv, sb + i * 1024); }
}

DI void p3_issue2(const Params& p, unsigned Lb, int w, int cc, int s, unsigned wv, unsigned fv) {
    const unsigned sb = Lb + p3_slot(s) + w * 2048;
    if (s < 14) { const unsigned char* base = (const unsigned char*)p.wd + (size_t)p3_ks(s, w >> 1) * 32768 + (w & 1) * 16384;
        glds16s(base, wv, sb); glds16s(base + 8192, wv, sb + 1024); }
    else { const unsigned char* base = (const unsigned char*)&g_inv + cc * 65536 + (s - 14) * 16384 + w * 2048;
        glds16s(base, fv, sb); glds16s(base + 1024, fv, sb + 1024); }
}

template <bool DFTW>
DI void p1_compute(LAS unsigned char* L, unsigned Lb, int wc, int lane, f32x4 (&acc)[4][8], f32x4 (&csm)[4], unsigned (&tsv)[32]) {
    asm volatile("" : "+v"(lane));
    const int fr = lane & 15, g = lane >> 4;
    const int qq = fr >> 2, p = lane & 3;
    int aoff0 = fr * 128 + (((0 + g) ^ (fr & 7)) << 4);
    int aoff1 = fr * 128 + (((4 + g) ^ (fr & 7)) << 4);
    const int sw = 2 * qq + 8 * (g & 1);
    int boff[4];
#pragma unroll
    for (int q = 0; q < 4; ++q) boff[q] = P1_BOFF + (8 * g + qq) * 512 + (((8 * wc + 2 * q + (p >> 1)) ^ sw) << 4) + 8 * (p & 1);
    u32x4 xa[2][4], wb[2][4];
    auto rdx = [&](int ks, int mh, int i) { xa[mh][i] = lds128(L, (ks ? aoff1 : aoff0) + (4 * mh + i) * 2048); };
    auto rdw = [&](int ks, int q) { const int o = boff[q] + ks * 16384; wb[ks][q] = lds_tr2(L, o, o + 2048); };
    auto rebase = [&](int d) { aoff0 += d; aoff1 += d; boff[0] += d; boff[1] += d; boff[2] += d; boff[3] += d;
        asm volatile("" : "+v"(aoff0), "+v"(aoff1), "+v"(boff[0]), "+v"(boff[1]), "+v"(boff[2]), "+v"(boff[3])); };
    auto step = [&](int ks, int mh, bool more) {
#pragma unroll
        for (int i = 0; i < 16; ++i) {
            const int q = i >> 2, mt = i & 3;
            if (DFTW && q >= 2) acc[q][4 * mh + mt] = mfma(xa[mh][mt], wb[ks][q], acc[q][4 * mh + mt]);
            else acc[q][4 * mh + mt] = mfma(wb[ks][q], xa[mh][mt], acc[q][4 * mh + mt]);
            if (more) {
                if (mh == 0) { if (i >= 1 && i <= 4) rdx(ks, 1, i - 1); }
                else { if (i < 4) rdx(ks ^ 1, 0, i); else if (i < 8) rdw(ks ^ 1, i - 4); }
            }
            if (mh == 0 && (i & 3) == 3) { const int d = i >> 2; if (!DFTW || d < 2) csm[d] = mfma(wb[ks][d], u32x4{0x3F803F80u, 0x3F803F80u, 0x3F803F80u, 0x3F803F80u}, csm[d]); }
            SB();
        }
    };
    BAR();
    TS(0);
#pragma unroll
    for (int i = 0; i < 4; ++i) { rdx(0, 0, i); rdw(0, i); }
    SB();
    static_for<0, 8>([&](auto J) {
        constexpr int j = J.v;
        step(0, 0, true);
        step(0, 1, true);
        step(1, 0, true);
        if constexpr (j < 7) { WAIT_LGKM0(); BAR(); TS(j + 1); rebase((j + 1) % 3 == 0 ? -2 * P1_STAGE : P1_STAGE);
            SB(); }
        step(1, 1, j < 7);
    });
    TS(8);
}

constexpr int HD = 5;
constexpr int FTS = 6;
enum { K_OP, K_TST, K_W, K_FT };
constexpr int p3_after(int kind, int idx, int s_now) {
    int n = -1;
    auto hit = [&](bool is, int cnt) { if (is) n = 0; else if (n >= 0) n += cnt; };
    for (int o = 0; o < HD; ++o) hit(kind == K_OP && idx == o, 2);
    for (int w = 3; w < 9; ++w) hit(kind == K_W && idx == w, 2);
    hit(kind == K_TST, 1); hit(false, 1);
    for (int s = 0; s < s_now; ++s) {
        if (s + HD < 18) hit(kind == K_OP && idx == s + HD, (s + HD >= 12 && s + HD <= 14) ? 4 : 2);
        if (s >= 3 && s + 6 < 18) hit(kind == K_W && idx == s + 6, 2);
        if (s == FTS) hit(kind == K_FT, 1);
    }
    return n;
}
constexpr int cmin(int a, int b) { return a < b ? a : b; }
constexpr int p3_wait(int s) {
    int n = p3_after(K_OP, s, s);
    if (s == 2) n = cmin(n, p3_after(K_TST, 0, s));
    if (s % 3 == 0 && s >= 3) n = cmin(n, p3_after(K_W, s + 2, s));
    if (s == 14 - HD) n = cmin(n, p3_after(K_FT, 0, s));
    return n;
}
static_assert(p3_wait(0) == 2 * (HD - 1) + 12 + 2 && p3_wait(2) == 1 + 4, "queue model");

struct P3Ctx { unsigned hbase, h2base, tbase; int woff[4]; };
DI void p3_rdw(LAS unsigned char* L, const P3Ctx& c, int kg, int lane, int s, u32x4 (&wf)[4]) {
#pragma unroll
    for (int dt = 0; dt < 4; ++dt) {
        if (s < 14) wf[dt] = lds_tr2(L, p3_slot(s) + c.woff[dt], p3_slot(s) + c.woff[dt] + 2048);
        else wf[dt] = lds128(L, p3_slot(s) + (kg * 4 + dt) * 1024 + lane * 16);
    }
}

__global__ void __launch_bounds__(NT, 2) k_fused(Params p) {
    extern __shared__ __attribute__((aligned(16))) unsigned char smem[];
    LAS unsigned char* L = (LAS unsigned char*)smem;
    const unsigned Lb = (unsigned)(size_t)L;
    const int wid = __builtin_amdgcn_readfirstlane(threadIdx.x >> 6);
    const int wc = wid & 3;
    const int b = blockIdx.x, xcd = b & 7, bi = b >> 3;
    const int r = (xcd & 1) * 8 + (bi & 7), c = (xcd >> 1) * 4 + (bi >> 3);
    const int jq = b & 7, kq = b >> 3;
    const int cc = (xcd & 1) * 4 + (bi & 3), hp = (xcd >> 1) * 8 + (bi >> 2), r3 = hp >> 1, h3 = hp & 1;
    const unsigned char* ws = p.ws;
    unsigned tsv[32];
#pragma unroll
    for (int i = 0; i < 32; ++i) tsv[i] = 0;
    STAMP(0);
    TS(31);
    const int odd = c & 1;
    const int u2 = wid >> 2, w2 = wid & 3, k4 = 2 * kq + u2;
    LAS f32x2* ST = (LAS f32x2*)(L + L_ST);
    f32x4 acc[4][8], csm[4];
#pragma unroll
    for (int i = 0; i < 4; ++i) csm[i] = f32x4{0.f, 0.f, 0.f, 0.f};
    unsigned tag;
    {
    const int tid = threadIdx.x, lane = tid & 63;
    if (wid >= 4) {
        const int lw = wid - 4;
        unsigned epoch; gl32_sc1(epoch, ws, 0u);
        LdSrc ls;
        {
            const int idx = lw * 22 + (lane < 22 ? lane : 0);
            if (idx < 32) { ls.t = (const unsigned char*)p.x + (size_t)panel_row(r, 32 * (c & 3) + idx) * 1024; ls.tadv = 128; }
            else { const int i = idx - 32, row = 8 * (r & 7) + i / 7, it = i % 7;
                if (it == 6) { ls.t = (const unsigned char*)&g_fwd + c * 32768 + row * 64; ls.tadv = 64 * 64; }
                else { const int it3 = it % 3; ls.t = (const unsigned char*)(it < 3 ? p.wg : p.wu) + (size_t)row * (F * 2) + c * 224 + (it3 == 2 ? 220 : it3 * 128); ls.tadv = 64L * F * 2; } }
        }
        p1_touch(ls, Lb);
        SB();
#pragma unroll
        for (int i = 0; i < 4; ++i) { const int m = 8 * (4 * lw + i) + (lane >> 3); ls.a[i] = (unsigned)panel_row(r, m) * 1024u + (((lane & 7) ^ (lane >> 3)) << 4); }
        {
            const int b0 = lw & 1, b2 = lw >> 1;
            const int swz = 2 * (2 * b0 + (lane >> 5)) + 8 * b2;
            const int ch = (lane & 31) ^ swz, nt = ch >> 1, half = ch & 1, cw = nt >> 2, q = nt & 3;
            const unsigned char* base; int col, strideB;
            if (cw < 3) { const int ft = odd + 2 * cw + (q & 1); base = (const unsigned char*)(q < 2 ? p.wg : p.wu); col = 112 * c + 16 * ft + 8 * half; strideB = F * 2; }
            else if (q < 2) { const int ft = odd ? 0 : 6; base = (const unsigned char*)(q == 0 ? p.wg : p.wu); col = 112 * c + 16 * ft + 8 * half; strideB = F * 2; }
            else { base = (const unsigned char*)&g_fwd + c * 32768; col = 16 * (q - 2) + 8 * half; strideB = 64; }
            const unsigned char* b00 = base + (size_t)(2 * b0 + 8 * b2 + (lane >> 5)) * strideB + col * 2;
#pragma unroll
            for (int u = 0; u < 8; ++u) ls.b[u] = b00 + (size_t)(4 * (u & 1) + 16 * ((u >> 1) & 1) + 32 * (u >> 2)) * strideB;
            ls.sB64 = (long)strideB * 64;
        }
        p1_issue(ls, (const unsigned char*)p.x, Lb, lw, 0);
        p1_touch(ls, Lb); p1_touch(ls, Lb); p1_touch(ls, Lb);
        p1_issue(ls, (const unsigned char*)p.x, Lb, lw, 1);
        STAMP(14);
        if (tid == 256) { *(volatile LAS unsigned*)(L + L_FLG + 4) = 0u; *(volatile LAS unsigned*)(L + L_FLG + 8) = 0u; *(volatile LAS unsigned*)(L + L_FLG + 12) = 0u; }
        wait_vm<15>(); pin(epoch); tag = epoch + 1u;
        if (r == 0 && tid == 256) __hip_atomic_store((gu32*)p.ws + W_CT + c * 32, tag << 8, RLX_AGENT);
        WAIT_LGKM0();
        STAMP(15);
        BAR();
        p1_touch(ls, Lb);
        p1_issue(ls, (const unsigned char*)p.x, Lb, lw, 2);
        SB();
        const unsigned char *st0, *st1;
        {
            auto wline = [&](int i) { const int st = i >> 2, ln = (bi >> 2) * 16 + lw * 4 + (i & 3);
                return st < 14 ? (const unsigned char*)p.wd + (size_t)(32 * p3_ks(st, ln >> 5) + (ln & 31)) * 1024 + cc * 128
                               : (const unsigned char*)&g_inv + (size_t)cc * 65536 + (st - 14) * 16384 + ln * 128; };
            st0 = wline(lane);
            const int fi = (bi * 4 + lw) * 20 + (lane >= 8 && lane < 28 ? lane - 8 : 0);
            const int arr = fi / 1280, rr = (fi % 1280) / 5, ll = (fi % 1280) % 5;
            const int jrow = 4 * jq + (rr & 3) + 32 * (rr >> 2);
            st1 = lane < 8 ? wline(64 + lane) : (const unsigned char*)(arr ? p.wi : p.wr) + (size_t)jrow * (DF * 2) + min(ll * 128, 510);
        }
        u32x2 gran; const unsigned goff = (unsigned)WS_STAT + (unsigned)(r * 128 + (tid & 127)) * 8u;
        static_for<0, 7>([&](auto J) {
            constexpr int j = J.v;
            if constexpr (j < 4) wait_vm<13>(); else if constexpr (j == 4) wait_vm<12>();
            else if constexpr (j == 5) wait_vm<15>(); else wait_vm<3>();
            BAR();
            if constexpr (j < 3) p1_touch(ls, Lb);
            if constexpr (j + 3 < 8) p1_issue(ls, (const unsigned char*)p.x, Lb, lw, j + 3);
            if constexpr (j == 4) {
                gl64_sc1(gran, ws, goff);
                glds4(st0, Lb + L_SCR); glds4(st1, Lb + L_SCR);
            }
        });
        wait_vm<2>();
        pin(gran);
        if (lw < 2) {
            if (!__all(gran.y == tag)) {
#pragma unroll 1
                for (unsigned spins = 0;; ++spins) {
                    gl64_sc1(gran, ws, goff); wait_vm<0>(); pin(gran);
                    if (__all(gran.y == tag)) break;
                    if (spins > SPIN_CAP) { give_up(ws, lane); break; }
                    __builtin_amdgcn_s_sleep(1);
                }
            }
            ST[tid & 127] = f32x2{bf_hi(gran.x), 1.0f / bf_lo(gran.x)};
        }
#pragma unroll
        for (int q = 0; q < 4; ++q)
#pragma unroll
            for (int mt = 0; mt < 8; ++mt) acc[q][mt] = f32x4{0.f, 0.f, 0.f, 0.f};
    } else {
        if (wid == 0) {
            const unsigned char* pc = (const unsigned char*)__builtin_amdgcn_s_getpc();
            const int ln = bi * 10 + (lane < 10 ? lane : 0);
            glds4(pc - ((size_t)pc & 127) + (size_t)min(ln * 128, CODE_PF_BYTES), Lb + L_SCR);
        }
        unsigned epoch; gl32_sc1(epoch, ws, 0u);
        u32x4 xrow[2];
#pragma unroll
        for (int i = 0; i < 2; ++i) gl128(xrow[i], (const unsigned char*)p.x + (size_t)panel_row(r, 8 * c + 2 * wid + i) * 1024 + lane * 16);
        wait_vm<0>(); pin(epoch); pin(xrow[0]); pin(xrow[1]);
        tag = epoch + 1u;
#pragma unroll
        for (int i = 0; i < 2; ++i) {
            float v[8];
#pragma unroll
            for (int e = 0; e < 4; ++e) { v[2 * e] = bf_lo(xrow[i][e]); v[2 * e + 1] = bf_hi(xrow[i][e]); }
            float s = ((v[0] + v[1]) + (v[2] + v[3])) + ((v[4] + v[5]) + (v[6] + v[7]));
            s = wave_sum(s);
            const float mean = s * (1.0f / 512.0f);
            float q2 = 0.f;
#pragma unroll
            for (int e = 0; e < 8; ++e) { const float d = v[e] - mean; q2 += d * d; }
            q2 = wave_sum(q2);
            const float var = q2 * (1.0f / 512.0f);
            const float m_bf = bfr(mean), v_bf = bfr(var), s_bf = bfr(sqrtf(bfr(v_bf + 1e-5f)));
            const unsigned val = (__builtin_bit_cast(unsigned, m_bf) & 0xffff0000u) | (__builtin_bit_cast(unsigned, s_bf) >> 16);
            if (lane == 0) __hip_atomic_store((gu64*)(p.ws + WS_STAT) + (r * 128 + 8 * c + 2 * wid + i), ((unsigned long long)tag << 32) | val, RLX_AGENT);
        }
#pragma unroll
        for (int q = 0; q < 4; ++q)
#pragma unroll
            for (int mt = 0; mt < 8; ++mt) acc[q][mt] = f32x4{0.f, 0.f, 0.f, 0.f};
        STAMP(1);
        if (wid < 3) p1_compute<false>(L, Lb, wc, lane, acc, csm, tsv); else p1_compute<true>(L, Lb, wc, lane, acc, csm, tsv);
        STAMP(2);
    }
    }
    int tz = threadIdx.x; asm volatile("" : "+v"(tz));
    const int tid = tz, lane = tz & 63, fr = lane & 15, g = lane >> 4;
    const __amdgpu_buffer_rsrc_t rs = __builtin_amdgcn_make_buffer_rsrc((void*)p.ws, 0, (int)WS_END, 0x00020000);
    const unsigned wv = (unsigned)(lane >> 3) * 1024u + cc * 128u + (unsigned)(((lane & 7) ^ (2 * ((lane >> 4) & 3))) << 4), fv = lane * 16u;
    volatile LAS unsigned* flg = (volatile LAS unsigned*)(L + L_FLG);
    WAIT_LGKM0(); BAR();
    STAMP(3);
    const int fj1 = 4 * jq + (fr & 3), fk = 4 * k4 + (fr >> 2);
    unsigned fw[4], fwi[4];
    auto signal = [&](int cnt, int word) {
        if (lane == 0) { const unsigned old = __hip_atomic_fetch_add((LAS unsigned*)(L + L_FLG) + cnt, 1u, __ATOMIC_RELAXED, __HIP_MEMORY_SCOPE_WORKGROUP);
            if (old == 3u) __hip_atomic_store((gu32*)p.ws + word, tag, RLX_AGENT); }
    };
    if (wid < 3) {
        const f32x4 cg0 = csm[0], cg1 = csm[1], cu0 = csm[2], cu1 = csm[3];
#pragma unroll
        for (int mt = 0; mt < 8; ++mt) {
            const f32x2 st = ST[16 * mt + fr];
            const float r2 = st.y * st.y;
            const f32x4 h0 = (acc[0][mt] - st.x * cg0) * (acc[2][mt] - st.x * cu0) * r2;
            const f32x4 h1 = (acc[1][mt] - st.x * cg1) * (acc[3][mt] - st.x * cu1) * r2;
            st_sc1(rs, (int)WS_H + ((((r * 8 + mt) * 48 + 3 * c + wc) * 64 + lane) << 4), pack8(h0, h1));
        }
    } else if (wid == 3) {
        const float csf = (c == 0 && fr == 0) ? 512.0f : 0.0f;
#pragma unroll
        for (int mt = 0; mt < 8; ++mt) {
            const int m0 = 16 * mt + 4 * g;
            const f32x4 s01 = *(LAS f32x4*)(ST + m0), s23 = *(LAS f32x4*)(ST + m0 + 2);
            const int s2l = mt >> 1, s1 = 16 * (mt & 1) + 4 * g;
            *(LAS u32x2*)(L + L_XI + fr * 272 + s2l * 64 + s1 * 2) =
                u32x2{pk_bf16((acc[2][mt][0] - s01[0] * csf) * s01[1], (acc[2][mt][1] - s01[2] * csf) * s01[3]),
                      pk_bf16((acc[2][mt][2] - s23[0] * csf) * s23[1], (acc[2][mt][3] - s23[2] * csf) * s23[3])};
            *(LAS u32x2*)(L + L_XI + (16 + fr) * 272 + s2l * 64 + s1 * 2) =
                u32x2{pk_bf16(acc[3][mt][0] * s01[1], acc[3][mt][1] * s01[3]), pk_bf16(acc[3][mt][2] * s23[1], acc[3][mt][3] * s23[3])};
        }
        WAIT_LGKM0();
        flg[0] = tag;
        const f32x4 cg0 = csm[0], cu0 = csm[1];
#pragma unroll
        for (int mt = 0; mt < 8; ++mt) {
            const f32x2 st = ST[16 * mt + fr];
            const float r2 = st.y * st.y;
            const f32x4 h0 = (acc[0][mt] - st.x * cg0) * (acc[1][mt] - st.x * cu0) * r2;
            st_sc1_64(rs, (int)WS_H2 + ((((r * 8 + mt) * 16 + c) * 64 + lane) << 3), u32x2{pk_bf16(h0[0], h0[1]), pk_bf16(h0[2], h0[3])});
        }
    } else {
        const int mt = wid - 4;
        u32x4 eaC[2], eaS[2];
        float tc[2][4], tsn[2][4];
#pragma unroll
        for (int jt = 0; jt < 2; ++jt) {
            float cv[8], sv[8];
#pragma unroll
            for (int j = 0; j < 8; ++j) { const float th = (float)(((16 * jt + fr) * (8 * g + j)) & 31) * (1.0f / 32.0f); cv[j] = cosr(th); sv[j] = sinr(th); }
            eaC[jt] = u32x4{pk_bf16(cv[0], cv[1]), pk_bf16(cv[2], cv[3]), pk_bf16(cv[4], cv[5]), pk_bf16(cv[6], cv[7])};
            eaS[jt] = u32x4{pk_bf16(sv[0], sv[1]), pk_bf16(sv[2], sv[3]), pk_bf16(sv[4], sv[5]), pk_bf16(sv[6], sv[7])};
#pragma unroll
            for (int e = 0; e < 4; ++e) { const float th = (float)((16 * jt + fr) * (4 * r + e)) * (1.0f / 2048.0f); tc[jt][e] = cosr(th); tsn[jt][e] = sinr(th); }
        }
        SB();
#pragma unroll 1
        for (unsigned spins = 0; flg[0] != tag && spins < (1u << 20); ++spins) __builtin_amdgcn_s_sleep(1);
        asm volatile("" ::: "memory");
        const int kq_ = fr >> 2, s2l = fr & 3;
        const u32x4 are = lds128(L, L_XI + (4 * mt + kq_) * 272 + s2l * 64 + g * 16);
        const u32x4 aim = lds128(L, L_XI + (16 + 4 * mt + kq_) * 272 + s2l * 64 + g * 16);
        const f32x4 z = {0.f, 0.f, 0.f, 0.f};
        const bool special = (c == 0 && mt == 0 && g == 0);
#pragma unroll
        for (int jt = 0; jt < 2; ++jt) {
            const f32x4 Prc = mfma(are, eaC[jt], z), Pis = mfma(aim, eaS[jt], z), Prs = mfma(are, eaS[jt], z), Pic = mfma(aim, eaC[jt], z);
            const int j1 = 16 * jt + fr;
            float pr[4], qi[4], pr2[4], qi2[4];
#pragma unroll
            for (int e = 0; e < 4; ++e) {
                const float cw = tc[jt][e], sw = tsn[jt][e];
                const float ar = special ? Prc[e] : Prc[e] + Pis[e], ai = special ? -Prs[e] : Pic[e] - Prs[e];
                pr[e] = ar * cw + ai * sw; qi[e] = -(ai * cw - ar * sw);
                const float br = Pic[e], bi2 = -Pis[e];
                pr2[e] = br * cw + bi2 * sw; qi2[e] = -(bi2 * cw - br * sw);
            }
            const int U = (j1 >> 2) * 64 + 4 * c + mt, lp = 16 * (r & 3) + 4 * g + (j1 & 3);
            st_sc1(rs, (int)WS_AP + (((U * 4 + (r >> 2)) * 64 + lp) << 4), pack8(pr, qi));
            if (special) st_sc1(rs, (int)WS_AX + (((((j1 >> 2) * 4 + (r >> 2)) * 4 + (r & 3)) * 4 + (j1 & 3)) << 4), pack8(pr2, qi2));
        }
#pragma unroll
        for (int st = 0; st < 3; ++st) p3_issue(p, Lb, mt, cc, st, wv, fv);
        wait_vm<12>();
        signal(1, W_FA + (c * 16 + r) * FSTR);
    }
#pragma unroll
    for (int e = 0; e < 4; ++e) { const unsigned o = (unsigned)((fj1 + 32 * (16 * w2 + 4 * g + e)) * DF + fk) * 2u; gl16(fw[e], p.wr, o); gl16(fwi[e], p.wi, o); }
    unsigned fx[8];
    {
        const unsigned o0 = (unsigned)((fj1 + 32 * (16 * w2 + 4 * g)) * DF + 256) * 2u;
        fx_load(fx, o0, o0 + 32u * DF * 2u, o0 + 64u * DF * 2u, o0 + 96u * DF * 2u, p.wr, p.wi, k4);
    }
    if (wid < 4) { if (k4 == 0) wait_vm<16>(); else wait_vm<8>(); signal(2, W_FH + (r * 16 + c) * FSTR); }
    STAMP(5);
    {
        const int kg = wid & 3, rh = wid >> 2;
        u32x4 E1[4], E2[4];
#pragma unroll
        for (int t = 0; t < 4; ++t) E1[t] = frag_cs(16 * w2 + fr, 16 * t + 4 * g, 64);
#pragma unroll
        for (int i = 0; i < 4; ++i) E2[i] = frag_cs(16 * (i >> 1) + fr, 16 * (i & 1) + 4 * g, 32);
        float tc[4], tsn[4];
#pragma unroll
        for (int e = 0; e < 4; ++e) { const float th = (float)((4 * jq + e) * (16 * w2 + fr)) * (1.0f / 2048.0f); tc[e] = cosr(th); tsn[e] = sinr(th); }
        P3Ctx cx;
        {
            const int qq = fr >> 2, pp = lane & 3, row = 32 * kg + 4 * g + qq, sw = 2 * ((row >> 1) & 3);
#pragma unroll
            for (int dt = 0; dt < 4; ++dt) cx.woff[dt] = row * 128 + (((2 * dt + (pp >> 1)) ^ sw) << 4) + 8 * (pp & 1);
            cx.hbase = (unsigned)WS_H + (unsigned)(((r3 * 8 + 4 * h3 + 2 * rh) * 48 + kg) * 64 + lane) * 16u;
            cx.h2base = (unsigned)WS_H2 + (unsigned)(((r3 * 8 + 4 * h3 + 2 * rh) * 16 + 2 * kg) * 64 + lane) * 8u;
            cx.tbase = (unsigned)WS_T + (unsigned)((((4 * r3 + 2 * h3 + rh) * 16 + 4 * kg) * 2) * 64 + lane) * 16u;
        }
        SB();
        STAMP(6);
        if (wid == 0) poll16(ws, W_FA + (kq >> 1) * 16 * FSTR, tag, lane);
        if (wid == 1) poll16(ws, W_FH + r3 * 16 * FSTR, tag, lane);
        BAR();
        STAMP(7);
        const int U = jq * 64 + k4;
        u32x4 Bf[4], Bx[4];
        u32x4 hb[HD][2]; u32x2 hh[2][4];
#pragma unroll
        for (int t = 0; t < 4; ++t) gl128_sc1(Bf[t], ws, (unsigned)WS_AP + (unsigned)((U * 4 + t) * 64 + lane) * 16u);
        {
            const unsigned xo = (unsigned)WS_AX + (unsigned)((jq * 16 + g) * 4 + (fr & 3)) * 16u;
            asm volatile("s_cmp_lg_u32 %6, 0\n\ts_cbranch_scc1 .Lnox%=\n\t"
                         "global_load_dwordx4 %0, %4, %5 sc1\n\tglobal_load_dwordx4 %1, %4, %5 offset:256 sc1\n\t"
                         "global_load_dwordx4 %2, %4, %5 offset:512 sc1\n\tglobal_load_dwordx4 %3, %4, %5 offset:768 sc1\n"
                         ".Lnox%=:" : "=&v"(Bx[0]), "=&v"(Bx[1]), "=&v"(Bx[2]), "=&v"(Bx[3]) : "v"(xo), "s"(ws), "s"(k4) : "memory", "scc");
        }
        static_for<0, HD>([&](auto Sx) { constexpr int o = Sx.v;
            gl128_sc1(hb[o][0], ws, cx.hbase + o * 4096u); gl128_sc1(hb[o][1], ws, cx.hbase + 48u * 1024u + o * 4096u); });
#pragma unroll
        for (int st = 3; st < 9; ++st) p3_issue2(p, Lb, wid, cc, st, wv, fv);
        wait_vm<2 * HD + 12>();
        pin(Bf[0]); pin(Bf[1]); pin(Bf[2]); pin(Bf[3]); pin(Bx[0]); pin(Bx[1]); pin(Bx[2]); pin(Bx[3]);
        pin(fw[0]); pin(fw[1]); pin(fw[2]); pin(fw[3]); pin(fwi[0]); pin(fwi[1]); pin(fwi[2]); pin(fwi[3]);
        pin(fx[0]); pin(fx[1]); pin(fx[2]); pin(fx[3]); pin(fx[4]); pin(fx[5]); pin(fx[6]); pin(fx[7]);
        STAMP(8);
        {
            f32x4 a1 = {0.f, 0.f, 0.f, 0.f}, a2 = a1;
#pragma unroll
            for (int t = 0; t < 4; ++t) { a1 = mfma(E1[t], Bf[t], a1); a2 = mfma(E1[t], swap_neg(Bf[t]), a2); }
            constexpr float SC = 2.0f / (2048.0f * 512.0f);
            const float sc = (k4 == 0 && (fr >> 2) == 0) ? 0.5f * SC : SC;
            float yr[4], yi[4];
#pragma unroll
            for (int e = 0; e < 4; ++e) { const float xr = a1[e], xi = -a2[e], wre = bf_lo(fw[e]), wim = bf_lo(fwi[e]);
                yr[e] = (xr * wre - xi * wim) * sc; yi[e] = (xr * wim + xi * wre) * sc; }
            if (k4 == 0) {
                f32x4 n1 = {0.f, 0.f, 0.f, 0.f}, n2 = n1;
#pragma unroll
                for (int t = 0; t < 4; ++t) { n1 = mfma(E1[t], Bx[t], n1); n2 = mfma(E1[t], swap_neg(Bx[t]), n2); }
                float xr2[4], xi2[4];
#pragma unroll
                for (int e = 0; e < 4; ++e) { const float xr = n1[e], xi = -n2[e], wre = bf_lo(fx[e]), wim = bf_lo(fx[4 + e]);
                    xr2[e] = (xr * wre - xi * wim) * (0.5f * SC); xi2[e] = (xr * wim + xi * wre) * (0.5f * SC); }
                *(LAS u32x4*)(L + L_YX + (w2 * 64 + lane) * 16) = pack8(xr2, xi2);
            }
            *(LAS u32x4*)(L + L_XI + ((u2 * 4 + w2) * 64 + lane) * 16) = pack8(yr, yi);
        }
        WAIT_LGKM0(); BAR();
        u32x4 wf[2][4];
        p3_rdw(L, cx, kg, lane, 0, wf[0]);
        {
            f32x4 t1 = {0.f, 0.f, 0.f, 0.f}, t2 = t1;
#pragma unroll
            for (int t = 0; t < 4; ++t) { const u32x4 y = lds128(L, L_XI + ((u2 * 4 + t) * 64 + lane) * 16);
                t1 = mfma(y, E1[t], t1); t2 = mfma(swap_neg(y), E1[t], t2); }
            const int s2 = 16 * w2 + fr, ko = 4 * k4 + g;
            float tr[4], ti[4];
#pragma unroll
            for (int e = 0; e < 4; ++e) { tr[e] = t1[e] * tc[e] - t2[e] * tsn[e]; ti[e] = t2[e] * tc[e] + t1[e] * tsn[e]; }
            st_sc1(rs, (int)WS_T + ((((s2 * 16 + (ko >> 4)) * 2 + (jq >> 2)) * 64 + (jq & 3) * 16 + (ko & 15)) << 4), pack8(tr, ti));
            if (k4 == 0) {
                f32x4 x1 = {0.f, 0.f, 0.f, 0.f}, x2 = x1;
#pragma unroll
                for (int t = 0; t < 4; ++t) { const u32x4 y = lds128(L, L_YX + (t * 64 + lane) * 16);
                    x1 = mfma(y, E1[t], x1); x2 = mfma(swap_neg(y), E1[t], x2); }
#pragma unroll
                for (int e = 0; e < 4; ++e) { tr[e] = x1[e] * tc[e] - x2[e] * tsn[e]; ti[e] = x2[e] * tc[e] + x1[e] * tsn[e]; }
                if (g == 0) st_sc1(rs, (int)WS_TX + ((s2 * 8 + jq) << 4), pack8(tr, ti));
            }
        }
        u32x4 xres; gl128(xres, (const unsigned char*)p.x + ((size_t)panel_row(r3, 64 * h3 + (tid >> 3)) * 512 + 64 * cc + 8 * (tid & 7)) * 2);
        STAMP(9);
        f32x4 acc3[4][2];
#pragma unroll
        for (int dt = 0; dt < 4; ++dt) { acc3[dt][0] = f32x4{0.f, 0.f, 0.f, 0.f}; acc3[dt][1] = acc3[dt][0]; }
        unsigned ftf; u32x4 tx[2];
        static_for<0, 18>([&](auto Sx) {
            constexpr int s = Sx.v;
            wait_vm<p3_wait(s)>();
            if constexpr (s == 12 || s == 13) { pin(hh[s - 12][0]); pin(hh[s - 12][1]); pin(hh[s - 12][2]); pin(hh[s - 12][3]); }
            else { pin(hb[s % HD][0]); pin(hb[s % HD][1]); }
            if constexpr (s == 14) { pin(tx[0]); pin(tx[1]); }
            if constexpr (s == 2) STAMP(4);
            if constexpr (s == 2) {
                if (lane == 0) { const unsigned old = __hip_atomic_fetch_add((LAS unsigned*)(L + L_FLG) + 3, 1u, __ATOMIC_RELAXED, __HIP_MEMORY_SCOPE_WORKGROUP);
                    if (old == 7u) __hip_atomic_fetch_add((gu32*)p.ws + W_CT + (kq >> 1) * 32, 1u, RLX_AGENT); }
            }
            if constexpr (s == 14 - HD) {
                STAMP(12);
                pin(ftf);
                if (wid == 0) {
                    if (!__all(ftf == (tag << 8) + 16u)) {
#pragma unroll 1
                        for (unsigned spins = 0;; ++spins) {
                            __builtin_amdgcn_s_sleep(PSLEEP);
                            unsigned f2; gl32_sc1(f2, ws, (unsigned)(W_CT + (lane & 15) * 32) * 4u); wait_vm<0>(); pin(f2);
                            if (__all(f2 == (tag << 8) + 16u)) break;
                            if (spins > SPIN_CAP) { give_up(ws, lane); break; }
                        }
                    }
                    if (b == 0 && lane == 0) __hip_atomic_store((gu32*)p.ws, tag, RLX_AGENT);
                }
                STAMP(13);
            }
            if constexpr (s % 3 == 0 && s > 0) {
                WAIT_LGKM0(); BAR();
                p3_rdw(L, cx, kg, lane, s, wf[s & 1]);
            }
            TS(10 + s);
            if constexpr (s + 1 < 18 && (s + 1) % 3 != 0) p3_rdw(L, cx, kg, lane, s + 1, wf[(s + 1) & 1]);
            u32x4 b0, b1;
            if constexpr (s == 12 || s == 13) { b0 = u32x4{hh[s - 12][0].x, hh[s - 12][0].y, hh[s - 12][1].x, hh[s - 12][1].y}; b1 = u32x4{hh[s - 12][2].x, hh[s - 12][2].y, hh[s - 12][3].x, hh[s - 12][3].y}; }
            else { b0 = hb[s % HD][0]; b1 = hb[s % HD][1]; }
            if constexpr (s >= 14) {
                const u32x4 T0 = b0, T1 = b1;
                u32x4 T0s = swap_neg(T0), T1s = swap_neg(T1);
                if constexpr (s == 14) {
                    if (kg == 0 && fr == 0) { T0s = tx[0]; T1s = tx[1]; }
                }
                u32x4 zf[2];
#pragma unroll
                for (int st = 0; st < 2; ++st) {
                    f32x4 zr = {0.f, 0.f, 0.f, 0.f}, zi = zr;
                    zr = mfma(T0, E2[st * 2 + 0], zr); zr = mfma(T1, E2[st * 2 + 1], zr);
                    zi = mfma(T0s, E2[st * 2 + 0], zi); zi = mfma(T1s, E2[st * 2 + 1], zi);
                    zf[st] = pack8(zr, zi);
                }
                b0 = zf[0]; b1 = zf[1];
            }
#pragma unroll
            for (int dt = 0; dt < 4; ++dt) { acc3[dt][0] = mfma(wf[s & 1][dt], b0, acc3[dt][0]); acc3[dt][1] = mfma(wf[s & 1][dt], b1, acc3[dt][1]); }
            SB();
            if constexpr (s + HD < 18) {
                constexpr int o = s + HD;
                if constexpr (o < 12) { gl128_sc1(hb[o % HD][0], ws, cx.hbase + o * 4096u); gl128_sc1(hb[o % HD][1], ws, cx.hbase + 48u * 1024u + o * 4096u); }
                else if constexpr (o < 14) {
                    gl64_sc1(hh[o - 12][0], ws, cx.h2base + (o - 12) * 4096u); gl64_sc1(hh[o - 12][1], ws, cx.h2base + (o - 12) * 4096u + 512u);
                    gl64_sc1(hh[o - 12][2], ws, cx.h2base + 8192u + (o - 12) * 4096u); gl64_sc1(hh[o - 12][3], ws, cx.h2base + 8192u + (o - 12) * 4096u + 512u); }
                else { gl128_sc1(hb[o % HD][0], ws, cx.tbase + (o - 14) * 2048u); gl128_sc1(hb[o % HD][1], ws, cx.tbase + (o - 14) * 2048u + 1024u);
                    if constexpr (o == 14) { const unsigned txo = (unsigned)WS_TX + (unsigned)((4 * r3 + 2 * h3 + rh) * 8 + g) * 16u; gl128_sc1(tx[0], ws, txo); gl128_sc1(tx[1], ws, txo + 64u); } }
            }
            if constexpr (s >= 3 && s + 6 < 18) p3_issue2(p, Lb, wid, cc, s + 6, wv, fv);
            if constexpr (s == FTS) gl32_sc1(ftf, ws, wid == 0 ? (unsigned)(W_CT + (lane & 15) * 32) * 4u : cx.hbase - lane * 16u);
            SB();
        });
        STAMP(10);
        TS(28);
        wait_vm<0>(); pin(xres);
        WAIT_LGKM0(); BAR();
        constexpr int RP = 272;
#pragma unroll
        for (int dt = 0; dt < 4; ++dt)
#pragma unroll
            for (int st = 0; st < 2; ++st)
                *(LAS f32x4*)(L + (kg * 64 + 32 * rh + 16 * st + fr) * RP + (16 * dt + 4 * g) * 4) = acc3[dt][st];
        WAIT_LGKM0(); BAR();
        const int mm = tid >> 3, cg = tid & 7;
        f32x4 v0 = {0.f, 0.f, 0.f, 0.f}, v1 = v0;
#pragma unroll
        for (int q = 0; q < 4; ++q) { v0 += *(LAS f32x4*)(L + (q * 64 + mm) * RP + cg * 32); v1 += *(LAS f32x4*)(L + (q * 64 + mm) * RP + cg * 32 + 16); }
        const size_t o = (size_t)panel_row(r3, 64 * h3 + mm) * 512 + 64 * cc + 8 * cg;
        {
            const u32x4 ov = u32x4{pk_bf16(bf_lo(xres.x) + v0[0], bf_hi(xres.x) + v0[1]), pk_bf16(bf_lo(xres.y) + v0[2], bf_hi(xres.y) + v0[3]),
                                   pk_bf16(bf_lo(xres.z) + v1[0], bf_hi(xres.z) + v1[1]), pk_bf16(bf_lo(xres.w) + v1[2], bf_hi(xres.w) + v1[3])};
            __builtin_nontemporal_store(ov, (GAS u32x4*)(p.out + o));
        }
    }
    STAMP(11);
}

extern "C" void kernel_launch(void* const* d_in, const int* in_sizes, int n_in, void* d_out, int out_size, void* d_ws, size_t ws_size,
                              hipStream_t stream) {
    Params p;
    p.x = (const uint16_t*)d_in[0]; p.wr = (const uint16_t*)d_in[1]; p.wi = (const uint16_t*)d_in[2];
    p.wg = (const uint16_t*)d_in[4]; p.wu = (const uint16_t*)d_in[5]; p.wd = (const uint16_t*)d_in[6];
    p.out = (uint16_t*)d_out; p.ws = (unsigned char*)d_ws;
    (void)hipFuncSetAttribute((const void*)k_fused, hipFuncAttributeMaxDynamicSharedMemorySize, LDS_BYTES);
    k_fused<<<256, NT, LDS_BYTES, stream>>>(p);
}
```

```cpp
#define STAMPS
#define TLIDX 11
#define TLIM 33
#define OUT_NT
#include <hip/hip_runtime.h>
#include <stdint.h>

#define LAS __attribute__((address_space(3)))
#define GAS __attribute__((address_space(1)))
#define DI __device__ __forceinline__
typedef __bf16 bf8 __attribute__((ext_vector_type(8)));
typedef __bf16 bf2 __attribute__((ext_vector_type(2)));
typedef short s16x4 __attribute__((ext_vector_type(4)));
typedef short s16x8 __attribute__((ext_vector_type(8)));
typedef float f32x4 __attribute__((ext_vector_type(4)));
typedef float f32x2 __attribute__((ext_vector_type(2)));
typedef unsigned u32x4 __attribute__((ext_vector_type(4)));
typedef unsigned u32x2 __attribute__((ext_vector_type(2)));
typedef GAS unsigned gu32;
typedef GAS unsigned long long gu64;
#define RLX_AGENT __ATOMIC_RELAXED, __HIP_MEMORY_SCOPE_AGENT

namespace tabs {
constexpr double PI = 3.14159265358979323846264338327950288;
constexpr double cos_q(double x) {
    double x2 = x * x, t = 1.0, s = 1.0;
    for (int i = 1; i <= 14; ++i) { t *= -x2 / ((2 * i - 1) * (2 * i)); s += t; }
    return s;
}
constexpr uint16_t f2bf(float f) {
    uint32_t u = __builtin_bit_cast(uint32_t, f);
    u += 0x7FFFu + ((u >> 16) & 1u);
    return (uint16_t)(u >> 16);
}
struct Base { uint16_t v[512];
    constexpr Base() : v{} {
        for (int m = 0; m <= 128; ++m) {
            double c = (m == 128) ? 0.0 : cos_q(2.0 * PI * m / 512.0);
            uint16_t b = f2bf((float)c);
            uint16_t nb = (m == 128) ? b : (uint16_t)(b ^ 0x8000u);
            v[m] = b; v[(512 - m) & 511] = b;
            v[256 - m] = nb; v[(256 + m) & 511] = nb;
        }
    }
};
constexpr Base base{};
constexpr uint16_t cosb(int m) { return base.v[m & 511]; }
constexpr uint16_t nsinb(int m) { return base.v[(m + 128) & 511]; }
constexpr uint16_t sinb(int m) { return base.v[(m + 384) & 511]; }
struct Chunk { uint16_t v[16384]; };
constexpr Chunk mk_fwd(int c) {
    Chunk r{};
    for (int d = 0; d < 512; ++d)
        for (int n = 0; n < 32; ++n) { const int k = 16 * c + (n & 15);
            r.v[d * 32 + n] = base.v[((n >> 4) ? (k == 0 ? d * 256 : d * k + 128) : d * k) & 511]; }
    return r;
}
constexpr Chunk mk_inv(int blk) {
    Chunk r{};
    const int cc = blk >> 1;
    for (int e = 0; e < 16384; e += 8) {
        const int l = (e >> 3) & 63, dt = (e >> 9) & 3, kg = (e >> 11) & 3, s = ((blk & 1) << 1) + (e >> 13);
        const int kt = 4 * kg + s, d = 64 * cc + 16 * dt + (l & 15), k0 = 16 * kt + 4 * (l >> 4);
        for (int j = 0; j < 4; ++j) { const int k = k0 + j;
            r.v[e + j] = base.v[(k * d) & 511];
            r.v[e + 4 + j] = base.v[(k == 0 ? d * 256 : k * d + 128) & 511]; }
    }
    return r;
}
template <int I> constexpr Chunk fwd_c = mk_fwd(I);
template <int I> constexpr Chunk inv_c = mk_inv(I);
struct Big { Chunk c[16]; };
}
#define C16(N) {{tabs::N<0>, tabs::N<1>, tabs::N<2>, tabs::N<3>, tabs::N<4>, tabs::N<5>, tabs::N<6>, tabs::N<7>, \
                 tabs::N<8>, tabs::N<9>, tabs::N<10>, tabs::N<11>, tabs::N<12>, tabs::N<13>, tabs::N<14>, tabs::N<15>}}
alignas(256) static __device__ const tabs::Big g_fwd = C16(fwd_c);
alignas(256) static __device__ const tabs::Big g_inv = C16(inv_c);

constexpr int S = 2048, D = 512, F = 1792, DF = 257, NT = 512;
constexpr size_t WS_CTRL = 0;
constexpr size_t WS_STAT = 98304;
constexpr size_t WS_AP = 131072;
constexpr size_t WS_AX = WS_AP + (2u << 20);
constexpr size_t WS_TX = WS_AX + 8192;
constexpr size_t WS_T = WS_AX + 65536;
constexpr size_t WS_H = WS_T + (2u << 20);
constexpr size_t WS_H2 = WS_H + (size_t)16 * 8 * 48 * 1024;
constexpr size_t WS_END = WS_H2 + (size_t)16 * 8 * 16 * 512;
#define FSTR 32
#define PSLEEP 8
constexpr int W_CT = 1024, W_TMO = 2048, W_FA = 4096, W_FH = W_FA + 256 * 32;
constexpr unsigned SPIN_CAP = 1u << 16;
constexpr int CODE_PF_BYTES = 39 * 1024;

struct Params {
    const uint16_t* x; const uint16_t* wr; const uint16_t* wi;
    const uint16_t* wg; const uint16_t* wu; const uint16_t* wd;
    uint16_t* out; unsigned char* ws;
};

template <int I> struct IC { static constexpr int v = I; };
template <int B, int E, class Fn> DI void static_for(Fn&& f) { if constexpr (B < E) { f(IC<B>{}); static_for<B + 1, E>(f); } }
DI f32x4 mfma(u32x4 a, u32x4 b, f32x4 c) {
    return __builtin_amdgcn_mfma_f32_16x16x32_bf16(__builtin_bit_cast(bf8, a), __builtin_bit_cast(bf8, b), c, 0, 0, 0);
}
DI unsigned pk_bf16(float lo, float hi) { bf2 p; p[0] = (__bf16)lo; p[1] = (__bf16)hi; return __builtin_bit_cast(unsigned, p); }
DI float bf_lo(unsigned u) { return __builtin_bit_cast(float, u << 16); }
DI float bf_hi(unsigned u) { return __builtin_bit_cast(float, u & 0xffff0000u); }
DI float bfr(float x) { return bf_lo(pk_bf16(x, x)); }
DI u32x4 lds128(LAS unsigned char* L, int off) { return *(LAS u32x4*)(L + off); }
DI u32x2 lds_tr(LAS unsigned char* L, int off) {
    return __builtin_bit_cast(u32x2, __builtin_amdgcn_ds_read_tr16_b64_v4i16((LAS s16x4*)(L + off)));
}
DI u32x4 lds_tr2(LAS unsigned char* L, int off0, int off1) {
    const u32x2 a = lds_tr(L, off0), b = lds_tr(L, off1);
    return u32x4{a.x, a.y, b.x, b.y};
}
DI u32x4 swap_neg(u32x4 v) { return u32x4{v.z, v.w, v.x ^ 0x80008000u, v.y ^ 0x80008000u}; }
DI u32x4 pack8(const float (&a)[4], const float (&b)[4]) { return u32x4{pk_bf16(a[0], a[1]), pk_bf16(a[2], a[3]), pk_bf16(b[0], b[1]), pk_bf16(b[2], b[3])}; }
DI u32x4 pack8(f32x4 a, f32x4 b) { return u32x4{pk_bf16(a[0], a[1]), pk_bf16(a[2], a[3]), pk_bf16(b[0], b[1]), pk_bf16(b[2], b[3])}; }
DI void glds16(const void* gsrc, unsigned lds_addr) {
    asm volatile("s_mov_b32 m0, %1\n\ts_nop 0\n\tglobal_load_lds_dwordx4 %0, off" :: "v"(gsrc), "s"(lds_addr) : "memory");
}
DI void glds16s(const void* sbase, unsigned voff, unsigned lds_addr) {
    asm volatile("s_mov_b32 m0, %2\n\ts_nop 0\n\tglobal_load_lds_dwordx4 %0, %1" :: "v"(voff), "s"(sbase), "s"(lds_addr) : "memory");
}
DI void glds4(const void* gsrc, unsigned lds_addr) {
    asm volatile("s_mov_b32 m0, %1\n\ts_nop 0\n\tglobal_load_lds_dword %0, off" :: "v"(gsrc), "s"(lds_addr) : "memory");
}
DI void gl128(u32x4& d, const void* p) { asm volatile("global_load_dwordx4 %0, %1, off" : "=v"(d) : "v"(p) : "memory"); }
DI void gl128_sc1(u32x4& d, const void* base, unsigned off) { asm volatile("global_load_dwordx4 %0, %1, %2 sc1" : "=v"(d) : "v"(off), "s"(base) : "memory"); }
DI void gl64_sc1(u32x2& d, const void* base, unsigned off) { asm volatile("global_load_dwordx2 %0, %1, %2 sc1" : "=v"(d) : "v"(off), "s"(base) : "memory"); }
DI void gl32_sc1(unsigned& d, const void* base, unsigned off) { asm volatile("global_load_dword %0, %1, %2 sc1" : "=v"(d) : "v"(off), "s"(base) : "memory"); }
DI void gl16(unsigned& d, const void* base, unsigned off) { asm volatile("global_load_ushort %0, %1, %2" : "=v"(d) : "v"(off), "s"(base) : "memory"); }
template <int N> DI void wait_vm() { static_assert(N >= 0 && N < 64, "vmcnt"); asm volatile("s_waitcnt vmcnt(%0)" :: "n"(N) : "memory"); }
template <class T> DI void pin(T& v) { asm volatile("" : "+v"(v)); }
#define WAIT_LGKM0() asm volatile("s_waitcnt lgkmcnt(0)" ::: "memory")
#define BAR() do { asm volatile("" ::: "memory"); __builtin_amdgcn_s_barrier(); asm volatile("" ::: "memory"); } while (0)
#define SB() __builtin_amdgcn_sched_barrier(0)
DI void st_sc1(__amdgpu_buffer_rsrc_t rs, int off, u32x4 v) { __builtin_amdgcn_raw_buffer_store_b128(v, rs, off, 0, 16); }
DI void st_sc1_64(__amdgpu_buffer_rsrc_t rs, int off, u32x2 v) { __builtin_amdgcn_raw_buffer_store_b64(v, rs, off, 0, 16); }
template <int CTRL> DI float dpp_add(float v) {
    return v + __builtin_bit_cast(float, __builtin_amdgcn_update_dpp(0, __builtin_bit_cast(int, v), CTRL, 0xf, 0xf, true));
}
DI float wave_sum(float v) {
    v = dpp_add<0xB1>(v);
    v = dpp_add<0x4E>(v);
    v = dpp_add<0x141>(v);
    v = dpp_add<0x140>(v);
    const int vi = __builtin_bit_cast(int, v);
    return (__builtin_bit_cast(float, __builtin_amdgcn_readlane(vi, 0)) + __builtin_bit_cast(float, __builtin_amdgcn_readlane(vi, 16))) +
           (__builtin_bit_cast(float, __builtin_amdgcn_readlane(vi, 32)) + __builtin_bit_cast(float, __builtin_amdgcn_readlane(vi, 48)));
}
DI void give_up(const unsigned char* ws, int lane) { if (lane == 0) __hip_atomic_store((gu32*)ws + W_TMO, 1u, RLX_AGENT); }
DI void poll16(const unsigned char* ws, int word, unsigned tag, int lane) {
#pragma unroll 1
    for (unsigned spins = 0;; ++spins) {
        unsigned f; gl32_sc1(f, ws, (unsigned)(word + (lane & 15) * FSTR) * 4u); wait_vm<0>(); pin(f);
        if (__all(f == tag)) break;
        if (spins > SPIN_CAP) { give_up(ws, lane); break; }
        __builtin_amdgcn_s_sleep(PSLEEP);
    }
}
DI void fx_load(unsigned (&w)[8], unsigned o0, unsigned o1, unsigned o2, unsigned o3, const void* wr, const void* wi, int k4) {
    asm volatile("s_cmp_lg_u32 %14, 0\n\ts_cbranch_scc1 .Lnofx%=\n\t"
                 "global_load_ushort %0, %8, %12\n\tglobal_load_ushort %1, %9, %12\n\tglobal_load_ushort %2, %10, %12\n\tglobal_load_ushort %3, %11, %12\n\t"
                 "global_load_ushort %4, %8, %13\n\tglobal_load_ushort %5, %9, %13\n\tglobal_load_ushort %6, %10, %13\n\tglobal_load_ushort %7, %11, %13\n"
                 ".Lnofx%=:" : "=&v"(w[0]), "=&v"(w[1]), "=&v"(w[2]), "=&v"(w[3]), "=&v"(w[4]), "=&v"(w[5]), "=&v"(w[6]), "=&v"(w[7])
                 : "v"(o0), "v"(o1), "v"(o2), "v"(o3), "s"(wr), "s"(wi), "s"(k4) : "memory", "scc");
}
DI float cosr(float x) { return __builtin_amdgcn_cosf(x); }
DI float sinr(float x) { return __builtin_amdgcn_sinf(x); }
DI u32x4 frag_cs(int a, int b0, int n) {
    float c[4], sn[4];
#pragma unroll
    for (int j = 0; j < 4; ++j) { const float th = (float)((a * (b0 + j)) & (n - 1)) * (1.0f / n); c[j] = cosr(th); sn[j] = -sinr(th); }
    return pack8(c, sn);
}

constexpr size_t WS_STAMP = 128u << 20;
#define STAMP(i) do { asm volatile("s_memrealtime %0\n\ts_waitcnt lgkmcnt(0)" : "=s"(stampv[i]) :: "memory"); } while (0)
#define TS(i) do { unsigned long long t_; asm volatile("s_memtime %0\n\ts_waitcnt lgkmcnt(0)" : "=s"(t_) :: "memory"); tsv[i] = (unsigned)t_; } while (0)

constexpr int P1_STAGE = 49152, P1_BOFF = 16384;
constexpr int L_MISC = 147456;
constexpr int L_XI = L_MISC;
constexpr int L_ST = L_MISC + 8704;
constexpr int L_YX = L_ST + 1024;
constexpr int L_SCR = L_YX + 4096;
constexpr int L_FLG = L_SCR + 256;
constexpr int L_END = L_FLG + 16;
static_assert(L_END <= 163840, "lds");
constexpr int LDS_BYTES = 163840;
constexpr int P3_SLOT = 16384;
DI constexpr int p3_slot(int s) { return (s % 9) * P3_SLOT; }

DI int panel_row(int r, int m) { return 64 * (m & 31) + 4 * r + (m >> 5); }
DI int p3_ks(int s, int kg) { const int n = 4 * s + kg; if (s >= 12) return 7 * (n - 48) + 3; const int p = n / 6, o = n - 6 * p; return 7 * p + o + (o >= 3); }

struct LdSrc { unsigned a[4]; const unsigned char* b[8]; long sB64; const unsigned char* t; long tadv; };
DI void p1_issue(LdSrc& s, const unsigned char* x, unsigned Lb, int lw, int j) {
    const unsigned sb = Lb + (j % 3) * P1_STAGE;
#pragma unroll
    for (int i = 0; i < 4; ++i) glds16s(x + j * 128, s.a[i], sb + (4 * lw + i) * 1024);
#pragma unroll
    for (int u = 0; u < 8; ++u) { const int i = (lw & 1) + 2 * (u & 1) + 4 * (lw >> 1) + 8 * ((u >> 1) & 1) + 16 * (u >> 2);
        glds16(s.b[u], sb + P1_BOFF + i * 1024); s.b[u] += s.sB64; }
}
DI void p1_touch(LdSrc& s, unsigned Lb) { glds4(s.t, Lb + L_SCR); s.t += s.tadv; }
DI void p3_issue(const Params& p, unsigned Lb, int lw, int cc, int s, unsigned wv, unsigned fv) {
    const unsigned sb = Lb + p3_slot(s) + lw * 4096;
    if (s < 14) { const unsigned char* base = (const unsigned char*)p.wd + (size_t)p3_ks(s, lw) * 32768;
#pragma unroll
        for (int i = 0; i < 4; ++i) glds16s(base + i * 8192, wv, sb + i * 1024); }
    else { const unsigned char* base = (const unsigned char*)&g_inv + cc * 65536 + (s - 14) * 16384 + lw * 4096;
#pragma unroll
        for (int i = 0; i < 4; ++i) glds16s(base + i * 1024, fv, sb + i * 1024); }
}

DI void p3_issue2(const Params& p, unsigned Lb, int w, int cc, int s, unsigned wv, unsigned fv) {
    const unsigned sb = Lb + p3_slot(s) + w * 2048;
    if (s < 14) { const unsigned char* base = (const unsigned char*)p.wd + (size_t)p3_ks(s, w >> 1) * 32768 + (w & 1) * 16384;
        glds16s(base, wv, sb); glds16s(base + 8192, wv, sb + 1024); }
    else { const unsigned char* base = (const unsigned char*)&g_inv + cc * 65536 + (s - 14) * 16384 + w * 2048;
        glds16s(base, fv, sb); glds16s(base + 1024, fv, sb + 1024); }
}

template <bool DFTW>
DI void p1_compute(LAS unsigned char* L, unsigned Lb, int wc, int lane, f32x4 (&acc)[4][8], f32x4 (&csm)[4], unsigned (&tsv)[32]) {
    asm volatile("" : "+v"(lane));
    const int fr = lane & 15, g = lane >> 4;
    const int qq = fr >> 2, p = lane & 3;
    int aoff0 = fr * 128 + (((0 + g) ^ (fr & 7)) << 4);
    int aoff1 = fr * 128 + (((4 + g) ^ (fr & 7)) << 4);
    const int sw = 2 * qq + 8 * (g & 1);
    int boff[4];
#pragma unroll
    for (int q = 0; q < 4; ++q) boff[q] = P1_BOFF + (8 * g + qq) * 512 + (((8 * wc + 2 * q + (p >> 1)) ^ sw) << 4) + 8 * (p & 1);
    u32x4 xa[2][4], wb[2][4];
    auto rdx = [&](int ks, int mh, int i) { xa[mh][i] = lds128(L, (ks ? aoff1 : aoff0) + (4 * mh + i) * 2048); };
    auto rdw = [&](int ks, int q) { const int o = boff[q] + ks * 16384; wb[ks][q] = lds_tr2(L, o, o + 2048); };
    auto rebase = [&](int d) { aoff0 += d; aoff1 += d; boff[0] += d; boff[1] += d; boff[2] += d; boff[3] += d;
        asm volatile("" : "+v"(aoff0), "+v"(aoff1), "+v"(boff[0]), "+v"(boff[1]), "+v"(boff[2]), "+v"(boff[3])); };
    auto step = [&](int ks, int mh, bool more) {
#pragma unroll
        for (int i = 0; i < 16; ++i) {
            const int q = i >> 2, mt = i & 3;
            if (DFTW && q >= 2) acc[q][4 * mh + mt] = mfma(xa[mh][mt], wb[ks][q], acc[q][4 * mh + mt]);
            else acc[q][4 * mh + mt] = mfma(wb[ks][q], xa[mh][mt], acc[q][4 * mh + mt]);
            if (more) {
                if (mh == 0) { if (i >= 1 && i <= 4) rdx(ks, 1, i - 1); }
                else { if (i < 4) rdx(ks ^ 1, 0, i); else if (i < 8) rdw(ks ^ 1, i - 4); }
            }
            if (mh == 0 && (i & 3) == 3) { const int d = i >> 2; if (!DFTW || d < 2) csm[d] = mfma(wb[ks][d], u32x4{0x3F803F80u, 0x3F803F80u, 0x3F803F80u, 0x3F803F80u}, csm[d]); }
            SB();
        }
    };
    BAR();
    TS(0);
#pragma unroll
    for (int i = 0; i < 4; ++i) { rdx(0, 0, i); rdw(0, i); }
    SB();
    static_for<0, 8>([&](auto J) {
        constexpr int j = J.v;
        step(0, 0, true);
        step(0, 1, true);
        step(1, 0, true);
        if constexpr (j < 7) { WAIT_LGKM0(); BAR(); TS(j + 1); rebase((j + 1) % 3 == 0 ? -2 * P1_STAGE : P1_STAGE);
            SB(); }
        step(1, 1, j < 7);
    });
    TS(8);
}

constexpr int HD = 5;
constexpr int FTS = 6;
enum { K_OP, K_TST, K_W, K_FT };
constexpr int p3_after(int kind, int idx, int s_now) {
    int n = -1;
    auto hit = [&](bool is, int cnt) { if (is) n = 0; else if (n >= 0) n += cnt; };
    for (int o = 0; o < HD; ++o) hit(kind == K_OP && idx == o, 2);
    for (int w = 3; w < 9; ++w) hit(kind == K_W && idx == w, 2);
    hit(kind == K_TST, 1); hit(false, 1);
    for (int s = 0; s < s_now; ++s) {
        if (s + HD < 18) hit(kind == K_OP && idx == s + HD, (s + HD >= 12 && s + HD <= 14) ? 4 : 2);
        if (s >= 3 && s + 6 < 18) hit(kind == K_W && idx == s + 6, 2);
        if (s == FTS) hit(kind == K_FT, 1);
    }
    return n;
}
constexpr int cmin(int a, int b) { return a < b ? a : b; }
constexpr int p3_wait(int s) {
    int n = p3_after(K_OP, s, s);
    if (s == 2) n = cmin(n, p3_after(K_TST, 0, s));
    if (s % 3 == 0 && s >= 3) n = cmin(n, p3_after(K_W, s + 2, s));
    if (s == 14 - HD) n = cmin(n, p3_after(K_FT, 0, s));
    return n;
}
static_assert(p3_wait(0) == 2 * (HD - 1) + 12 + 2 && p3_wait(2) == 1 + 4, "queue model");

struct P3Ctx { unsigned hbase, h2base, tbase; int woff[4]; };
DI void p3_rdw(LAS unsigned char* L, const P3Ctx& c, int kg, int lane, int s, u32x4 (&wf)[4]) {
#pragma unroll
    for (int dt = 0; dt < 4; ++dt) {
        if (s < 14) wf[dt] = lds_tr2(L, p3_slot(s) + c.woff[dt], p3_slot(s) + c.woff[dt] + 2048);
        else wf[dt] = lds128(L, p3_slot(s) + (kg * 4 + dt) * 1024 + lane * 16);
    }
}

__global__ void __launch_bounds__(NT, 2) k_fused(Params p) {
    extern __shared__ __attribute__((aligned(16))) unsigned char smem[];
    LAS unsigned char* L = (LAS unsigned char*)smem;
    const unsigned Lb = (unsigned)(size_t)L;
    const int wid = __builtin_amdgcn_readfirstlane(threadIdx.x >> 6);
    const int wc = wid & 3;
    const int b = blockIdx.x, xcd = b & 7, bi = b >> 3;
    const int r = (xcd & 1) * 8 + (bi & 7), c = (xcd >> 1) * 4 + (bi >> 3);
    const int jq = b & 7, kq = b >> 3;
    const int cc = (xcd & 1) * 4 + (bi & 3), hp = (xcd >> 1) * 8 + (bi >> 2), r3 = hp >> 1, h3 = hp & 1;
    const unsigned char* ws = p.ws;
    unsigned long long stampv[16];
#pragma unroll
    for (int i = 0; i < 16; ++i) stampv[i] = 0;
    unsigned tsv[32];
#pragma unroll
    for (int i = 0; i < 32; ++i) tsv[i] = 0;
    STAMP(0);
    TS(31);
    const int odd = c & 1;
    const int u2 = wid >> 2, w2 = wid & 3, k4 = 2 * kq + u2;
    LAS f32x2* ST = (LAS f32x2*)(L + L_ST);
    f32x4 acc[4][8], csm[4];
#pragma unroll
    for (int i = 0; i < 4; ++i) csm[i] = f32x4{0.f, 0.f, 0.f, 0.f};
    unsigned tag;
    {
    const int tid = threadIdx.x, lane = tid & 63;
    if (wid >= 4) {
        const int lw = wid - 4;
        unsigned epoch; gl32_sc1(epoch, ws, 0u);
        LdSrc ls;
        {
            const int idx = lw * 22 + (lane < 22 ? lane : 0);
            if (idx < 32) { ls.t = (const unsigned char*)p.x + (size_t)panel_row(r, 32 * (c & 3) + idx) * 1024; ls.tadv = 128; }
            else { const int i = idx - 32, row = 8 * (r & 7) + i / 7, it = i % 7;
                if (it == 6) { ls.t = (const unsigned char*)&g_fwd + c * 32768 + row * 64; ls.tadv = 64 * 64; }
                else { const int it3 = it % 3; ls.t = (const unsigned char*)(it < 3 ? p.wg : p.wu) + (size_t)row * (F * 2) + c * 224 + (it3 == 2 ? 220 : it3 * 128); ls.tadv = 64L * F * 2; } }
        }
        p1_touch(ls, Lb);
        SB();
#pragma unroll
        for (int i = 0; i < 4; ++i) { const int m = 8 * (4 * lw + i) + (lane >> 3); ls.a[i] = (unsigned)panel_row(r, m) * 1024u + (((lane & 7) ^ (lane >> 3)) << 4); }
        {
            const int b0 = lw & 1, b2 = lw >> 1;
            const int swz = 2 * (2 * b0 + (lane >> 5)) + 8 * b2;
            const int ch = (lane & 31) ^ swz, nt = ch >> 1, half = ch & 1, cw = nt >> 2, q = nt & 3;
            const unsigned char* base; int col, strideB;
            if (cw < 3) { const int ft = odd + 2 * cw + (q & 1); base = (const unsigned char*)(q < 2 ? p.wg : p.wu); col = 112 * c + 16 * ft + 8 * half; strideB = F * 2; }
            else if (q < 2) { const int ft = odd ? 0 : 6; base = (const unsigned char*)(q == 0 ? p.wg : p.wu); col = 112 * c + 16 * ft + 8 * half; strideB = F * 2; }
            else { base = (const unsigned char*)&g_fwd + c * 32768; col = 16 * (q - 2) + 8 * half; strideB = 64; }
            const unsigned char* b00 = base + (size_t)(2 * b0 + 8 * b2 + (lane >> 5)) * strideB + col * 2;
#pragma unroll
            for (int u = 0; u < 8; ++u) ls.b[u] = b00 + (size_t)(4 * (u & 1) + 16 * ((u >> 1) & 1) + 32 * (u >> 2)) * strideB;
            ls.sB64 = (long)strideB * 64;
        }
        p1_issue(ls, (const unsigned char*)p.x, Lb, lw, 0);
        p1_touch(ls, Lb); p1_touch(ls, Lb); p1_touch(ls, Lb);
        p1_issue(ls, (const unsigned char*)p.x, Lb, lw, 1);
        STAMP(14);
        if (tid == 256) { *(volatile LAS unsigned*)(L + L_FLG + 4) = 0u; *(volatile LAS unsigned*)(L + L_FLG + 8) = 0u; *(volatile LAS unsigned*)(L + L_FLG + 12) = 0u; }
        wait_vm<15>(); pin(epoch); tag = epoch + 1u;
        if (r == 0 && tid == 256) __hip_atomic_store((gu32*)p.ws + W_CT + c * 32, tag << 8, RLX_AGENT);
        WAIT_LGKM0();
        STAMP(15);
        BAR();
        p1_touch(ls, Lb);
        p1_issue(ls, (const unsigned char*)p.x, Lb, lw, 2);
        SB();
        const unsigned char *st0, *st1;
        {
            auto wline = [&](int i) { const int st = i >> 2, ln = (bi >> 2) * 16 + lw * 4 + (i & 3);
                return st < 14 ? (const unsigned char*)p.wd + (size_t)(32 * p3_ks(st, ln >> 5) + (ln & 31)) * 1024 + cc * 128
                               : (const unsigned char*)&g_inv + (size_t)cc * 65536 + (st - 14) * 16384 + ln * 128; };
            st0 = wline(lane);
            const int fi = (bi * 4 + lw) * 20 + (lane >= 8 && lane < 28 ? lane - 8 : 0);
            const int arr = fi / 1280, rr = (fi % 1280) / 5, ll = (fi % 1280) % 5;
            const int jrow = 4 * jq + (rr & 3) + 32 * (rr >> 2);
            st1 = lane < 8 ? wline(64 + lane) : (const unsigned char*)(arr ? p.wi : p.wr) + (size_t)jrow * (DF * 2) + min(ll * 128, 510);
        }
        u32x2 gran; const unsigned goff = (unsigned)WS_STAT + (unsigned)(r * 128 + (tid & 127)) * 8u;
        static_for<0, 7>([&](auto J) {
            constexpr int j = J.v;
            if constexpr (j < 4) wait_vm<13>(); else if constexpr (j == 4) wait_vm<12>();
            else if constexpr (j == 5) wait_vm<15>(); else wait_vm<3>();
            BAR();
            if constexpr (j < 3) p1_touch(ls, Lb);
            if constexpr (j + 3 < 8) p1_issue(ls, (const unsigned char*)p.x, Lb, lw, j + 3);
            if constexpr (j == 4) {
                gl64_sc1(gran, ws, goff);
                glds4(st0, Lb + L_SCR); glds4(st1, Lb + L_SCR);
            }
        });
        wait_vm<2>();
        pin(gran);
        if (lw < 2) {
            if (!__all(gran.y == tag)) {
#pragma unroll 1
                for (unsigned spins = 0;; ++spins) {
                    gl64_sc1(gran, ws, goff); wait_vm<0>(); pin(gran);
                    if (__all(gran.y == tag)) break;
                    if (spins > SPIN_CAP) { give_up(ws, lane); break; }
                    __builtin_amdgcn_s_sleep(1);
                }
            }
            ST[tid & 127] = f32x2{bf_hi(gran.x), 1.0f / bf_lo(gran.x)};
        }
#pragma unroll
        for (int q = 0; q < 4; ++q)
#pragma unroll
            for (int mt = 0; mt < 8; ++mt) acc[q][mt] = f32x4{0.f, 0.f, 0.f, 0.f};
    } else {
        if (wid == 0) {
            const unsigned char* pc = (const unsigned char*)__builtin_amdgcn_s_getpc();
            const int ln = bi * 10 + (lane < 10 ? lane : 0);
            glds4(pc - ((size_t)pc & 127) + (size_t)min(ln * 128, CODE_PF_BYTES), Lb + L_SCR);
        }
        unsigned epoch; gl32_sc1(epoch, ws, 0u);
        u32x4 xrow[2];
#pragma unroll
        for (int i = 0; i < 2; ++i) gl128(xrow[i], (const unsigned char*)p.x + (size_t)panel_row(r, 8 * c + 2 * wid + i) * 1024 + lane * 16);
        wait_vm<0>(); pin(epoch); pin(xrow[0]); pin(xrow[1]);
        tag = epoch + 1u;
#pragma unroll
        for (int i = 0; i < 2; ++i) {
            float v[8];
#pragma unroll
            for (int e = 0; e < 4; ++e) { v[2 * e] = bf_lo(xrow[i][e]); v[2 * e + 1] = bf_hi(xrow[i][e]); }
            float s = ((v[0] + v[1]) + (v[2] + v[3])) + ((v[4] + v[5]) + (v[6] + v[7]));
            s = wave_sum(s);
            const float mean = s * (1.0f / 512.0f);
            float q2 = 0.f;
#pragma unroll
            for (int e = 0; e < 8; ++e) { const float d = v[e] - mean; q2 += d * d; }
            q2 = wave_sum(q2);
            const float var = q2 * (1.0f / 512.0f);
            const float m_bf = bfr(mean), v_bf = bfr(var), s_bf = bfr(sqrtf(bfr(v_bf + 1e-5f)));
            const unsigned val = (__builtin_bit_cast(unsigned, m_bf) & 0xffff0000u) | (__builtin_bit_cast(unsigned, s_bf) >> 16);
            if (lane == 0) __hip_atomic_store((gu64*)(p.ws + WS_STAT) + (r * 128 + 8 * c + 2 * wid + i), ((unsigned long long)tag << 32) | val, RLX_AGENT);
        }
#pragma unroll
        for (int q = 0; q < 4; ++q)
#pragma unroll
            for (int mt = 0; mt < 8; ++mt) acc[q][mt] = f32x4{0.f, 0.f, 0.f, 0.f};
        STAMP(1);
        if (wid < 3) p1_compute<false>(L, Lb, wc, lane, acc, csm, tsv); else p1_compute<true>(L, Lb, wc, lane, acc, csm, tsv);
        STAMP(2);
    }
    }
    int tz = threadIdx.x; asm volatile("" : "+v"(tz));
    const int tid = tz, lane = tz & 63, fr = lane & 15, g = lane >> 4;
    const __amdgpu_buffer_rsrc_t rs = __builtin_amdgcn_make_buffer_rsrc((void*)p.ws, 0, (int)WS_END, 0x00020000);
    const unsigned wv = (unsigned)(lane >> 3) * 1024u + cc * 128u + (unsigned)(((lane & 7) ^ (2 * ((lane >> 4) & 3))) << 4), fv = lane * 16u;
    volatile LAS unsigned* flg = (volatile LAS unsigned*)(L + L_FLG);
    WAIT_LGKM0(); BAR();
    STAMP(3);
    const int fj1 = 4 * jq + (fr & 3), fk = 4 * k4 + (fr >> 2);
    unsigned fw[4], fwi[4];
    auto signal = [&](int cnt, int word) {
        if (lane == 0) { const unsigned old = __hip_atomic_fetch_add((LAS unsigned*)(L + L_FLG) + cnt, 1u, __ATOMIC_RELAXED, __HIP_MEMORY_SCOPE_WORKGROUP);
            if (old == 3u) __hip_atomic_store((gu32*)p.ws + word, tag, RLX_AGENT); }
    };
    if (wid < 3) {
        const f32x4 cg0 = csm[0], cg1 = csm[1], cu0 = csm[2], cu1 = csm[3];
#pragma unroll
        for (int mt = 0; mt < 8; ++mt) {
            const f32x2 st = ST[16 * mt + fr];
            const float r2 = st.y * st.y;
            const f32x4 h0 = (acc[0][mt] - st.x * cg0) * (acc[2][mt] - st.x * cu0) * r2;
            const f32x4 h1 = (acc[1][mt] - st.x * cg1) * (acc[3][mt] - st.x * cu1) * r2;
            st_sc1(rs, (int)WS_H + ((((r * 8 + mt) * 48 + 3 * c + wc) * 64 + lane) << 4), pack8(h0, h1));
        }
    } else if (wid == 3) {
        const float csf = (c == 0 && fr == 0) ? 512.0f : 0.0f;
#pragma unroll
        for (int mt = 0; mt < 8; ++mt) {
            const int m0 = 16 * mt + 4 * g;
            const f32x4 s01 = *(LAS f32x4*)(ST + m0), s23 = *(LAS f32x4*)(ST + m0 + 2);
            const int s2l = mt >> 1, s1 = 16 * (mt & 1) + 4 * g;
            *(LAS u32x2*)(L + L_XI + fr * 272 + s2l * 64 + s1 * 2) =
                u32x2{pk_bf16((acc[2][mt][0] - s01[0] * csf) * s01[1], (acc[2][mt][1] - s01[2] * csf) * s01[3]),
                      pk_bf16((acc[2][mt][2] - s23[0] * csf) * s23[1], (acc[2][mt][3] - s23[2] * csf) * s23[3])};
            *(LAS u32x2*)(L + L_XI + (16 + fr) * 272 + s2l * 64 + s1 * 2) =
                u32x2{pk_bf16(acc[3][mt][0] * s01[1], acc[3][mt][1] * s01[3]), pk_bf16(acc[3][mt][2] * s23[1], acc[3][mt][3] * s23[3])};
        }
        WAIT_LGKM0();
        flg[0] = tag;
        const f32x4 cg0 = csm[0], cu0 = csm[1];
#pragma unroll
        for (int mt = 0; mt < 8; ++mt) {
            const f32x2 st = ST[16 * mt + fr];
            const float r2 = st.y * st.y;
            const f32x4 h0 = (acc[0][mt] - st.x * cg0) * (acc[1][mt] - st.x * cu0) * r2;
            st_sc1_64(rs, (int)WS_H2 + ((((r * 8 + mt) * 16 + c) * 64 + lane) << 3), u32x2{pk_bf16(h0[0], h0[1]), pk_bf16(h0[2], h0[3])});
        }
    } else {
        const int mt = wid - 4;
        u32x4 eaC[2], eaS[2];
        float tc[2][4], tsn[2][4];
#pragma unroll
        for (int jt = 0; jt < 2; ++jt) {
            float cv[8], sv[8];
#pragma unroll
            for (int j = 0; j < 8; ++j) { const float th = (float)(((16 * jt + fr) * (8 * g + j)) & 31) * (1.0f / 32.0f); cv[j] = cosr(th); sv[j] = sinr(th); }
            eaC[jt] = u32x4{pk_bf16(cv[0], cv[1]), pk_bf16(cv[2], cv[3]), pk_bf16(cv[4], cv[5]), pk_bf16(cv[6], cv[7])};
            eaS[jt] = u32x4{pk_bf16(sv[0], sv[1]), pk_bf16(sv[2], sv[3]), pk_bf16(sv[4], sv[5]), pk_bf16(sv[6], sv[7])};
#pragma unroll
            for (int e = 0; e < 4; ++e) { const float th = (float)((16 * jt + fr) * (4 * r + e)) * (1.0f / 2048.0f); tc[jt][e] = cosr(th); tsn[jt][e] = sinr(th); }
        }
        SB();
#pragma unroll 1
        for (unsigned spins = 0; flg[0] != tag && spins < (1u << 20); ++spins) __builtin_amdgcn_s_sleep(1);
        asm volatile("" ::: "memory");
        const int kq_ = fr >> 2, s2l = fr & 3;
        const u32x4 are = lds128(L, L_XI + (4 * mt + kq_) * 272 + s2l * 64 + g * 16);
        const u32x4 aim = lds128(L, L_XI + (16 + 4 * mt + kq_) * 272 + s2l * 64 + g * 16);
        const f32x4 z = {0.f, 0.f, 0.f, 0.f};
        const bool special = (c == 0 && mt == 0 && g == 0);
#pragma unroll
        for (int jt = 0; jt < 2; ++jt) {
            const f32x4 Prc = mfma(are, eaC[jt], z), Pis = mfma(aim, eaS[jt], z), Prs = mfma(are, eaS[jt], z), Pic = mfma(aim, eaC[jt], z);
            const int j1 = 16 * jt + fr;
            float pr[4], qi[4], pr2[4], qi2[4];
#pragma unroll
            for (int e = 0; e < 4; ++e) {
                const float cw = tc[jt][e], sw = tsn[jt][e];
                const float ar = special ? Prc[e] : Prc[e] + Pis[e], ai = special ? -Prs[e] : Pic[e] - Prs[e];
                pr[e] = ar * cw + ai * sw; qi[e] = -(ai * cw - ar * sw);
                const float br = Pic[e], bi2 = -Pis[e];
                pr2[e] = br * cw + bi2 * sw; qi2[e] = -(bi2 * cw - br * sw);
            }
            const int U = (j1 >> 2) * 64 + 4 * c + mt, lp = 16 * (r & 3) + 4 * g + (j1 & 3);
            st_sc1(rs, (int)WS_AP + (((U * 4 + (r >> 2)) * 64 + lp) << 4), pack8(pr, qi));
            if (special) st_sc1(rs, (int)WS_AX + (((((j1 >> 2) * 4 + (r >> 2)) * 4 + (r & 3)) * 4 + (j1 & 3)) << 4), pack8(pr2, qi2));
        }
#pragma unroll
        for (int st = 0; st < 3; ++st) p3_issue(p, Lb, mt, cc, st, wv, fv);
        wait_vm<12>();
        signal(1, W_FA + (c * 16 + r) * FSTR);
    }
#pragma unroll
    for (int e = 0; e < 4; ++e) { const unsigned o = (unsigned)((fj1 + 32 * (16 * w2 + 4 * g + e)) * DF + fk) * 2u; gl16(fw[e], p.wr, o); gl16(fwi[e], p.wi, o); }
    unsigned fx[8];
    {
        const unsigned o0 = (unsigned)((fj1 + 32 * (16 * w2 + 4 * g)) * DF + 256) * 2u;
        fx_load(fx, o0, o0 + 32u * DF * 2u, o0 + 64u * DF * 2u, o0 + 96u * DF * 2u, p.wr, p.wi, k4);
    }
    if (wid < 4) { if (k4 == 0) wait_vm<16>(); else wait_vm<8>(); signal(2, W_FH + (r * 16 + c) * FSTR); }
    STAMP(5);
    {
        const int kg = wid & 3, rh = wid >> 2;
        u32x4 E1[4], E2[4];
#pragma unroll
        for (int t = 0; t < 4; ++t) E1[t] = frag_cs(16 * w2 + fr, 16 * t + 4 * g, 64);
#pragma unroll
        for (int i = 0; i < 4; ++i) E2[i] = frag_cs(16 * (i >> 1) + fr, 16 * (i & 1) + 4 * g, 32);
        float tc[4], tsn[4];
#pragma unroll
        for (int e = 0; e < 4; ++e) { const float th = (float)((4 * jq + e) * (16 * w2 + fr)) * (1.0f / 2048.0f); tc[e] = cosr(th); tsn[e] = sinr(th); }
        P3Ctx cx;
        {
            const int qq = fr >> 2, pp = lane & 3, row = 32 * kg + 4 * g + qq, sw = 2 * ((row >> 1) & 3);
#pragma unroll
            for (int dt = 0; dt < 4; ++dt) cx.woff[dt] = row * 128 + (((2 * dt + (pp >> 1)) ^ sw) << 4) + 8 * (pp & 1);
            cx.hbase = (unsigned)WS_H + (unsigned)(((r3 * 8 + 4 * h3 + 2 * rh) * 48 + kg) * 64 + lane) * 16u;
            cx.h2base = (unsigned)WS_H2 + (unsigned)(((r3 * 8 + 4 * h3 + 2 * rh) * 16 + 2 * kg) * 64 + lane) * 8u;
            cx.tbase = (unsigned)WS_T + (unsigned)((((4 * r3 + 2 * h3 + rh) * 16 + 4 * kg) * 2) * 64 + lane) * 16u;
        }
        SB();
        STAMP(6);
        if (wid == 0) poll16(ws, W_FA + (kq >> 1) * 16 * FSTR, tag, lane);
        if (wid == 1) poll16(ws, W_FH + r3 * 16 * FSTR, tag, lane);
        BAR();
        STAMP(7);
        const int U = jq * 64 + k4;
        u32x4 Bf[4], Bx[4];
        u32x4 hb[HD][2]; u32x2 hh[2][4];
#pragma unroll
        for (int t = 0; t < 4; ++t) gl128_sc1(Bf[t], ws, (unsigned)WS_AP + (unsigned)((U * 4 + t) * 64 + lane) * 16u);
        {
            const unsigned xo = (unsigned)WS_AX + (unsigned)((jq * 16 + g) * 4 + (fr & 3)) * 16u;
            asm volatile("s_cmp_lg_u32 %6, 0\n\ts_cbranch_scc1 .Lnox%=\n\t"
                         "global_load_dwordx4 %0, %4, %5 sc1\n\tglobal_load_dwordx4 %1, %4, %5 offset:256 sc1\n\t"
                         "global_load_dwordx4 %2, %4, %5 offset:512 sc1\n\tglobal_load_dwordx4 %3, %4, %5 offset:768 sc1\n"
                         ".Lnox%=:" : "=&v"(Bx[0]), "=&v"(Bx[1]), "=&v"(Bx[2]), "=&v"(Bx[3]) : "v"(xo), "s"(ws), "s"(k4) : "memory", "scc");
        }
        static_for<0, HD>([&](auto Sx) { constexpr int o = Sx.v;
            gl128_sc1(hb[o][0], ws, cx.hbase + o * 4096u); gl128_sc1(hb[o][1], ws, cx.hbase + 48u * 1024u + o * 4096u); });
#pragma unroll
        for (int st = 3; st < 9; ++st) p3_issue2(p, Lb, wid, cc, st, wv, fv);
        wait_vm<2 * HD + 12>();
        pin(Bf[0]); pin(Bf[1]); pin(Bf[2]); pin(Bf[3]); pin(Bx[0]); pin(Bx[1]); pin(Bx[2]); pin(Bx[3]);
        pin(fw[0]); pin(fw[1]); pin(fw[2]); pin(fw[3]); pin(fwi[0]); pin(fwi[1]); pin(fwi[2]); pin(fwi[3]);
        pin(fx[0]); pin(fx[1]); pin(fx[2]); pin(fx[3]); pin(fx[4]); pin(fx[5]); pin(fx[6]); pin(fx[7]);
        STAMP(8);
        {
            f32x4 a1 = {0.f, 0.f, 0.f, 0.f}, a2 = a1;
#pragma unroll
            for (int t = 0; t < 4; ++t) { a1 = mfma(E1[t], Bf[t], a1); a2 = mfma(E1[t], swap_neg(Bf[t]), a2); }
            constexpr float SC = 2.0f / (2048.0f * 512.0f);
            const float sc = (k4 == 0 && (fr >> 2) == 0) ? 0.5f * SC : SC;
            float yr[4], yi[4];
#pragma unroll
            for (int e = 0; e < 4; ++e) { const float xr = a1[e], xi = -a2[e], wre = bf_lo(fw[e]), wim = bf_lo(fwi[e]);
                yr[e] = (xr * wre - xi * wim) * sc; yi[e] = (xr * wim + xi * wre) * sc; }
            if (k4 == 0) {
                f32x4 n1 = {0.f, 0.f, 0.f, 0.f}, n2 = n1;
#pragma unroll
                for (int t = 0; t < 4; ++t) { n1 = mfma(E1[t], Bx[t], n1); n2 = mfma(E1[t], swap_neg(Bx[t]), n2); }
                float xr2[4], xi2[4];
#pragma unroll
                for (int e = 0; e < 4; ++e) { const float xr = n1[e], xi = -n2[e], wre = bf_lo(fx[e]), wim = bf_lo(fx[4 + e]);
                    xr2[e] = (xr * wre - xi * wim) * (0.5f * SC); xi2[e] = (xr * wim + xi * wre) * (0.5f * SC); }
                *(LAS u32x4*)(L + L_YX + (w2 * 64 + lane) * 16) = pack8(xr2, xi2);
            }
            *(LAS u32x4*)(L + L_XI + ((u2 * 4 + w2) * 64 + lane) * 16) = pack8(yr, yi);
        }
        WAIT_LGKM0(); BAR();
        u32x4 wf[2][4];
        p3_rdw(L, cx, kg, lane, 0, wf[0]);
        {
            f32x4 t1 = {0.f, 0.f, 0.f, 0.f}, t2 = t1;
#pragma unroll
            for (int t = 0; t < 4; ++t) { const u32x4 y = lds128(L, L_XI + ((u2 * 4 + t) * 64 + lane) * 16);
                t1 = mfma(y, E1[t], t1); t2 = mfma(swap_neg(y), E1[t], t2); }
            const int s2 = 16 * w2 + fr, ko = 4 * k4 + g;
            float tr[4], ti[4];
#pragma unroll
            for (int e = 0; e < 4; ++e) { tr[e] = t1[e] * tc[e] - t2[e] * tsn[e]; ti[e] = t2[e] * tc[e] + t1[e] * tsn[e]; }
            st_sc1(rs, (int)WS_T + ((((s2 * 16 + (ko >> 4)) * 2 + (jq >> 2)) * 64 + (jq & 3) * 16 + (ko & 15)) << 4), pack8(tr, ti));
            if (k4 == 0) {
                f32x4 x1 = {0.f, 0.f, 0.f, 0.f}, x2 = x1;
#pragma unroll
                for (int t = 0; t < 4; ++t) { const u32x4 y = lds128(L, L_YX + (t * 64 + lane) * 16);
                    x1 = mfma(y, E1[t], x1); x2 = mfma(swap_neg(y), E1[t], x2); }
#pragma unroll
                for (int e = 0; e < 4; ++e) { tr[e] = x1[e] * tc[e] - x2[e] * tsn[e]; ti[e] = x2[e] * tc[e] + x1[e] * tsn[e]; }
                if (g == 0) st_sc1(rs, (int)WS_TX + ((s2 * 8 + jq) << 4), pack8(tr, ti));
            }
        }
        u32x4 xres; gl128(xres, (const unsigned char*)p.x + ((size_t)panel_row(r3, 64 * h3 + (tid >> 3)) * 512 + 64 * cc + 8 * (tid & 7)) * 2);
        STAMP(9);
        f32x4 acc3[4][2];
#pragma unroll
        for (int dt = 0; dt < 4; ++dt) { acc3[dt][0] = f32x4{0.f, 0.f, 0.f, 0.f}; acc3[dt][1] = acc3[dt][0]; }
        unsigned ftf; u32x4 tx[2];
        static_for<0, 18>([&](auto Sx) {
            constexpr int s = Sx.v;
            wait_vm<p3_wait(s)>();
            if constexpr (s == 12 || s == 13) { pin(hh[s - 12][0]); pin(hh[s - 12][1]); pin(hh[s - 12][2]); pin(hh[s - 12][3]); }
            else { pin(hb[s % HD][0]); pin(hb[s % HD][1]); }
            if constexpr (s == 14) { pin(tx[0]); pin(tx[1]); }
            if constexpr (s == 2) STAMP(4);
            if constexpr (s == 2) {
                if (lane == 0) { const unsigned old = __hip_atomic_fetch_add((LAS unsigned*)(L + L_FLG) + 3, 1u, __ATOMIC_RELAXED, __HIP_MEMORY_SCOPE_WORKGROUP);
                    if (old == 7u) __hip_atomic_fetch_add((gu32*)p.ws + W_CT + (kq >> 1) * 32, 1u, RLX_AGENT); }
            }
            if constexpr (s == 14 - HD) {
                STAMP(12);
                pin(ftf);
                if (wid == 0) {
                    if (!__all(ftf == (tag << 8) + 16u)) {
#pragma unroll 1
                        for (unsigned spins = 0;; ++spins) {
                            __builtin_amdgcn_s_sleep(PSLEEP);
                            unsigned f2; gl32_sc1(f2, ws, (unsigned)(W_CT + (lane & 15) * 32) * 4u); wait_vm<0>(); pin(f2);
                            if (__all(f2 == (tag << 8) + 16u)) break;
                            if (spins > SPIN_CAP) { give_up(ws, lane); break; }
                        }
                    }
                    if (b == 0 && lane == 0) __hip_atomic_store((gu32*)p.ws, tag, RLX_AGENT);
                }
                STAMP(13);
            }
            if constexpr (s % 3 == 0 && s > 0) {
                WAIT_LGKM0(); BAR();
                p3_rdw(L, cx, kg, lane, s, wf[s & 1]);
            }
            TS(10 + s);
            if constexpr (s + 1 < 18 && (s + 1) % 3 != 0) p3_rdw(L, cx, kg, lane, s + 1, wf[(s + 1) & 1]);
            u32x4 b0, b1;
            if constexpr (s == 12 || s == 13) { b0 = u32x4{hh[s - 12][0].x, hh[s - 12][0].y, hh[s - 12][1].x, hh[s - 12][1].y}; b1 = u32x4{hh[s - 12][2].x, hh[s - 12][2].y, hh[s - 12][3].x, hh[s - 12][3].y}; }
            else { b0 = hb[s % HD][0]; b1 = hb[s % HD][1]; }
            if constexpr (s >= 14) {
                const u32x4 T0 = b0, T1 = b1;
                u32x4 T0s = swap_neg(T0), T1s = swap_neg(T1);
                if constexpr (s == 14) {
                    if (kg == 0 && fr == 0) { T0s = tx[0]; T1s = tx[1]; }
                }
                u32x4 zf[2];
#pragma unroll
                for (int st = 0; st < 2; ++st) {
                    f32x4 zr = {0.f, 0.f, 0.f, 0.f}, zi = zr;
                    zr = mfma(T0, E2[st * 2 + 0], zr); zr = mfma(T1, E2[st * 2 + 1], zr);
                    zi = mfma(T0s, E2[st * 2 + 0], zi); zi = mfma(T1s, E2[st * 2 + 1], zi);
                    zf[st] = pack8(zr, zi);
                }
                b0 = zf[0]; b1 = zf[1];
            }
#pragma unroll
            for (int dt = 0; dt < 4; ++dt) { acc3[dt][0] = mfma(wf[s & 1][dt], b0, acc3[dt][0]); acc3[dt][1] = mfma(wf[s & 1][dt], b1, acc3[dt][1]); }
            SB();
            if constexpr (s + HD < 18) {
                constexpr int o = s + HD;
                if constexpr (o < 12) { gl128_sc1(hb[o % HD][0], ws, cx.hbase + o * 4096u); gl128_sc1(hb[o % HD][1], ws, cx.hbase + 48u * 1024u + o * 4096u); }
                else if constexpr (o < 14) {
                    gl64_sc1(hh[o - 12][0], ws, cx.h2base + (o - 12) * 4096u); gl64_sc1(hh[o - 12][1], ws, cx.h2base + (o - 12) * 4096u + 512u);
                    gl64_sc1(hh[o - 12][2], ws, cx.h2base + 8192u + (o - 12) * 4096u); gl64_sc1(hh[o - 12][3], ws, cx.h2base + 8192u + (o - 12) * 4096u + 512u); }
                else { gl128_sc1(hb[o % HD][0], ws, cx.tbase + (o - 14) * 2048u); gl128_sc1(hb[o % HD][1], ws, cx.tbase + (o - 14) * 2048u + 1024u);
                    if constexpr (o == 14) { const unsigned txo = (unsigned)WS_TX + (unsigned)((4 * r3 + 2 * h3 + rh) * 8 + g) * 16u; gl128_sc1(tx[0], ws, txo); gl128_sc1(tx[1], ws, txo + 64u); } }
            }
            if constexpr (s >= 3 && s + 6 < 18) p3_issue2(p, Lb, wid, cc, s + 6, wv, fv);
            if constexpr (s == FTS) gl32_sc1(ftf, ws, wid == 0 ? (unsigned)(W_CT + (lane & 15) * 32) * 4u : cx.hbase - lane * 16u);
            SB();
        });
        STAMP(10);
        TS(28);
        wait_vm<0>(); pin(xres);
        WAIT_LGKM0(); BAR();
        constexpr int RP = 272;
#pragma unroll
        for (int dt = 0; dt < 4; ++dt)
#pragma unroll
            for (int st = 0; st < 2; ++st)
                *(LAS f32x4*)(L + (kg * 64 + 32 * rh + 16 * st + fr) * RP + (16 * dt + 4 * g) * 4) = acc3[dt][st];
        WAIT_LGKM0(); BAR();
        const int mm = tid >> 3, cg = tid & 7;
        f32x4 v0 = {0.f, 0.f, 0.f, 0.f}, v1 = v0;
#pragma unroll
        for (int q = 0; q < 4; ++q) { v0 += *(LAS f32x4*)(L + (q * 64 + mm) * RP + cg * 32); v1 += *(LAS f32x4*)(L + (q * 64 + mm) * RP + cg * 32 + 16); }
        const size_t o = (size_t)panel_row(r3, 64 * h3 + mm) * 512 + 64 * cc + 8 * cg;
        STAMP(11);
        if ((float)(long long)(stampv[TLIDX] - stampv[0]) * 0.01f > (float)TLIM && stampv[TLIDX] != 0) v0[0] += 0.05f;
        {
            const u32x4 ov = u32x4{pk_bf16(bf_lo(xres.x) + v0[0], bf_hi(xres.x) + v0[1]), pk_bf16(bf_lo(xres.y) + v0[2], bf_hi(xres.y) + v0[3]),
                                   pk_bf16(bf_lo(xres.z) + v1[0], bf_hi(xres.z) + v1[1]), pk_bf16(bf_lo(xres.w) + v1[2], bf_hi(xres.w) + v1[3])};
            __builtin_nontemporal_store(ov, (GAS u32x4*)(p.out + o));
        }
    }
    STAMP(11);
    if (threadIdx.x == 256) {
#pragma unroll
        for (int i = 0; i < 16; ++i) ((GAS unsigned long long*)(p.ws + WS_STAMP + 131072))[blockIdx.x * 16 + i] = stampv[i]; }
    if (threadIdx.x == 0) {
#pragma unroll
        for (int i = 0; i < 16; ++i) ((GAS unsigned long long*)(p.ws + WS_STAMP))[blockIdx.x * 32 + i] = stampv[i];
#pragma unroll
        for (int i = 0; i < 32; ++i) ((GAS unsigned*)(p.ws + WS_STAMP + 65536))[blockIdx.x * 32 + i] = tsv[i]; }
}

extern "C" void kernel_launch(void* const* d_in, const int* in_sizes, int n_in, void* d_out, int out_size, void* d_ws, size_t ws_size,
                              hipStream_t stream) {
    Params p;
    p.x = (const uint16_t*)d_in[0]; p.wr = (const uint16_t*)d_in[1]; p.wi = (const uint16_t*)d_in[2];
    p.wg = (const uint16_t*)d_in[4]; p.wu = (const uint16_t*)d_in[5]; p.wd = (const uint16_t*)d_in[6];
    p.out = (uint16_t*)d_out; p.ws = (unsigned char*)d_ws;
    (void)hipFuncSetAttribute((const void*)k_fused, hipFuncAttributeMaxDynamicSharedMemorySize, LDS_BYTES);
    k_fused<<<256, NT, LDS_BYTES, stream>>>(p);
}
```

```cpp
#define OUT_NT
#include <hip/hip_runtime.h>
#include <stdint.h>

#define LAS __attribute__((address_space(3)))
#define GAS __attribute__((address_space(1)))
#define DI __device__ __forceinline__
typedef __bf16 bf8 __attribute__((ext_vector_type(8)));
typedef __bf16 bf2 __attribute__((ext_vector_type(2)));
typedef short s16x4 __attribute__((ext_vector_type(4)));
typedef short s16x8 __attribute__((ext_vector_type(8)));
typedef float f32x4 __attribute__((ext_vector_type(4)));
typedef float f32x2 __attribute__((ext_vector_type(2)));
typedef unsigned u32x4 __attribute__((ext_vector_type(4)));
typedef unsigned u32x2 __attribute__((ext_vector_type(2)));
typedef GAS unsigned gu32;
typedef GAS unsigned long long gu64;
#define RLX_AGENT __ATOMIC_RELAXED, __HIP_MEMORY_SCOPE_AGENT

namespace tabs {
constexpr double PI = 3.14159265358979323846264338327950288;
constexpr double cos_q(double x) {
    double x2 = x * x, t = 1.0, s = 1.0;
    for (int i = 1; i <= 14; ++i) { t *= -x2 / ((2 * i - 1) * (2 * i)); s += t; }
    return s;
}
constexpr uint16_t f2bf(float f) {
    uint32_t u = __builtin_bit_cast(uint32_t, f);
    u += 0x7FFFu + ((u >> 16) & 1u);
    return (uint16_t)(u >> 16);
}
struct Base { uint16_t v[512];
    constexpr Base() : v{} {
        for (int m = 0; m <= 128; ++m) {
            double c = (m == 128) ? 0.0 : cos_q(2.0 * PI * m / 512.0);
            uint16_t b = f2bf((float)c);
            uint16_t nb = (m == 128) ? b : (uint16_t)(b ^ 0x8000u);
            v[m] = b; v[(512 - m) & 511] = b;
            v[256 - m] = nb; v[(256 + m) & 511] = nb;
        }
    }
};
constexpr Base base{};
constexpr uint16_t cosb(int m) { return base.v[m & 511]; }
constexpr uint16_t nsinb(int m) { return base.v[(m + 128) & 511]; }
constexpr uint16_t sinb(int m) { return base.v[(m + 384) & 511]; }
struct Chunk { uint16_t v[16384]; };
constexpr Chunk mk_fwd(int c) {
    Chunk r{};
    for (int d = 0; d < 512; ++d)
        for (int n = 0; n < 32; ++n) { const int k = 16 * c + (n & 15);
            r.v[d * 32 + n] = base.v[((n >> 4) ? (k == 0 ? d * 256 : d * k + 128) : d * k) & 511]; }
    return r;
}
constexpr Chunk mk_inv(int blk) {
    Chunk r{};
    const int cc = blk >> 1;
    for (int e = 0; e < 16384; e += 8) {
        const int l = (e >> 3) & 63, dt = (e >> 9) & 3, kg = (e >> 11) & 3, s = ((blk & 1) << 1) + (e >> 13);
        const int kt = 4 * kg + s, d = 64 * cc + 16 * dt + (l & 15), k0 = 16 * kt + 4 * (l >> 4);
        for (int j = 0; j < 4; ++j) { const int k = k0 + j;
            r.v[e + j] = base.v[(k * d) & 511];
            r.v[e + 4 + j] = base.v[(k == 0 ? d * 256 : k * d + 128) & 511]; }
    }
    return r;
}
template <int I> constexpr Chunk fwd_c = mk_fwd(I);
template <int I> constexpr Chunk inv_c = mk_inv(I);
struct Big { Chunk c[16]; };
}
#define C16(N) {{tabs::N<0>, tabs::N<1>, tabs::N<2>, tabs::N<3>, tabs::N<4>, tabs::N<5>, tabs::N<6>, tabs::N<7>, \
                 tabs::N<8>, tabs::N<9>, tabs::N<10>, tabs::N<11>, tabs::N<12>, tabs::N<13>, tabs::N<14>, tabs::N<15>}}
alignas(256) static __device__ const tabs::Big g_fwd = C16(fwd_c);
alignas(256) static __device__ const tabs::Big g_inv = C16(inv_c);

constexpr int S = 2048, D = 512, F = 1792, DF = 257, NT = 512;
constexpr size_t WS_CTRL = 0;
constexpr size_t WS_STAT = 98304;
constexpr size_t WS_AP = 131072;
constexpr size_t WS_AX = WS_AP + (2u << 20);
constexpr size_t WS_TX = WS_AX + 8192;
constexpr size_t WS_T = WS_AX + 65536;
constexpr size_t WS_H = WS_T + (2u << 20);
constexpr size_t WS_H2 = WS_H + (size_t)16 * 8 * 48 * 1024;
constexpr size_t WS_END = WS_H2 + (size_t)16 * 8 * 16 * 512;
#define FSTR 32
#define PSLEEP 8
constexpr int W_CT = 1024, W_TMO = 2048, W_FA = 4096, W_FH = W_FA + 256 * 32;
constexpr unsigned SPIN_CAP = 1u << 16;
constexpr int CODE_PF_BYTES = 37 * 1024;

struct Params {
    const uint16_t* x; const uint16_t* wr; const uint16_t* wi;
    const uint16_t* wg; const uint16_t* wu; const uint16_t* wd;
    uint16_t* out; unsigned char* ws;
};

template <int I> struct IC { static constexpr int v = I; };
template <int B, int E, class Fn> DI void static_for(Fn&& f) { if constexpr (B < E) { f(IC<B>{}); static_for<B + 1, E>(f); } }
DI f32x4 mfma(u32x4 a, u32x4 b, f32x4 c) {
    return __builtin_amdgcn_mfma_f32_16x16x32_bf16(__builtin_bit_cast(bf8, a), __builtin_bit_cast(bf8, b), c, 0, 0, 0);
}
DI unsigned pk_bf16(float lo, float hi) { bf2 p; p[0] = (__bf16)lo; p[1] = (__bf16)hi; return __builtin_bit_cast(unsigned, p); }
DI float bf_lo(unsigned u) { return __builtin_bit_cast(float, u << 16); }
DI float bf_hi(unsigned u) { return __builtin_bit_cast(float, u & 0xffff0000u); }
DI float bfr(float x) { return bf_lo(pk_bf16(x, x)); }
DI u32x4 lds128(LAS unsigned char* L, int off) { return *(LAS u32x4*)(L + off); }
DI u32x2 lds_tr(LAS unsigned char* L, int off) {
    return __builtin_bit_cast(u32x2, __builtin_amdgcn_ds_read_tr16_b64_v4i16((LAS s16x4*)(L + off)));
}
DI u32x4 lds_tr2(LAS unsigned char* L, int off0, int off1) {
    const u32x2 a = lds_tr(L, off0), b = lds_tr(L, off1);
    return u32x4{a.x, a.y, b.x, b.y};
}
DI u32x4 swap_neg(u32x4 v) { return u32x4{v.z, v.w, v.x ^ 0x80008000u, v.y ^ 0x80008000u}; }
DI u32x4 pack8(const float (&a)[4], const float (&b)[4]) { return u32x4{pk_bf16(a[0], a[1]), pk_bf16(a[2], a[3]), pk_bf16(b[0], b[1]), pk_bf16(b[2], b[3])}; }
DI u32x4 pack8(f32x4 a, f32x4 b) { return u32x4{pk_bf16(a[0], a[1]), pk_bf16(a[2], a[3]), pk_bf16(b[0], b[1]), pk_bf16(b[2], b[3])}; }
DI void glds16(const void* gsrc, unsigned lds_addr) {
    asm volatile("s_mov_b32 m0, %1\n\ts_nop 0\n\tglobal_load_lds_dwordx4 %0, off" :: "v"(gsrc), "s"(lds_addr) : "memory");
}
DI void glds16s(const void* sbase, unsigned voff, unsigned lds_addr) {
    asm volatile("s_mov_b32 m0, %2\n\ts_nop 0\n\tglobal_load_lds_dwordx4 %0, %1" :: "v"(voff), "s"(sbase), "s"(lds_addr) : "memory");
}
DI void glds4(const void* gsrc, unsigned lds_addr) {
    asm volatile("s_mov_b32 m0, %1\n\ts_nop 0\n\tglobal_load_lds_dword %0, off" :: "v"(gsrc), "s"(lds_addr) : "memory");
}
DI void gl128(u32x4& d, const void* p) { asm volatile("global_load_dwordx4 %0, %1, off" : "=v"(d) : "v"(p) : "memory"); }
DI void gl128_sc1(u32x4& d, const void* base, unsigned off) { asm volatile("global_load_dwordx4 %0, %1, %2 sc1" : "=v"(d) : "v"(off), "s"(base) : "memory"); }
DI void gl64_sc1(u32x2& d, const void* base, unsigned off) { asm volatile("global_load_dwordx2 %0, %1, %2 sc1" : "=v"(d) : "v"(off), "s"(base) : "memory"); }
DI void gl32_sc1(unsigned& d, const void* base, unsigned off) { asm volatile("global_load_dword %0, %1, %2 sc1" : "=v"(d) : "v"(off), "s"(base) : "memory"); }
DI void gl16(unsigned& d, const void* base, unsigned off) { asm volatile("global_load_ushort %0, %1, %2" : "=v"(d) : "v"(off), "s"(base) : "memory"); }
template <int N> DI void wait_vm() { static_assert(N >= 0 && N < 64, "vmcnt"); asm volatile("s_waitcnt vmcnt(%0)" :: "n"(N) : "memory"); }
template <class T> DI void pin(T& v) { asm volatile("" : "+v"(v)); }
#define WAIT_LGKM0() asm volatile("s_waitcnt lgkmcnt(0)" ::: "memory")
#define BAR() do { asm volatile("" ::: "memory"); __builtin_amdgcn_s_barrier(); asm volatile("" ::: "memory"); } while (0)
#define SB() __builtin_amdgcn_sched_barrier(0)
DI void st_sc1(__amdgpu_buffer_rsrc_t rs, int off, u32x4 v) { __builtin_amdgcn_raw_buffer_store_b128(v, rs, off, 0, 16); }
DI void st_sc1_64(__amdgpu_buffer_rsrc_t rs, int off, u32x2 v) { __builtin_amdgcn_raw_buffer_store_b64(v, rs, off, 0, 16); }
template <int CTRL> DI float dpp_add(float v) {
    return v + __builtin_bit_cast(float, __builtin_amdgcn_update_dpp(0, __builtin_bit_cast(int, v), CTRL, 0xf, 0xf, true));
}
DI float wave_sum(float v) {
    v = dpp_add<0xB1>(v);
    v = dpp_add<0x4E>(v);
    v = dpp_add<0x141>(v);
    v = dpp_add<0x140>(v);
    const int vi = __builtin_bit_cast(int, v);
    return (__builtin_bit_cast(float, __builtin_amdgcn_readlane(vi, 0)) + __builtin_bit_cast(float, __builtin_amdgcn_readlane(vi, 16))) +
           (__builtin_bit_cast(float, __builtin_amdgcn_readlane(vi, 32)) + __builtin_bit_cast(float, __builtin_amdgcn_readlane(vi, 48)));
}
DI void give_up(const unsigned char* ws, int lane) { if (lane == 0) __hip_atomic_store((gu32*)ws + W_TMO, 1u, RLX_AGENT); }
DI void poll16(const unsigned char* ws, int word, unsigned tag, int lane) {
#pragma unroll 1
    for (unsigned spins = 0;; ++spins) {
        unsigned f; gl32_sc1(f, ws, (unsigned)(word + (lane & 15) * FSTR) * 4u); wait_vm<0>(); pin(f);
        if (__all(f == tag)) break;
        if (spins > SPIN_CAP) { give_up(ws, lane); break; }
        __builtin_amdgcn_s_sleep(PSLEEP);
    }
}
DI void fx_load(unsigned (&w)[8], unsigned o0, unsigned o1, unsigned o2, unsigned o3, const void* wr, const void* wi, int k4) {
    asm volatile("s_cmp_lg_u32 %14, 0\n\ts_cbranch_scc1 .Lnofx%=\n\t"
                 "global_load_ushort %0, %8, %12\n\tglobal_load_ushort %1, %9, %12\n\tglobal_load_ushort %2, %10, %12\n\tglobal_load_ushort %3, %11, %12\n\t"
                 "global_load_ushort %4, %8, %13\n\tglobal_load_ushort %5, %9, %13\n\tglobal_load_ushort %6, %10, %13\n\tglobal_load_ushort %7, %11, %13\n"
                 ".Lnofx%=:" : "=&v"(w[0]), "=&v"(w[1]), "=&v"(w[2]), "=&v"(w[3]), "=&v"(w[4]), "=&v"(w[5]), "=&v"(w[6]), "=&v"(w[7])
                 : "v"(o0), "v"(o1), "v"(o2), "v"(o3), "s"(wr), "s"(wi), "s"(k4) : "memory", "scc");
}
DI float cosr(float x) { return __builtin_amdgcn_cosf(x); }
DI float sinr(float x) { return __builtin_amdgcn_sinf(x); }
DI u32x4 frag_cs(int a, int b0, int n) {
    float c[4], sn[4];
#pragma unroll
    for (int j = 0; j < 4; ++j) { const float th = (float)((a * (b0 + j)) & (n - 1)) * (1.0f / n); c[j] = cosr(th); sn[j] = -sinr(th); }
    return pack8(c, sn);
}

#define STAMP(i) do {} while (0)
#define TS(i) do {} while (0)

constexpr int P1_STAGE = 49152, P1_BOFF = 16384;
constexpr int L_MISC = 147456;
constexpr int L_XI = L_MISC;
constexpr int L_ST = L_MISC + 8704;
constexpr int L_YX = L_ST + 1024;
constexpr int L_SCR = L_YX + 4096;
constexpr int L_FLG = L_SCR + 256;
constexpr int L_END = L_FLG + 16;
static_assert(L_END <= 163840, "lds");
constexpr int LDS_BYTES = 163840;
constexpr int L_RAW = 98304;
constexpr int P3_SLOT = 16384;
DI constexpr int p3_slot(int s) { return (s % 9) * P3_SLOT; }

DI int panel_row(int r, int m) { return 64 * (m & 31) + 4 * r + (m >> 5); }
DI int p3_ks(int s, int kg) { const int n = 4 * s + kg; if (s >= 12) return 7 * (n - 48) + 3; const int p = n / 6, o = n - 6 * p; return 7 * p + o + (o >= 3); }

struct LdSrc { unsigned a[4]; const unsigned char* b[8]; long sB64; const unsigned char* t; long tadv; };
DI void p1_issue(LdSrc& s, const unsigned char* x, unsigned Lb, int lw, int j) {
    const unsigned sb = Lb + (j % 3) * P1_STAGE;
#pragma unroll
    for (int i = 0; i < 4; ++i) glds16s(x + j * 128, s.a[i], sb + (4 * lw + i) * 1024);
#pragma unroll
    for (int u = 0; u < 8; ++u) { const int i = (lw & 1) + 2 * (u & 1) + 4 * (lw >> 1) + 8 * ((u >> 1) & 1) + 16 * (u >> 2);
        glds16(s.b[u], sb + P1_BOFF + i * 1024); s.b[u] += s.sB64; }
}
DI void p1_touch(LdSrc& s, unsigned Lb) { glds4(s.t, Lb + L_SCR); s.t += s.tadv; }
DI void p3_issue(const Params& p, unsigned Lb, int lw, int cc, int s, unsigned wv, unsigned fv) {
    const unsigned sb = Lb + p3_slot(s) + lw * 4096;
    if (s < 14) { const unsigned char* base = (const unsigned char*)p.wd + (size_t)p3_ks(s, lw) * 32768;
#pragma unroll
        for (int i = 0; i < 4; ++i) glds16s(base + i * 8192, wv, sb + i * 1024); }
    else { const unsigned char* base = (const unsigned char*)&g_inv + cc * 65536 + (s - 14) * 16384 + lw * 4096;
#pragma unroll
        for (int i = 0; i < 4; ++i) glds16s(base + i * 1024, fv, sb + i * 1024); }
}

DI void p3_issue2(const Params& p, unsigned Lb, int w, int cc, int s, unsigned wv, unsigned fv) {
    const unsigned sb = Lb + p3_slot(s) + w * 2048;
    if (s < 14) { const unsigned char* base = (const unsigned char*)p.wd + (size_t)p3_ks(s, w >> 1) * 32768 + (w & 1) * 16384;
        glds16s(base, wv, sb); glds16s(base + 8192, wv, sb + 1024); }
    else { const unsigned char* base = (const unsigned char*)&g_inv + cc * 65536 + (s - 14) * 16384 + w * 2048;
        glds16s(base, fv, sb); glds16s(base + 1024, fv, sb + 1024); }
}

template <bool DFTW>
DI void p1_compute(LAS unsigned char* L, unsigned Lb, int wc, int lane, f32x4 (&acc)[4][8], f32x4 (&csm)[4], unsigned (&tsv)[32]) {
    asm volatile("" : "+v"(lane));
    const int fr = lane & 15, g = lane >> 4;
    const int qq = fr >> 2, p = lane & 3;
    int aoff0 = fr * 128 + (((0 + g) ^ (fr & 7)) << 4);
    int aoff1 = fr * 128 + (((4 + g) ^ (fr & 7)) << 4);
    const int sw = 2 * qq + 8 * (g & 1);
    int boff[4];
#pragma unroll
    for (int q = 0; q < 4; ++q) boff[q] = P1_BOFF + (8 * g + qq) * 512 + (((8 * wc + 2 * q + (p >> 1)) ^ sw) << 4) + 8 * (p & 1);
    u32x4 xa[2][4], wb[2][4];
    auto rdx = [&](int ks, int mh, int i) { xa[mh][i] = lds128(L, (ks ? aoff1 : aoff0) + (4 * mh + i) * 2048); };
    auto rdw = [&](int ks, int q) { const int o = boff[q] + ks * 16384; wb[ks][q] = lds_tr2(L, o, o + 2048); };
    auto rebase = [&](int d) { aoff0 += d; aoff1 += d; boff[0] += d; boff[1] += d; boff[2] += d; boff[3] += d;
        asm volatile("" : "+v"(aoff0), "+v"(aoff1), "+v"(boff[0]), "+v"(boff[1]), "+v"(boff[2]), "+v"(boff[3])); };
    auto step = [&](int ks, int mh, bool more) {
#pragma unroll
        for (int i = 0; i < 16; ++i) {
            const int q = i >> 2, mt = i & 3;
            if (DFTW && q >= 2) acc[q][4 * mh + mt] = mfma(xa[mh][mt], wb[ks][q], acc[q][4 * mh + mt]);
            else acc[q][4 * mh + mt] = mfma(wb[ks][q], xa[mh][mt], acc[q][4 * mh + mt]);
            if (more) {
                if (mh == 0) { if (i >= 1 && i <= 4) rdx(ks, 1, i - 1); }
                else { if (i < 4) rdx(ks ^ 1, 0, i); else if (i < 8) rdw(ks ^ 1, i - 4); }
            }
            if (mh == 0 && (i & 3) == 3) { const int d = i >> 2; if (!DFTW || d < 2) csm[d] = mfma(wb[ks][d], u32x4{0x3F803F80u, 0x3F803F80u, 0x3F803F80u, 0x3F803F80u}, csm[d]); }
            SB();
        }
    };
    BAR();
    TS(0);
#pragma unroll
    for (int i = 0; i < 4; ++i) { rdx(0, 0, i); rdw(0, i); }
    SB();
    static_for<0, 8>([&](auto J) {
        constexpr int j = J.v;
        step(0, 0, true);
        step(0, 1, true);
        step(1, 0, true);
        if constexpr (j < 7) { WAIT_LGKM0(); BAR(); TS(j + 1); rebase((j + 1) % 3 == 0 ? -2 * P1_STAGE : P1_STAGE);
            SB(); }
        step(1, 1, j < 7);
    });
    TS(8);
}

constexpr int HD = 5;
constexpr int FTS = 10, FTC = 12;
constexpr int p3_issue_at(int o) { return o < 14 ? o - HD : (o < 16 ? FTC : FTC + 1); }
constexpr int p3_opcnt(int o) { return (o >= 12 && o <= 14) ? 4 : 2; }
enum { K_OP, K_TST, K_W, K_FT };
constexpr int p3_after(int kind, int idx, int s_now) {
    int n = -1;
    auto hit = [&](bool is, int cnt) { if (is) n = 0; else if (n >= 0) n += cnt; };
    for (int o = 0; o < HD; ++o) hit(kind == K_OP && idx == o, 2);
    for (int w = 3; w < 9; ++w) hit(kind == K_W && idx == w, 2);
    hit(kind == K_TST, 1); hit(false, 1);
    for (int s = 0; s < s_now; ++s) {
        for (int o = HD; o < 18; ++o) if (p3_issue_at(o) == s) hit(kind == K_OP && idx == o, p3_opcnt(o));
        if (s >= 3 && s + 6 < 18) hit(kind == K_W && idx == s + 6, 2);
        if (s == FTS) hit(kind == K_FT, 1);
    }
    return n;
}
constexpr int cmin(int a, int b) { return a < b ? a : b; }
constexpr int p3_wait(int s) {
    int n = p3_after(K_OP, s, s);
    if (s == 2) n = cmin(n, p3_after(K_TST, 0, s));
    if (s % 3 == 0 && s >= 3) n = cmin(n, p3_after(K_W, s + 2, s));
    if (s == FTC) n = cmin(n, p3_after(K_FT, 0, s));
    return n;
}
static_assert(p3_wait(0) == 2 * (HD - 1) + 12 + 2 && p3_wait(2) == 1 + 4, "queue model");

struct P3Ctx { unsigned hbase, h2base, tbase; int woff[4]; };
DI void p3_rdw(LAS unsigned char* L, const P3Ctx& c, int kg, int lane, int s, u32x4 (&wf)[4]) {
#pragma unroll
    for (int dt = 0; dt < 4; ++dt) {
        if (s < 14) wf[dt] = lds_tr2(L, p3_slot(s) + c.woff[dt], p3_slot(s) + c.woff[dt] + 2048);
        else wf[dt] = lds128(L, p3_slot(s) + (kg * 4 + dt) * 1024 + lane * 16);
    }
}

__global__ void __launch_bounds__(NT, 2) k_fused(Params p) {
    extern __shared__ __attribute__((aligned(16))) unsigned char smem[];
    LAS unsigned char* L = (LAS unsigned char*)smem;
    const unsigned Lb = (unsigned)(size_t)L;
    const int wid = __builtin_amdgcn_readfirstlane(threadIdx.x >> 6);
    const int wc = wid & 3;
    const int b = blockIdx.x, xcd = b & 7, bi = b >> 3;
    const int r = (xcd & 1) * 8 + (bi & 7), c = (xcd >> 1) * 4 + (bi >> 3);
    const int jq = b & 7, kq = b >> 3;
    const int cc = (xcd & 1) * 4 + (bi & 3), hp = (xcd >> 1) * 8 + (bi >> 2), r3 = hp >> 1, h3 = hp & 1;
    const unsigned char* ws = p.ws;
    unsigned tsv[32];
#pragma unroll
    for (int i = 0; i < 32; ++i) tsv[i] = 0;
    STAMP(0);
    TS(31);
    const int odd = c & 1;
    const int u2 = wid >> 2, w2 = wid & 3, k4 = 2 * kq + u2;
    LAS f32x2* ST = (LAS f32x2*)(L + L_ST);
    f32x4 acc[4][8], csm[4];
    u32x4 eaC[2], eaS[2]; float tcA[2][4], tsA[2][4];
#pragma unroll
    for (int i = 0; i < 4; ++i) csm[i] = f32x4{0.f, 0.f, 0.f, 0.f};
    unsigned tag;
    {
    const int tid = threadIdx.x, lane = tid & 63;
    if (wid >= 4) {
        const int lw = wid - 4;
        unsigned epoch; gl32_sc1(epoch, ws, 0u);
        LdSrc ls;
        {
            const int idx = lw * 22 + (lane < 22 ? lane : 0);
            if (idx < 32) { ls.t = (const unsigned char*)p.x + (size_t)panel_row(r, 32 * (c & 3) + idx) * 1024; ls.tadv = 128; }
            else { const int i = idx - 32, row = 8 * (r & 7) + i / 7, it = i % 7;
                if (it == 6) { ls.t = (const unsigned char*)&g_fwd + c * 32768 + row * 64; ls.tadv = 64 * 64; }
                else { const int it3 = it % 3; ls.t = (const unsigned char*)(it < 3 ? p.wg : p.wu) + (size_t)row * (F * 2) + c * 224 + (it3 == 2 ? 220 : it3 * 128); ls.tadv = 64L * F * 2; } }
        }
        p1_touch(ls, Lb);
        SB();
#pragma unroll
        for (int i = 0; i < 4; ++i) { const int m = 8 * (4 * lw + i) + (lane >> 3); ls.a[i] = (unsigned)panel_row(r, m) * 1024u + (((lane & 7) ^ (lane >> 3)) << 4); }
        {
            const int b0 = lw & 1, b2 = lw >> 1;
            const int swz = 2 * (2 * b0 + (lane >> 5)) + 8 * b2;
            const int ch = (lane & 31) ^ swz, nt = ch >> 1, half = ch & 1, cw = nt >> 2, q = nt & 3;
            const unsigned char* base; int col, strideB;
            if (cw < 3) { const int ft = odd + 2 * cw + (q & 1); base = (const unsigned char*)(q < 2 ? p.wg : p.wu); col = 112 * c + 16 * ft + 8 * half; strideB = F * 2; }
            else if (q < 2) { const int ft = odd ? 0 : 6; base = (const unsigned char*)(q == 0 ? p.wg : p.wu); col = 112 * c + 16 * ft + 8 * half; strideB = F * 2; }
            else { base = (const unsigned char*)&g_fwd + c * 32768; col = 16 * (q - 2) + 8 * half; strideB = 64; }
            const unsigned char* b00 = base + (size_t)(2 * b0 + 8 * b2 + (lane >> 5)) * strideB + col * 2;
#pragma unroll
            for (int u = 0; u < 8; ++u) ls.b[u] = b00 + (size_t)(4 * (u & 1) + 16 * ((u >> 1) & 1) + 32 * (u >> 2)) * strideB;
            ls.sB64 = (long)strideB * 64;
        }
        p1_issue(ls, (const unsigned char*)p.x, Lb, lw, 0);
        p1_touch(ls, Lb); p1_touch(ls, Lb); p1_touch(ls, Lb);
        p1_issue(ls, (const unsigned char*)p.x, Lb, lw, 1);
        STAMP(14);
        if (tid == 256) { *(volatile LAS unsigned*)(L + L_FLG + 4) = 0u; *(volatile LAS unsigned*)(L + L_FLG + 8) = 0u; *(volatile LAS unsigned*)(L + L_FLG + 12) = 0u; }
        wait_vm<15>(); pin(epoch); tag = epoch + 1u;
        if (r == 0 && tid == 256) __hip_atomic_store((gu32*)p.ws + W_CT + c * 32, tag << 8, RLX_AGENT);
        WAIT_LGKM0();
        STAMP(15);
        BAR();
        p1_touch(ls, Lb);
        p1_issue(ls, (const unsigned char*)p.x, Lb, lw, 2);
        SB();
        const unsigned char *st0, *st1;
        {
            auto wline = [&](int i) { const int st = i >> 2, ln = (bi >> 2) * 16 + lw * 4 + (i & 3);
                return st < 14 ? (const unsigned char*)p.wd + (size_t)(32 * p3_ks(st, ln >> 5) + (ln & 31)) * 1024 + cc * 128
                               : (const unsigned char*)&g_inv + (size_t)cc * 65536 + (st - 14) * 16384 + ln * 128; };
            st0 = wline(lane);
            const int fi = (bi * 4 + lw) * 20 + (lane >= 8 && lane < 28 ? lane - 8 : 0);
            const int arr = fi / 1280, rr = (fi % 1280) / 5, ll = (fi % 1280) % 5;
            const int jrow = 4 * jq + (rr & 3) + 32 * (rr >> 2);
            st1 = lane < 8 ? wline(64 + lane) : (const unsigned char*)(arr ? p.wi : p.wr) + (size_t)jrow * (DF * 2) + min(ll * 128, 510);
        }
        u32x2 gran; const unsigned goff = (unsigned)WS_STAT + (unsigned)(r * 128 + (tid & 127)) * 8u;
        static_for<0, 7>([&](auto J) {
            constexpr int j = J.v;
            if constexpr (j < 4) wait_vm<13>(); else if constexpr (j == 4) wait_vm<12>();
            else if constexpr (j == 5) wait_vm<15>(); else wait_vm<3>();
            BAR();
            if constexpr (j < 3) p1_touch(ls, Lb);
            if constexpr (j + 3 < 8) p1_issue(ls, (const unsigned char*)p.x, Lb, lw, j + 3);
            if constexpr (j == 4) {
                gl64_sc1(gran, ws, goff);
                glds4(st0, Lb + L_SCR); glds4(st1, Lb + L_SCR);
            }
        });
        wait_vm<2>();
        pin(gran);
        if (lw < 2) {
            if (!__all(gran.y == tag)) {
#pragma unroll 1
                for (unsigned spins = 0;; ++spins) {
                    gl64_sc1(gran, ws, goff); wait_vm<0>(); pin(gran);
                    if (__all(gran.y == tag)) break;
                    if (spins > SPIN_CAP) { give_up(ws, lane); break; }
                    __builtin_amdgcn_s_sleep(1);
                }
            }
            ST[tid & 127] = f32x2{bf_hi(gran.x), 1.0f / bf_lo(gran.x)};
        }
        {
            const int fr = lane & 15, g = lane >> 4;
#pragma unroll
            for (int jt = 0; jt < 2; ++jt) {
                float cv[8], sv[8];
#pragma unroll
                for (int j = 0; j < 8; ++j) { const float th = (float)(((16 * jt + fr) * (8 * g + j)) & 31) * (1.0f / 32.0f); cv[j] = cosr(th); sv[j] = sinr(th); }
                eaC[jt] = u32x4{pk_bf16(cv[0], cv[1]), pk_bf16(cv[2], cv[3]), pk_bf16(cv[4], cv[5]), pk_bf16(cv[6], cv[7])};
                eaS[jt] = u32x4{pk_bf16(sv[0], sv[1]), pk_bf16(sv[2], sv[3]), pk_bf16(sv[4], sv[5]), pk_bf16(sv[6], sv[7])};
#pragma unroll
                for (int e = 0; e < 4; ++e) { const float th = (float)((16 * jt + fr) * (4 * r + e)) * (1.0f / 2048.0f); tcA[jt][e] = cosr(th); tsA[jt][e] = sinr(th); }
            }
        }
#pragma unroll
        for (int q = 0; q < 4; ++q)
#pragma unroll
            for (int mt = 0; mt < 8; ++mt) acc[q][mt] = f32x4{0.f, 0.f, 0.f, 0.f};
    } else {
        if (wid == 0) {
            const unsigned char* pc = (const unsigned char*)__builtin_amdgcn_s_getpc();
            const int ln = bi * 10 + (lane < 10 ? lane : 0);
            glds4(pc - ((size_t)pc & 127) + (size_t)min(ln * 128, CODE_PF_BYTES), Lb + L_SCR);
        }
        unsigned epoch; gl32_sc1(epoch, ws, 0u);
        u32x4 xrow[2];
#pragma unroll
        for (int i = 0; i < 2; ++i) gl128(xrow[i], (const unsigned char*)p.x + (size_t)panel_row(r, 8 * c + 2 * wid + i) * 1024 + lane * 16);
        wait_vm<0>(); pin(epoch); pin(xrow[0]); pin(xrow[1]);
        tag = epoch + 1u;
#pragma unroll
        for (int i = 0; i < 2; ++i) {
            float v[8];
#pragma unroll
            for (int e = 0; e < 4; ++e) { v[2 * e] = bf_lo(xrow[i][e]); v[2 * e + 1] = bf_hi(xrow[i][e]); }
            float s = ((v[0] + v[1]) + (v[2] + v[3])) + ((v[4] + v[5]) + (v[6] + v[7]));
            s = wave_sum(s);
            const float mean = s * (1.0f / 512.0f);
            float q2 = 0.f;
#pragma unroll
            for (int e = 0; e < 8; ++e) { const float d = v[e] - mean; q2 += d * d; }
            q2 = wave_sum(q2);
            const float var = q2 * (1.0f / 512.0f);
            const float m_bf = bfr(mean), v_bf = bfr(var), s_bf = bfr(sqrtf(bfr(v_bf + 1e-5f)));
            const unsigned val = (__builtin_bit_cast(unsigned, m_bf) & 0xffff0000u) | (__builtin_bit_cast(unsigned, s_bf) >> 16);
            if (lane == 0) __hip_atomic_store((gu64*)(p.ws + WS_STAT) + (r * 128 + 8 * c + 2 * wid + i), ((unsigned long long)tag << 32) | val, RLX_AGENT);
        }
#pragma unroll
        for (int q = 0; q < 4; ++q)
#pragma unroll
            for (int mt = 0; mt < 8; ++mt) acc[q][mt] = f32x4{0.f, 0.f, 0.f, 0.f};
        STAMP(1);
        if (wid < 3) p1_compute<false>(L, Lb, wc, lane, acc, csm, tsv); else p1_compute<true>(L, Lb, wc, lane, acc, csm, tsv);
        STAMP(2);
    }
    }
    int tz = threadIdx.x; asm volatile("" : "+v"(tz));
    const int tid = tz, lane = tz & 63, fr = lane & 15, g = lane >> 4;
    const __amdgpu_buffer_rsrc_t rs = __builtin_amdgcn_make_buffer_rsrc((void*)p.ws, 0, (int)WS_END, 0x00020000);
    const unsigned wv = (unsigned)(lane >> 3) * 1024u + cc * 128u + (unsigned)(((lane & 7) ^ (2 * ((lane >> 4) & 3))) << 4), fv = lane * 16u;
    volatile LAS unsigned* flg = (volatile LAS unsigned*)(L + L_FLG);
    WAIT_LGKM0(); BAR();
    STAMP(3);
    const int fj1 = 4 * jq + (fr & 3), fk = 4 * k4 + (fr >> 2);
    unsigned fw[4], fwi[4];
    auto signal = [&](int cnt, int word) {
        if (lane == 0) { const unsigned old = __hip_atomic_fetch_add((LAS unsigned*)(L + L_FLG) + cnt, 1u, __ATOMIC_RELAXED, __HIP_MEMORY_SCOPE_WORKGROUP);
            if (old == 3u) __hip_atomic_store((gu32*)p.ws + word, tag, RLX_AGENT); }
    };
    if (wid < 3) {
        const f32x4 cg0 = csm[0], cg1 = csm[1], cu0 = csm[2], cu1 = csm[3];
#pragma unroll
        for (int mt = 0; mt < 8; ++mt) {
            const f32x2 st = ST[16 * mt + fr];
            const float r2 = st.y * st.y;
            const f32x4 h0 = (acc[0][mt] - st.x * cg0) * (acc[2][mt] - st.x * cu0) * r2;
            const f32x4 h1 = (acc[1][mt] - st.x * cg1) * (acc[3][mt] - st.x * cu1) * r2;
            st_sc1(rs, (int)WS_H + ((((r * 8 + mt) * 48 + 3 * c + wc) * 64 + lane) << 4), pack8(h0, h1));
        }
    } else if (wid == 3) {
#pragma unroll
        for (int q = 0; q < 2; ++q)
#pragma unroll
            for (int mt = 0; mt < 8; ++mt) *(LAS f32x4*)(L + L_RAW + ((q * 8 + mt) * 64 + lane) * 16) = acc[2 + q][mt];
        WAIT_LGKM0();
        flg[0] = tag;
        const f32x4 cg0 = csm[0], cu0 = csm[1];
#pragma unroll
        for (int mt = 0; mt < 8; ++mt) {
            const f32x2 st = ST[16 * mt + fr];
            const float r2 = st.y * st.y;
            const f32x4 h0 = (acc[0][mt] - st.x * cg0) * (acc[1][mt] - st.x * cu0) * r2;
            st_sc1_64(rs, (int)WS_H2 + ((((r * 8 + mt) * 16 + c) * 64 + lane) << 3), u32x2{pk_bf16(h0[0], h0[1]), pk_bf16(h0[2], h0[3])});
        }
    } else {
        const int mt = wid - 4;
        const int kq_ = fr >> 2, s2l = fr & 3;
        __builtin_amdgcn_s_setprio(3);
#pragma unroll 1
        for (unsigned spins = 0; flg[0] != tag && spins < (1u << 20); ++spins) __builtin_amdgcn_s_sleep(1);
        asm volatile("" ::: "memory");
        u32x4 are, aim;
        {
            const int m0 = 32 * s2l + 8 * g, tl = 2 * s2l + (g >> 1), ln = 4 * mt + kq_ + 32 * (g & 1);
            const f32x4 r0 = *(LAS f32x4*)(L + L_RAW + (tl * 64 + ln) * 16), r1 = *(LAS f32x4*)(L + L_RAW + (tl * 64 + ln + 16) * 16);
            const f32x4 i0 = *(LAS f32x4*)(L + L_RAW + ((8 + tl) * 64 + ln) * 16), i1 = *(LAS f32x4*)(L + L_RAW + ((8 + tl) * 64 + ln + 16) * 16);
            const f32x4 sa = *(LAS f32x4*)(ST + m0), sb = *(LAS f32x4*)(ST + m0 + 2), sc = *(LAS f32x4*)(ST + m0 + 4), sd = *(LAS f32x4*)(ST + m0 + 6);
            const float csf = (c == 0 && mt == 0 && kq_ == 0) ? 512.0f : 0.0f;
            are = u32x4{pk_bf16((r0[0] - sa[0] * csf) * sa[1], (r0[1] - sa[2] * csf) * sa[3]), pk_bf16((r0[2] - sb[0] * csf) * sb[1], (r0[3] - sb[2] * csf) * sb[3]),
                        pk_bf16((r1[0] - sc[0] * csf) * sc[1], (r1[1] - sc[2] * csf) * sc[3]), pk_bf16((r1[2] - sd[0] * csf) * sd[1], (r1[3] - sd[2] * csf) * sd[3])};
            aim = u32x4{pk_bf16(i0[0] * sa[1], i0[1] * sa[3]), pk_bf16(i0[2] * sb[1], i0[3] * sb[3]), pk_bf16(i1[0] * sc[1], i1[1] * sc[3]), pk_bf16(i1[2] * sd[1], i1[3] * sd[3])};
        }
        const f32x4 z = {0.f, 0.f, 0.f, 0.f};
        const bool special = (c == 0 && mt == 0 && g == 0);
#pragma unroll
        for (int jt = 0; jt < 2; ++jt) {
            const f32x4 Prc = mfma(are, eaC[jt], z), Pis = mfma(aim, eaS[jt], z), Prs = mfma(are, eaS[jt], z), Pic = mfma(aim, eaC[jt], z);
            const int j1 = 16 * jt + fr;
            float pr[4], qi[4], pr2[4], qi2[4];
#pragma unroll
            for (int e = 0; e < 4; ++e) {
                const float cw = tcA[jt][e], sw = tsA[jt][e];
                const float ar = special ? Prc[e] : Prc[e] + Pis[e], ai = special ? -Prs[e] : Pic[e] - Prs[e];
                pr[e] = ar * cw + ai * sw; qi[e] = -(ai * cw - ar * sw);
                const float br = Pic[e], bi2 = -Pis[e];
                pr2[e] = br * cw + bi2 * sw; qi2[e] = -(bi2 * cw - br * sw);
            }
            const int U = (j1 >> 2) * 64 + 4 * c + mt, lp = 16 * (r & 3) + 4 * g + (j1 & 3);
            st_sc1(rs, (int)WS_AP + (((U * 4 + (r >> 2)) * 64 + lp) << 4), pack8(pr, qi));
            if (special) st_sc1(rs, (int)WS_AX + (((((j1 >> 2) * 4 + (r >> 2)) * 4 + (r & 3)) * 4 + (j1 & 3)) << 4), pack8(pr2, qi2));
        }
        __builtin_amdgcn_s_setprio(0);
        wait_vm<0>();
        signal(1, W_FA + (c * 16 + r) * FSTR);
#pragma unroll
        for (int st = 0; st < 3; ++st) p3_issue(p, Lb, mt, cc, st, wv, fv);
    }
#pragma unroll
    for (int e = 0; e < 4; ++e) { const unsigned o = (unsigned)((fj1 + 32 * (16 * w2 + 4 * g + e)) * DF + fk) * 2u; gl16(fw[e], p.wr, o); gl16(fwi[e], p.wi, o); }
    unsigned fx[8];
    {
        const unsigned o0 = (unsigned)((fj1 + 32 * (16 * w2 + 4 * g)) * DF + 256) * 2u;
        fx_load(fx, o0, o0 + 32u * DF * 2u, o0 + 64u * DF * 2u, o0 + 96u * DF * 2u, p.wr, p.wi, k4);
    }
    if (wid < 4) { if (k4 == 0) wait_vm<16>(); else wait_vm<8>(); signal(2, W_FH + (r * 16 + c) * FSTR); }
    STAMP(5);
    {
        const int kg = wid & 3, rh = wid >> 2;
        u32x4 E1[4], E2[4];
#pragma unroll
        for (int t = 0; t < 4; ++t) E1[t] = frag_cs(16 * w2 + fr, 16 * t + 4 * g, 64);
#pragma unroll
        for (int i = 0; i < 4; ++i) E2[i] = frag_cs(16 * (i >> 1) + fr, 16 * (i & 1) + 4 * g, 32);
        float tc[4], tsn[4];
#pragma unroll
        for (int e = 0; e < 4; ++e) { const float th = (float)((4 * jq + e) * (16 * w2 + fr)) * (1.0f / 2048.0f); tc[e] = cosr(th); tsn[e] = sinr(th); }
        P3Ctx cx;
        {
            const int qq = fr >> 2, pp = lane & 3, row = 32 * kg + 4 * g + qq, sw = 2 * ((row >> 1) & 3);
#pragma unroll
            for (int dt = 0; dt < 4; ++dt) cx.woff[dt] = row * 128 + (((2 * dt + (pp >> 1)) ^ sw) << 4) + 8 * (pp & 1);
            cx.hbase = (unsigned)WS_H + (unsigned)(((r3 * 8 + 4 * h3 + 2 * rh) * 48 + kg) * 64 + lane) * 16u;
            cx.h2base = (unsigned)WS_H2 + (unsigned)(((r3 * 8 + 4 * h3 + 2 * rh) * 16 + 2 * kg) * 64 + lane) * 8u;
            cx.tbase = (unsigned)WS_T + (unsigned)((((4 * r3 + 2 * h3 + rh) * 16 + 4 * kg) * 2) * 64 + lane) * 16u;
        }
        SB();
        STAMP(6);
        if (wid == 0) poll16(ws, W_FA + (kq >> 1) * 16 * FSTR, tag, lane);
        if (wid == 1) poll16(ws, W_FH + r3 * 16 * FSTR, tag, lane);
        BAR();
        STAMP(7);
        const int U = jq * 64 + k4;
        u32x4 Bf[4], Bx[4];
        u32x4 hb[HD][2]; u32x2 hh[2][4];
#pragma unroll
        for (int t = 0; t < 4; ++t) gl128_sc1(Bf[t], ws, (unsigned)WS_AP + (unsigned)((U * 4 + t) * 64 + lane) * 16u);
        {
            const unsigned xo = (unsigned)WS_AX + (unsigned)((jq * 16 + g) * 4 + (fr & 3)) * 16u;
            asm volatile("s_cmp_lg_u32 %6, 0\n\ts_cbranch_scc1 .Lnox%=\n\t"
                         "global_load_dwordx4 %0, %4, %5 sc1\n\tglobal_load_dwordx4 %1, %4, %5 offset:256 sc1\n\t"
                         "global_load_dwordx4 %2, %4, %5 offset:512 sc1\n\tglobal_load_dwordx4 %3, %4, %5 offset:768 sc1\n"
                         ".Lnox%=:" : "=&v"(Bx[0]), "=&v"(Bx[1]), "=&v"(Bx[2]), "=&v"(Bx[3]) : "v"(xo), "s"(ws), "s"(k4) : "memory", "scc");
        }
        static_for<0, HD>([&](auto Sx) { constexpr int o = Sx.v;
            gl128_sc1(hb[o][0], ws, cx.hbase + o * 4096u); gl128_sc1(hb[o][1], ws, cx.hbase + 48u * 1024u + o * 4096u); });
#pragma unroll
        for (int st = 3; st < 9; ++st) p3_issue2(p, Lb, wid, cc, st, wv, fv);
        wait_vm<2 * HD + 12>();
        pin(Bf[0]); pin(Bf[1]); pin(Bf[2]); pin(Bf[3]); pin(Bx[0]); pin(Bx[1]); pin(Bx[2]); pin(Bx[3]);
        pin(fw[0]); pin(fw[1]); pin(fw[2]); pin(fw[3]); pin(fwi[0]); pin(fwi[1]); pin(fwi[2]); pin(fwi[3]);
        pin(fx[0]); pin(fx[1]); pin(fx[2]); pin(fx[3]); pin(fx[4]); pin(fx[5]); pin(fx[6]); pin(fx[7]);
        STAMP(8);
        {
            f32x4 a1 = {0.f, 0.f, 0.f, 0.f}, a2 = a1;
#pragma unroll
            for (int t = 0; t < 4; ++t) { a1 = mfma(E1[t], Bf[t], a1); a2 = mfma(E1[t], swap_neg(Bf[t]), a2); }
            constexpr float SC = 2.0f / (2048.0f * 512.0f);
            const float sc = (k4 == 0 && (fr >> 2) == 0) ? 0.5f * SC : SC;
            float yr[4], yi[4];
#pragma unroll
            for (int e = 0; e < 4; ++e) { const float xr = a1[e], xi = -a2[e], wre = bf_lo(fw[e]), wim = bf_lo(fwi[e]);
                yr[e] = (xr * wre - xi * wim) * sc; yi[e] = (xr * wim + xi * wre) * sc; }
            if (k4 == 0) {
                f32x4 n1 = {0.f, 0.f, 0.f, 0.f}, n2 = n1;
#pragma unroll
                for (int t = 0; t < 4; ++t) { n1 = mfma(E1[t], Bx[t], n1); n2 = mfma(E1[t], swap_neg(Bx[t]), n2); }
                float xr2[4], xi2[4];
#pragma unroll
                for (int e = 0; e < 4; ++e) { const float xr = n1[e], xi = -n2[e], wre = bf_lo(fx[e]), wim = bf_lo(fx[4 + e]);
                    xr2[e] = (xr * wre - xi * wim) * (0.5f * SC); xi2[e] = (xr * wim + xi * wre) * (0.5f * SC); }
                *(LAS u32x4*)(L + L_YX + (w2 * 64 + lane) * 16) = pack8(xr2, xi2);
            }
            *(LAS u32x4*)(L + L_XI + ((u2 * 4 + w2) * 64 + lane) * 16) = pack8(yr, yi);
        }
        WAIT_LGKM0(); BAR();
        u32x4 wf[2][4];
        p3_rdw(L, cx, kg, lane, 0, wf[0]);
        {
            f32x4 t1 = {0.f, 0.f, 0.f, 0.f}, t2 = t1;
#pragma unroll
            for (int t = 0; t < 4; ++t) { const u32x4 y = lds128(L, L_XI + ((u2 * 4 + t) * 64 + lane) * 16);
                t1 = mfma(y, E1[t], t1); t2 = mfma(swap_neg(y), E1[t], t2); }
            const int s2 = 16 * w2 + fr, ko = 4 * k4 + g;
            float tr[4], ti[4];
#pragma unroll
            for (int e = 0; e < 4; ++e) { tr[e] = t1[e] * tc[e] - t2[e] * tsn[e]; ti[e] = t2[e] * tc[e] + t1[e] * tsn[e]; }
            st_sc1(rs, (int)WS_T + ((((s2 * 16 + (ko >> 4)) * 2 + (jq >> 2)) * 64 + (jq & 3) * 16 + (ko & 15)) << 4), pack8(tr, ti));
            if (k4 == 0) {
                f32x4 x1 = {0.f, 0.f, 0.f, 0.f}, x2 = x1;
#pragma unroll
                for (int t = 0; t < 4; ++t) { const u32x4 y = lds128(L, L_YX + (t * 64 + lane) * 16);
                    x1 = mfma(y, E1[t], x1); x2 = mfma(swap_neg(y), E1[t], x2); }
#pragma unroll
                for (int e = 0; e < 4; ++e) { tr[e] = x1[e] * tc[e] - x2[e] * tsn[e]; ti[e] = x2[e] * tc[e] + x1[e] * tsn[e]; }
                if (g == 0) st_sc1(rs, (int)WS_TX + ((s2 * 8 + jq) << 4), pack8(tr, ti));
            }
        }
        u32x4 xres; gl128(xres, (const unsigned char*)p.x + ((size_t)panel_row(r3, 64 * h3 + (tid >> 3)) * 512 + 64 * cc + 8 * (tid & 7)) * 2);
        STAMP(9);
        f32x4 acc3[4][2];
#pragma unroll
        for (int dt = 0; dt < 4; ++dt) { acc3[dt][0] = f32x4{0.f, 0.f, 0.f, 0.f}; acc3[dt][1] = acc3[dt][0]; }
        unsigned ftf; u32x4 tx[2];
        static_for<0, 18>([&](auto Sx) {
            constexpr int s = Sx.v;
            wait_vm<p3_wait(s)>();
            if constexpr (s == 12 || s == 13) { pin(hh[s - 12][0]); pin(hh[s - 12][1]); pin(hh[s - 12][2]); pin(hh[s - 12][3]); }
            else { pin(hb[s % HD][0]); pin(hb[s % HD][1]); }
            if constexpr (s == 14) { pin(tx[0]); pin(tx[1]); }
            if constexpr (s == 2) STAMP(4);
            if constexpr (s == 2) {
                if (lane == 0) { const unsigned old = __hip_atomic_fetch_add((LAS unsigned*)(L + L_FLG) + 3, 1u, __ATOMIC_RELAXED, __HIP_MEMORY_SCOPE_WORKGROUP);
                    if (old == 7u) __hip_atomic_fetch_add((gu32*)p.ws + W_CT + (kq >> 1) * 32, 1u, RLX_AGENT); }
            }
            if constexpr (s == FTC) {
                STAMP(12);
                pin(ftf);
                if (wid == 0) {
                    if (!__all(ftf == (tag << 8) + 16u)) {
#pragma unroll 1
                        for (unsigned spins = 0;; ++spins) {
                            __builtin_amdgcn_s_sleep(PSLEEP);
                            unsigned f2; gl32_sc1(f2, ws, (unsigned)(W_CT + (lane & 15) * 32) * 4u); wait_vm<0>(); pin(f2);
                            if (__all(f2 == (tag << 8) + 16u)) break;
                            if (spins > SPIN_CAP) { give_up(ws, lane); break; }
                        }
                    }
                    if (b == 0 && lane == 0) __hip_atomic_store((gu32*)p.ws, tag, RLX_AGENT);
                }
                STAMP(13);
            }
            if constexpr (s % 3 == 0 && s > 0) {
                WAIT_LGKM0(); BAR();
                p3_rdw(L, cx, kg, lane, s, wf[s & 1]);
            }
            TS(10 + s);
            if constexpr (s + 1 < 18 && (s + 1) % 3 != 0) p3_rdw(L, cx, kg, lane, s + 1, wf[(s + 1) & 1]);
            u32x4 b0, b1;
            if constexpr (s == 12 || s == 13) { b0 = u32x4{hh[s - 12][0].x, hh[s - 12][0].y, hh[s - 12][1].x, hh[s - 12][1].y}; b1 = u32x4{hh[s - 12][2].x, hh[s - 12][2].y, hh[s - 12][3].x, hh[s - 12][3].y}; }
            else { b0 = hb[s % HD][0]; b1 = hb[s % HD][1]; }
            if constexpr (s >= 14) {
                const u32x4 T0 = b0, T1 = b1;
                u32x4 T0s = swap_neg(T0), T1s = swap_neg(T1);
                if constexpr (s == 14) {
                    if (kg == 0 && fr == 0) { T0s = tx[0]; T1s = tx[1]; }
                }
                u32x4 zf[2];
#pragma unroll
                for (int st = 0; st < 2; ++st) {
                    f32x4 zr = {0.f, 0.f, 0.f, 0.f}, zi = zr;
                    zr = mfma(T0, E2[st * 2 + 0], zr); zr = mfma(T1, E2[st * 2 + 1], zr);
                    zi = mfma(T0s, E2[st * 2 + 0], zi); zi = mfma(T1s, E2[st * 2 + 1], zi);
                    zf[st] = pack8(zr, zi);
                }
                b0 = zf[0]; b1 = zf[1];
            }
#pragma unroll
            for (int dt = 0; dt < 4; ++dt) { acc3[dt][0] = mfma(wf[s & 1][dt], b0, acc3[dt][0]); acc3[dt][1] = mfma(wf[s & 1][dt], b1, acc3[dt][1]); }
            SB();
            static_for<HD, 18>([&](auto Ox) {
                constexpr int o = Ox.v;
                if constexpr (p3_issue_at(o) == s) {
                    if constexpr (o < 12) { gl128_sc1(hb[o % HD][0], ws, cx.hbase + o * 4096u); gl128_sc1(hb[o % HD][1], ws, cx.hbase + 48u * 1024u + o * 4096u); }
                    else if constexpr (o < 14) {
                        gl64_sc1(hh[o - 12][0], ws, cx.h2base + (o - 12) * 4096u); gl64_sc1(hh[o - 12][1], ws, cx.h2base + (o - 12) * 4096u + 512u);
                        gl64_sc1(hh[o - 12][2], ws, cx.h2base + 8192u + (o - 12) * 4096u); gl64_sc1(hh[o - 12][3], ws, cx.h2base + 8192u + (o - 12) * 4096u + 512u); }
                    else { gl128_sc1(hb[o % HD][0], ws, cx.tbase + (o - 14) * 2048u); gl128_sc1(hb[o % HD][1], ws, cx.tbase + (o - 14) * 2048u + 1024u);
                        if constexpr (o == 14) { const unsigned txo = (unsigned)WS_TX + (unsigned)((4 * r3 + 2 * h3 + rh) * 8 + g) * 16u; gl128_sc1(tx[0], ws, txo); gl128_sc1(tx[1], ws, txo + 64u); } }
                }
            });
            if constexpr (s >= 3 && s + 6 < 18) p3_issue2(p, Lb, wid, cc, s + 6, wv, fv);
            if constexpr (s == FTS) gl32_sc1(ftf, ws, wid == 0 ? (unsigned)(W_CT + (lane & 15) * 32) * 4u : cx.hbase - lane * 16u);
            SB();
        });
        STAMP(10);
        TS(28);
        wait_vm<0>(); pin(xres);
        WAIT_LGKM0(); BAR();
        constexpr int RP = 272;
#pragma unroll
        for (int dt = 0; dt < 4; ++dt)
#pragma unroll
            for (int st = 0; st < 2; ++st)
                *(LAS f32x4*)(L + (kg * 64 + 32 * rh + 16 * st + fr) * RP + (16 * dt + 4 * g) * 4) = acc3[dt][st];
        WAIT_LGKM0(); BAR();
        const int mm = tid >> 3, cg = tid & 7;
        f32x4 v0 = {0.f, 0.f, 0.f, 0.f}, v1 = v0;
#pragma unroll
        for (int q = 0; q < 4; ++q) { v0 += *(LAS f32x4*)(L + (q * 64 + mm) * RP + cg * 32); v1 += *(LAS f32x4*)(L + (q * 64 + mm) * RP + cg * 32 + 16); }
        const size_t o = (size_t)panel_row(r3, 64 * h3 + mm) * 512 + 64 * cc + 8 * cg;
        {
            const u32x4 ov = u32x4{pk_bf16(bf_lo(xres.x) + v0[0], bf_hi(xres.x) + v0[1]), pk_bf16(bf_lo(xres.y) + v0[2], bf_hi(xres.y) + v0[3]),
                                   pk_bf16(bf_lo(xres.z) + v1[0], bf_hi(xres.z) + v1[1]), pk_bf16(bf_lo(xres.w) + v1[2], bf_hi(xres.w) + v1[3])};
            __builtin_nontemporal_store(ov, (GAS u32x4*)(p.out + o));
        }
    }
    STAMP(11);
}

extern "C" void kernel_launch(void* const* d_in, const int* in_sizes, int n_in, void* d_out, int out_size, void* d_ws, size_t ws_size,
                              hipStream_t stream) {
    Params p;
    p.x = (const uint16_t*)d_in[0]; p.wr = (const uint16_t*)d_in[1]; p.wi = (const uint16_t*)d_in[2];
    p.wg = (const uint16_t*)d_in[4]; p.wu = (const uint16_t*)d_in[5]; p.wd = (const uint16_t*)d_in[6];
    p.out = (uint16_t*)d_out; p.ws = (unsigned char*)d_ws;
    (void)hipFuncSetAttribute((const void*)k_fused, hipFuncAttributeMaxDynamicSharedMemorySize, LDS_BYTES);
    k_fused<<<256, NT, LDS_BYTES, stream>>>(p);
}
```

```cpp
#define OUT_NT
#include <hip/hip_runtime.h>
#include <stdint.h>

#define LAS __attribute__((address_space(3)))
#define GAS __attribute__((address_space(1)))
#define DI __device__ __forceinline__
typedef __bf16 bf8 __attribute__((ext_vector_type(8)));
typedef __bf16 bf2 __attribute__((ext_vector_type(2)));
typedef short s16x4 __attribute__((ext_vector_type(4)));
typedef short s16x8 __attribute__((ext_vector_type(8)));
typedef float f32x4 __attribute__((ext_vector_type(4)));
typedef float f32x2 __attribute__((ext_vector_type(2)));
typedef unsigned u32x4 __attribute__((ext_vector_type(4)));
typedef unsigned u32x2 __attribute__((ext_vector_type(2)));
typedef GAS unsigned gu32;
typedef GAS unsigned long long gu64;
#define RLX_AGENT __ATOMIC_RELAXED, __HIP_MEMORY_SCOPE_AGENT

namespace tabs {
constexpr double PI = 3.14159265358979323846264338327950288;
constexpr double cos_q(double x) {
    double x2 = x * x, t = 1.0, s = 1.0;
    for (int i = 1; i <= 14; ++i) { t *= -x2 / ((2 * i - 1) * (2 * i)); s += t; }
    return s;
}
constexpr uint16_t f2bf(float f) {
    uint32_t u = __builtin_bit_cast(uint32_t, f);
    u += 0x7FFFu + ((u >> 16) & 1u);
    return (uint16_t)(u >> 16);
}
struct Base { uint16_t v[512];
    constexpr Base() : v{} {
        for (int m = 0; m <= 128; ++m) {
            double c = (m == 128) ? 0.0 : cos_q(2.0 * PI * m / 512.0);
            uint16_t b = f2bf((float)c);
            uint16_t nb = (m == 128) ? b : (uint16_t)(b ^ 0x8000u);
            v[m] = b; v[(512 - m) & 511] = b;
            v[256 - m] = nb; v[(256 + m) & 511] = nb;
        }
    }
};
constexpr Base base{};
constexpr uint16_t cosb(int m) { return base.v[m & 511]; }
constexpr uint16_t nsinb(int m) { return base.v[(m + 128) & 511]; }
constexpr uint16_t sinb(int m) { return base.v[(m + 384) & 511]; }
struct Chunk { uint16_t v[16384]; };
constexpr Chunk mk_fwd(int c) {
    Chunk r{};
    for (int d = 0; d < 512; ++d)
        for (int n = 0; n < 32; ++n) { const int k = 16 * c + (n & 15);
            r.v[d * 32 + n] = base.v[((n >> 4) ? (k == 0 ? d * 256 : d * k + 128) : d * k) & 511]; }
    return r;
}
constexpr Chunk mk_inv(int blk) {
    Chunk r{};
    const int cc = blk >> 1;
    for (int e = 0; e < 16384; e += 8) {
        const int l = (e >> 3) & 63, dt = (e >> 9) & 3, kg = (e >> 11) & 3, s = ((blk & 1) << 1) + (e >> 13);
        const int kt = 4 * kg + s, d = 64 * cc + 16 * dt + (l & 15), k0 = 16 * kt + 4 * (l >> 4);
        for (int j = 0; j < 4; ++j) { const int k = k0 + j;
            r.v[e + j] = base.v[(k * d) & 511];
            r.v[e + 4 + j] = base.v[(k == 0 ? d * 256 : k * d + 128) & 511]; }
    }
    return r;
}
template <int I> constexpr Chunk fwd_c = mk_fwd(I);
template <int I> constexpr Chunk inv_c = mk_inv(I);
struct Big { Chunk c[16]; };
}
#define C16(N) {{tabs::N<0>, tabs::N<1>, tabs::N<2>, tabs::N<3>, tabs::N<4>, tabs::N<5>, tabs::N<6>, tabs::N<7>, \
                 tabs::N<8>, tabs::N<9>, tabs::N<10>, tabs::N<11>, tabs::N<12>, tabs::N<13>, tabs::N<14>, tabs::N<15>}}
alignas(256) static __device__ const tabs::Big g_fwd = C16(fwd_c);
alignas(256) static __device__ const tabs::Big g_inv = C16(inv_c);

constexpr int S = 2048, D = 512, F = 1792, DF = 257, NT = 512;
constexpr size_t WS_CTRL = 0;
constexpr size_t WS_STAT = 98304;
constexpr size_t WS_AP = 131072;
constexpr size_t WS_AX = WS_AP + (2u << 20);
constexpr size_t WS_TX = WS_AX + 8192;
constexpr size_t WS_T = WS_AX + 65536;
constexpr size_t WS_H = WS_T + (2u << 20);
constexpr size_t WS_H2 = WS_H + (size_t)16 * 8 * 48 * 1024;
constexpr size_t WS_END = WS_H2 + (size_t)16 * 8 * 16 * 512;
#define FSTR 32
#define PSLEEP 8
constexpr int W_CT = 1024, W_TMO = 2048, W_FA = 4096, W_FH = W_FA + 256 * 32;
constexpr unsigned SPIN_CAP = 1u << 16;
constexpr int CODE_PF_BYTES = 37 * 1024;

struct Params {
    const uint16_t* x; const uint16_t* wr; const uint16_t* wi;
    const uint16_t* wg; const uint16_t* wu; const uint16_t* wd;
    uint16_t* out; unsigned char* ws;
};

template <int I> struct IC { static constexpr int v = I; };
template <int B, int E, class Fn> DI void static_for(Fn&& f) { if constexpr (B < E) { f(IC<B>{}); static_for<B + 1, E>(f); } }
DI f32x4 mfma(u32x4 a, u32x4 b, f32x4 c) {
    return __builtin_amdgcn_mfma_f32_16x16x32_bf16(__builtin_bit_cast(bf8, a), __builtin_bit_cast(bf8, b), c, 0, 0, 0);
}
DI unsigned pk_bf16(float lo, float hi) { bf2 p; p[0] = (__bf16)lo; p[1] = (__bf16)hi; return __builtin_bit_cast(unsigned, p); }
DI float bf_lo(unsigned u) { return __builtin_bit_cast(float, u << 16); }
DI float bf_hi(unsigned u) { return __builtin_bit_cast(float, u & 0xffff0000u); }
DI float bfr(float x) { return bf_lo(pk_bf16(x, x)); }
DI u32x4 lds128(LAS unsigned char* L, int off) { return *(LAS u32x4*)(L + off); }
DI u32x2 lds_tr(LAS unsigned char* L, int off) {
    return __builtin_bit_cast(u32x2, __builtin_amdgcn_ds_read_tr16_b64_v4i16((LAS s16x4*)(L + off)));
}
DI u32x4 lds_tr2(LAS unsigned char* L, int off0, int off1) {
    const u32x2 a = lds_tr(L, off0), b = lds_tr(L, off1);
    return u32x4{a.x, a.y, b.x, b.y};
}
DI u32x4 swap_neg(u32x4 v) { return u32x4{v.z, v.w, v.x ^ 0x80008000u, v.y ^ 0x80008000u}; }
DI u32x4 pack8(const float (&a)[4], const float (&b)[4]) { return u32x4{pk_bf16(a[0], a[1]), pk_bf16(a[2], a[3]), pk_bf16(b[0], b[1]), pk_bf16(b[2], b[3])}; }
DI u32x4 pack8(f32x4 a, f32x4 b) { return u32x4{pk_bf16(a[0], a[1]), pk_bf16(a[2], a[3]), pk_bf16(b[0], b[1]), pk_bf16(b[2], b[3])}; }
DI void glds16(const void* gsrc, unsigned lds_addr) {
    asm volatile("s_mov_b32 m0, %1\n\ts_nop 0\n\tglobal_load_lds_dwordx4 %0, off" :: "v"(gsrc), "s"(lds_addr) : "memory");
}
DI void glds16s(const void* sbase, unsigned voff, unsigned lds_addr) {
    asm volatile("s_mov_b32 m0, %2\n\ts_nop 0\n\tglobal_load_lds_dwordx4 %0, %1" :: "v"(voff), "s"(sbase), "s"(lds_addr) : "memory");
}
DI void glds4(const void* gsrc, unsigned lds_addr) {
    asm volatile("s_mov_b32 m0, %1\n\ts_nop 0\n\tglobal_load_lds_dword %0, off" :: "v"(gsrc), "s"(lds_addr) : "memory");
}
DI void gl128(u32x4& d, const void* p) { asm volatile("global_load_dwordx4 %0, %1, off" : "=v"(d) : "v"(p) : "memory"); }
DI void gl128_sc1(u32x4& d, const void* base, unsigned off) { asm volatile("global_load_dwordx4 %0, %1, %2 sc1" : "=v"(d) : "v"(off), "s"(base) : "memory"); }
DI void gl64_sc1(u32x2& d, const void* base, unsigned off) { asm volatile("global_load_dwordx2 %0, %1, %2 sc1" : "=v"(d) : "v"(off), "s"(base) : "memory"); }
DI void gl32_sc1(unsigned& d, const void* base, unsigned off) { asm volatile("global_load_dword %0, %1, %2 sc1" : "=v"(d) : "v"(off), "s"(base) : "memory"); }
DI void gl16(unsigned& d, const void* base, unsigned off) { asm volatile("global_load_ushort %0, %1, %2" : "=v"(d) : "v"(off), "s"(base) : "memory"); }
template <int N> DI void wait_vm() { static_assert(N >= 0 && N < 64, "vmcnt"); asm volatile("s_waitcnt vmcnt(%0)" :: "n"(N) : "memory"); }
template <class T> DI void pin(T& v) { asm volatile("" : "+v"(v)); }
#define WAIT_LGKM0() asm volatile("s_waitcnt lgkmcnt(0)" ::: "memory")
#define BAR() do { asm volatile("" ::: "memory"); __builtin_amdgcn_s_barrier(); asm volatile("" ::: "memory"); } while (0)
#define SB() __builtin_amdgcn_sched_barrier(0)
DI void st_sc1(__amdgpu_buffer_rsrc_t rs, int off, u32x4 v) { __builtin_amdgcn_raw_buffer_store_b128(v, rs, off, 0, 16); }
DI void st_sc1_64(__amdgpu_buffer_rsrc_t rs, int off, u32x2 v) { __builtin_amdgcn_raw_buffer_store_b64(v, rs, off, 0, 16); }
template <int CTRL> DI float dpp_add(float v) {
    return v + __builtin_bit_cast(float, __builtin_amdgcn_update_dpp(0, __builtin_bit_cast(int, v), CTRL, 0xf, 0xf, true));
}
DI float wave_sum(float v) {
    v = dpp_add<0xB1>(v);
    v = dpp_add<0x4E>(v);
    v = dpp_add<0x141>(v);
    v = dpp_add<0x140>(v);
    const int vi = __builtin_bit_cast(int, v);
    return (__builtin_bit_cast(float, __builtin_amdgcn_readlane(vi, 0)) + __builtin_bit_cast(float, __builtin_amdgcn_readlane(vi, 16))) +
           (__builtin_bit_cast(float, __builtin_amdgcn_readlane(vi, 32)) + __builtin_bit_cast(float, __builtin_amdgcn_readlane(vi, 48)));
}
DI void give_up(const unsigned char* ws, int lane) { if (lane == 0) __hip_atomic_store((gu32*)ws + W_TMO, 1u, RLX_AGENT); }
DI void poll16(const unsigned char* ws, int word, unsigned tag, int lane) {
#pragma unroll 1
    for (unsigned spins = 0;; ++spins) {
        unsigned f; gl32_sc1(f, ws, (unsigned)(word + (lane & 15) * FSTR) * 4u); wait_vm<0>(); pin(f);
        if (__all(f == tag)) break;
        if (spins > SPIN_CAP) { give_up(ws, lane); break; }
        __builtin_amdgcn_s_sleep(PSLEEP);
    }
}
DI void fx_load(unsigned (&w)[8], unsigned o0, unsigned o1, unsigned o2, unsigned o3, const void* wr, const void* wi, int k4) {
    asm volatile("s_cmp_lg_u32 %14, 0\n\ts_cbranch_scc1 .Lnofx%=\n\t"
                 "global_load_ushort %0, %8, %12\n\tglobal_load_ushort %1, %9, %12\n\tglobal_load_ushort %2, %10, %12\n\tglobal_load_ushort %3, %11, %12\n\t"
                 "global_load_ushort %4, %8, %13\n\tglobal_load_ushort %5, %9, %13\n\tglobal_load_ushort %6, %10, %13\n\tglobal_load_ushort %7, %11, %13\n"
                 ".Lnofx%=:" : "=&v"(w[0]), "=&v"(w[1]), "=&v"(w[2]), "=&v"(w[3]), "=&v"(w[4]), "=&v"(w[5]), "=&v"(w[6]), "=&v"(w[7])
                 : "v"(o0), "v"(o1), "v"(o2), "v"(o3), "s"(wr), "s"(wi), "s"(k4) : "memory", "scc");
}
DI float cosr(float x) { return __builtin_amdgcn_cosf(x); }
DI float sinr(float x) { return __builtin_amdgcn_sinf(x); }
DI u32x4 frag_cs(int a, int b0, int n) {
    float c[4], sn[4];
#pragma unroll
    for (int j = 0; j < 4; ++j) { const float th = (float)((a * (b0 + j)) & (n - 1)) * (1.0f / n); c[j] = cosr(th); sn[j] = -sinr(th); }
    return pack8(c, sn);
}

#define STAMP(i) do {} while (0)
#define TS(i) do {} while (0)

constexpr int P1_STAGE = 49152, P1_BOFF = 16384;
constexpr int L_MISC = 147456;
constexpr int L_XI = L_MISC;
constexpr int L_ST = L_MISC + 8704;
constexpr int L_YX = L_ST + 1024;
constexpr int L_SCR = L_YX + 4096;
constexpr int L_FLG = L_SCR + 256;
constexpr int L_END = L_FLG + 16;
static_assert(L_END <= 163840, "lds");
constexpr int LDS_BYTES = 163840;
constexpr int L_RAW = 98304;
constexpr int P3_SLOT = 16384;
DI constexpr int p3_slot(int s) { return (s % 9) * P3_SLOT; }

DI int panel_row(int r, int m) { return 64 * (m & 31) + 4 * r + (m >> 5); }
DI int p3_ks(int s, int kg) { const int n = 4 * s + kg; if (s >= 12) return 7 * (n - 48) + 3; const int p = n / 6, o = n - 6 * p; return 7 * p + o + (o >= 3); }

struct LdSrc { unsigned a[4]; const unsigned char* b[8]; long sB64; const unsigned char* t; long tadv; };
DI void p1_issue(LdSrc& s, const unsigned char* x, unsigned Lb, int lw, int j) {
    const unsigned sb = Lb + (j % 3) * P1_STAGE;
#pragma unroll
    for (int i = 0; i < 4; ++i) glds16s(x + j * 128, s.a[i], sb + (4 * lw + i) * 1024);
#pragma unroll
    for (int u = 0; u < 8; ++u) { const int i = (lw & 1) + 2 * (u & 1) + 4 * (lw >> 1) + 8 * ((u >> 1) & 1) + 16 * (u >> 2);
        glds16(s.b[u], sb + P1_BOFF + i * 1024); s.b[u] += s.sB64; }
}
DI void p1_touch(LdSrc& s, unsigned Lb) { glds4(s.t, Lb + L_SCR); s.t += s.tadv; }
DI void p3_issue(const Params& p, unsigned Lb, int lw, int cc, int s, unsigned wv, unsigned fv) {
    const unsigned sb = Lb + p3_slot(s) + lw * 4096;
    if (s < 14) { const unsigned char* base = (const unsigned char*)p.wd + (size_t)p3_ks(s, lw) * 32768;
#pragma unroll
        for (int i = 0; i < 4; ++i) glds16s(base + i * 8192, wv, sb + i * 1024); }
    else { const unsigned char* base = (const unsigned char*)&g_inv + cc * 65536 + (s - 14) * 16384 + lw * 4096;
#pragma unroll
        for (int i = 0; i < 4; ++i) glds16s(base + i * 1024, fv, sb + i * 1024); }
}

DI void p3_issue2(const Params& p, unsigned Lb, int w, int cc, int s, unsigned wv, unsigned fv) {
    const unsigned sb = Lb + p3_slot(s) + w * 2048;
    if (s < 14) { const unsigned char* base = (const unsigned char*)p.wd + (size_t)p3_ks(s, w >> 1) * 32768 + (w & 1) * 16384;
        glds16s(base, wv, sb); glds16s(base + 8192, wv, sb + 1024); }
    else { const unsigned char* base = (const unsigned char*)&g_inv + cc * 65536 + (s - 14) * 16384 + w * 2048;
        glds16s(base, fv, sb); glds16s(base + 1024, fv, sb + 1024); }
}

template <bool DFTW>
DI void p1_compute(LAS unsigned char* L, unsigned Lb, int wc, int lane, f32x4 (&acc)[4][8], f32x4 (&csm)[4], unsigned (&tsv)[32]) {
    asm volatile("" : "+v"(lane));
    const int fr = lane & 15, g = lane >> 4;
    const int qq = fr >> 2, p = lane & 3;
    int aoff0 = fr * 128 + (((0 + g) ^ (fr & 7)) << 4);
    int aoff1 = fr * 128 + (((4 + g) ^ (fr & 7)) << 4);
    const int sw = 2 * qq + 8 * (g & 1);
    int boff[4];
#pragma unroll
    for (int q = 0; q < 4; ++q) boff[q] = P1_BOFF + (8 * g + qq) * 512 + (((8 * wc + 2 * q + (p >> 1)) ^ sw) << 4) + 8 * (p & 1);
    u32x4 xa[2][4], wb[2][4];
    auto rdx = [&](int ks, int mh, int i) { xa[mh][i] = lds128(L, (ks ? aoff1 : aoff0) + (4 * mh + i) * 2048); };
    auto rdw = [&](int ks, int q) { const int o = boff[q] + ks * 16384; wb[ks][q] = lds_tr2(L, o, o + 2048); };
    auto rebase = [&](int d) { aoff0 += d; aoff1 += d; boff[0] += d; boff[1] += d; boff[2] += d; boff[3] += d;
        asm volatile("" : "+v"(aoff0), "+v"(aoff1), "+v"(boff[0]), "+v"(boff[1]), "+v"(boff[2]), "+v"(boff[3])); };
    auto step = [&](int ks, int mh, bool more) {
#pragma unroll
        for (int i = 0; i < 16; ++i) {
            const int q = i >> 2, mt = i & 3;
            if (DFTW && q >= 2) acc[q][4 * mh + mt] = mfma(xa[mh][mt], wb[ks][q], acc[q][4 * mh + mt]);
            else acc[q][4 * mh + mt] = mfma(wb[ks][q], xa[mh][mt], acc[q][4 * mh + mt]);
            if (more) {
                if (mh == 0) { if (i >= 1 && i <= 4) rdx(ks, 1, i - 1); }
                else { if (i < 4) rdx(ks ^ 1, 0, i); else if (i < 8) rdw(ks ^ 1, i - 4); }
            }
            if (mh == 0 && (i & 3) == 3) { const int d = i >> 2; if (!DFTW || d < 2) csm[d] = mfma(wb[ks][d], u32x4{0x3F803F80u, 0x3F803F80u, 0x3F803F80u, 0x3F803F80u}, csm[d]); }
            SB();
        }
    };
    BAR();
    TS(0);
#pragma unroll
    for (int i = 0; i < 4; ++i) { rdx(0, 0, i); rdw(0, i); }
    SB();
    static_for<0, 8>([&](auto J) {
        constexpr int j = J.v;
        step(0, 0, true);
        step(0, 1, true);
        step(1, 0, true);
        if constexpr (j < 7) { WAIT_LGKM0(); BAR(); TS(j + 1); rebase((j + 1) % 3 == 0 ? -2 * P1_STAGE : P1_STAGE);
            SB(); }
        step(1, 1, j < 7);
    });
    TS(8);
}

constexpr int HD = 5;
constexpr int FTS = 10, FTC = 12;
constexpr int p3_issue_at(int o) { return o < 14 ? o - HD : FTC; }
constexpr int p3_opcnt(int o) { return (o >= 12 && o <= 14) ? 4 : 2; }
enum { K_OP, K_TST, K_W, K_FT };
constexpr int p3_after(int kind, int idx, int s_now) {
    int n = -1;
    auto hit = [&](bool is, int cnt) { if (is) n = 0; else if (n >= 0) n += cnt; };
    for (int o = 0; o < HD; ++o) hit(kind == K_OP && idx == o, 2);
    hit(kind == K_TST, 1); hit(false, 1);
    for (int s = 0; s < s_now; ++s) {
        for (int o = HD; o < 18; ++o) if (p3_issue_at(o) == s) hit(kind == K_OP && idx == o, p3_opcnt(o));
        if (s >= 3 && s + 6 < 18) hit(kind == K_W && idx == s + 6, 2);
        if (s == FTS) hit(kind == K_FT, 1);
    }
    return n;
}
constexpr int cmin(int a, int b) { return a < b ? a : b; }
constexpr int p3_wait(int s) {
    int n = p3_after(K_OP, s, s);
    if (s == 3) n = cmin(n, p3_after(K_TST, 0, s));
    if (s % 3 == 0 && s >= 9) n = cmin(n, p3_after(K_W, s + 2, s));
    if (s == FTC) n = cmin(n, p3_after(K_FT, 0, s));
    return n;
}
static_assert(p3_wait(0) == 2 * (HD - 1) + 2 && p3_wait(3) == 1 + 6, "queue model");

struct P3Ctx { unsigned hbase, h2base, tbase; int woff[4]; };
DI void p3_rdw(LAS unsigned char* L, const P3Ctx& c, int kg, int lane, int s, u32x4 (&wf)[4]) {
#pragma unroll
    for (int dt = 0; dt < 4; ++dt) {
        if (s < 14) wf[dt] = lds_tr2(L, p3_slot(s) + c.woff[dt], p3_slot(s) + c.woff[dt] + 2048);
        else wf[dt] = lds128(L, p3_slot(s) + (kg * 4 + dt) * 1024 + lane * 16);
    }
}

__global__ void __launch_bounds__(NT, 2) k_fused(Params p) {
    extern __shared__ __attribute__((aligned(16))) unsigned char smem[];
    LAS unsigned char* L = (LAS unsigned char*)smem;
    const unsigned Lb = (unsigned)(size_t)L;
    const int wid = __builtin_amdgcn_readfirstlane(threadIdx.x >> 6);
    const int wc = wid & 3;
    const int b = blockIdx.x, xcd = b & 7, bi = b >> 3;
    const int r = (xcd & 1) * 8 + (bi & 7), c = (xcd >> 1) * 4 + (bi >> 3);
    const int jq = b & 7, kq = b >> 3;
    const int cc = (xcd & 1) * 4 + (bi & 3), hp = (xcd >> 1) * 8 + (bi >> 2), r3 = hp >> 1, h3 = hp & 1;
    const unsigned char* ws = p.ws;
    unsigned tsv[32];
#pragma unroll
    for (int i = 0; i < 32; ++i) tsv[i] = 0;
    STAMP(0);
    TS(31);
    const int odd = c & 1;
    const int u2 = wid >> 2, w2 = wid & 3, k4 = 2 * kq + u2;
    LAS f32x2* ST = (LAS f32x2*)(L + L_ST);
    f32x4 acc[4][8], csm[4];
    u32x4 eaC[2], eaS[2]; float tcA[2][4], tsA[2][4];
#pragma unroll
    for (int i = 0; i < 4; ++i) csm[i] = f32x4{0.f, 0.f, 0.f, 0.f};
    unsigned tag;
    {
    const int tid = threadIdx.x, lane = tid & 63;
    if (wid >= 4) {
        const int lw = wid - 4;
        unsigned epoch; gl32_sc1(epoch, ws, 0u);
        LdSrc ls;
        {
            const int idx = lw * 22 + (lane < 22 ? lane : 0);
            if (idx < 32) { ls.t = (const unsigned char*)p.x + (size_t)panel_row(r, 32 * (c & 3) + idx) * 1024; ls.tadv = 128; }
            else { const int i = idx - 32, row = 8 * (r & 7) + i / 7, it = i % 7;
                if (it == 6) { ls.t = (const unsigned char*)&g_fwd + c * 32768 + row * 64; ls.tadv = 64 * 64; }
                else { const int it3 = it % 3; ls.t = (const unsigned char*)(it < 3 ? p.wg : p.wu) + (size_t)row * (F * 2) + c * 224 + (it3 == 2 ? 220 : it3 * 128); ls.tadv = 64L * F * 2; } }
        }
        p1_touch(ls, Lb);
        SB();
#pragma unroll
        for (int i = 0; i < 4; ++i) { const int m = 8 * (4 * lw + i) + (lane >> 3); ls.a[i] = (unsigned)panel_row(r, m) * 1024u + (((lane & 7) ^ (lane >> 3)) << 4); }
        {
            const int b0 = lw & 1, b2 = lw >> 1;
            const int swz = 2 * (2 * b0 + (lane >> 5)) + 8 * b2;
            const int ch = (lane & 31) ^ swz, nt = ch >> 1, half = ch & 1, cw = nt >> 2, q = nt & 3;
            const unsigned char* base; int col, strideB;
            if (cw < 3) { const int ft = odd + 2 * cw + (q & 1); base = (const unsigned char*)(q < 2 ? p.wg : p.wu); col = 112 * c + 16 * ft + 8 * half; strideB = F * 2; }
            else if (q < 2) { const int ft = odd ? 0 : 6; base = (const unsigned char*)(q == 0 ? p.wg : p.wu); col = 112 * c + 16 * ft + 8 * half; strideB = F * 2; }
            else { base = (const unsigned char*)&g_fwd + c * 32768; col = 16 * (q - 2) + 8 * half; strideB = 64; }
            const unsigned char* b00 = base + (size_t)(2 * b0 + 8 * b2 + (lane >> 5)) * strideB + col * 2;
#pragma unroll
            for (int u = 0; u < 8; ++u) ls.b[u] = b00 + (size_t)(4 * (u & 1) + 16 * ((u >> 1) & 1) + 32 * (u >> 2)) * strideB;
            ls.sB64 = (long)strideB * 64;
        }
        p1_issue(ls, (const unsigned char*)p.x, Lb, lw, 0);
        p1_touch(ls, Lb); p1_touch(ls, Lb); p1_touch(ls, Lb);
        p1_issue(ls, (const unsigned char*)p.x, Lb, lw, 1);
        STAMP(14);
        if (tid == 256) { *(volatile LAS unsigned*)(L + L_FLG + 4) = 0u; *(volatile LAS unsigned*)(L + L_FLG + 8) = 0u; *(volatile LAS unsigned*)(L + L_FLG + 12) = 0u; }
        wait_vm<15>(); pin(epoch); tag = epoch + 1u;
        if (r == 0 && tid == 256) __hip_atomic_store((gu32*)p.ws + W_CT + c * 32, tag << 8, RLX_AGENT);
        WAIT_LGKM0();
        STAMP(15);
        BAR();
        p1_touch(ls, Lb);
        p1_issue(ls, (const unsigned char*)p.x, Lb, lw, 2);
        SB();
        const unsigned char *st0, *st1;
        {
            auto wline = [&](int i) { const int st = i >> 2, ln = (bi >> 2) * 16 + lw * 4 + (i & 3);
                return st < 14 ? (const unsigned char*)p.wd + (size_t)(32 * p3_ks(st, ln >> 5) + (ln & 31)) * 1024 + cc * 128
                               : (const unsigned char*)&g_inv + (size_t)cc * 65536 + (st - 14) * 16384 + ln * 128; };
            st0 = wline(lane);
            const int fi = (bi * 4 + lw) * 20 + (lane >= 8 && lane < 28 ? lane - 8 : 0);
            const int arr = fi / 1280, rr = (fi % 1280) / 5, ll = (fi % 1280) % 5;
            const int jrow = 4 * jq + (rr & 3) + 32 * (rr >> 2);
            st1 = lane < 8 ? wline(64 + lane) : (const unsigned char*)(arr ? p.wi : p.wr) + (size_t)jrow * (DF * 2) + min(ll * 128, 510);
        }
        u32x2 gran; const unsigned goff = (unsigned)WS_STAT + (unsigned)(r * 128 + (tid & 127)) * 8u;
        static_for<0, 7>([&](auto J) {
            constexpr int j = J.v;
            if constexpr (j < 4) wait_vm<13>(); else if constexpr (j == 4) wait_vm<12>();
            else if constexpr (j == 5) wait_vm<15>(); else wait_vm<3>();
            BAR();
            if constexpr (j < 3) p1_touch(ls, Lb);
            if constexpr (j + 3 < 8) p1_issue(ls, (const unsigned char*)p.x, Lb, lw, j + 3);
            if constexpr (j == 4) {
                gl64_sc1(gran, ws, goff);
                glds4(st0, Lb + L_SCR); glds4(st1, Lb + L_SCR);
            }
        });
        wait_vm<2>();
        pin(gran);
        if (lw < 2) {
            if (!__all(gran.y == tag)) {
#pragma unroll 1
                for (unsigned spins = 0;; ++spins) {
                    gl64_sc1(gran, ws, goff); wait_vm<0>(); pin(gran);
                    if (__all(gran.y == tag)) break;
                    if (spins > SPIN_CAP) { give_up(ws, lane); break; }
                    __builtin_amdgcn_s_sleep(1);
                }
            }
            ST[tid & 127] = f32x2{bf_hi(gran.x), 1.0f / bf_lo(gran.x)};
        }
        {
            const int fr = lane & 15, g = lane >> 4;
#pragma unroll
            for (int jt = 0; jt < 2; ++jt) {
                float cv[8], sv[8];
#pragma unroll
                for (int j = 0; j < 8; ++j) { const float th = (float)(((16 * jt + fr) * (8 * g + j)) & 31) * (1.0f / 32.0f); cv[j] = cosr(th); sv[j] = sinr(th); }
                eaC[jt] = u32x4{pk_bf16(cv[0], cv[1]), pk_bf16(cv[2], cv[3]), pk_bf16(cv[4], cv[5]), pk_bf16(cv[6], cv[7])};
                eaS[jt] = u32x4{pk_bf16(sv[0], sv[1]), pk_bf16(sv[2], sv[3]), pk_bf16(sv[4], sv[5]), pk_bf16(sv[6], sv[7])};
#pragma unroll
                for (int e = 0; e < 4; ++e) { const float th = (float)((16 * jt + fr) * (4 * r + e)) * (1.0f / 2048.0f); tcA[jt][e] = cosr(th); tsA[jt][e] = sinr(th); }
            }
        }
#pragma unroll
        for (int q = 0; q < 4; ++q)
#pragma unroll
            for (int mt = 0; mt < 8; ++mt) acc[q][mt] = f32x4{0.f, 0.f, 0.f, 0.f};
    } else {
        if (wid == 0) {
            const unsigned char* pc = (const unsigned char*)__builtin_amdgcn_s_getpc();
            const int ln = bi * 10 + (lane < 10 ? lane : 0);
            glds4(pc - ((size_t)pc & 127) + (size_t)min(ln * 128, CODE_PF_BYTES), Lb + L_SCR);
        }
        unsigned epoch; gl32_sc1(epoch, ws, 0u);
        u32x4 xrow[2];
#pragma unroll
        for (int i = 0; i < 2; ++i) gl128(xrow[i], (const unsigned char*)p.x + (size_t)panel_row(r, 8 * c + 2 * wid + i) * 1024 + lane * 16);
        wait_vm<0>(); pin(epoch); pin(xrow[0]); pin(xrow[1]);
        tag = epoch + 1u;
#pragma unroll
        for (int i = 0; i < 2; ++i) {
            float v[8];
#pragma unroll
            for (int e = 0; e < 4; ++e) { v[2 * e] = bf_lo(xrow[i][e]); v[2 * e + 1] = bf_hi(xrow[i][e]); }
            float s = ((v[0] + v[1]) + (v[2] + v[3])) + ((v[4] + v[5]) + (v[6] + v[7]));
            s = wave_sum(s);
            const float mean = s * (1.0f / 512.0f);
            float q2 = 0.f;
#pragma unroll
            for (int e = 0; e < 8; ++e) { const float d = v[e] - mean; q2 += d * d; }
            q2 = wave_sum(q2);
            const float var = q2 * (1.0f / 512.0f);
            const float m_bf = bfr(mean), v_bf = bfr(var), s_bf = bfr(sqrtf(bfr(v_bf + 1e-5f)));
            const unsigned val = (__builtin_bit_cast(unsigned, m_bf) & 0xffff0000u) | (__builtin_bit_cast(unsigned, s_bf) >> 16);
            if (lane == 0) __hip_atomic_store((gu64*)(p.ws + WS_STAT) + (r * 128 + 8 * c + 2 * wid + i), ((unsigned long long)tag << 32) | val, RLX_AGENT);
        }
#pragma unroll
        for (int q = 0; q < 4; ++q)
#pragma unroll
            for (int mt = 0; mt < 8; ++mt) acc[q][mt] = f32x4{0.f, 0.f, 0.f, 0.f};
        STAMP(1);
        if (wid < 3) p1_compute<false>(L, Lb, wc, lane, acc, csm, tsv); else p1_compute<true>(L, Lb, wc, lane, acc, csm, tsv);
        STAMP(2);
    }
    }
    int tz = threadIdx.x; asm volatile("" : "+v"(tz));
    const int tid = tz, lane = tz & 63, fr = lane & 15, g = lane >> 4;
    const __amdgpu_buffer_rsrc_t rs = __builtin_amdgcn_make_buffer_rsrc((void*)p.ws, 0, (int)WS_END, 0x00020000);
    const unsigned wv = (unsigned)(lane >> 3) * 1024u + cc * 128u + (unsigned)(((lane & 7) ^ (2 * ((lane >> 4) & 3))) << 4), fv = lane * 16u;
    volatile LAS unsigned* flg = (volatile LAS unsigned*)(L + L_FLG);
    WAIT_LGKM0(); BAR();
    STAMP(3);
    const int fj1 = 4 * jq + (fr & 3), fk = 4 * k4 + (fr >> 2);
    unsigned fw[4], fwi[4];
    auto signal = [&](int cnt, int word) {
        if (lane == 0) { const unsigned old = __hip_atomic_fetch_add((LAS unsigned*)(L + L_FLG) + cnt, 1u, __ATOMIC_RELAXED, __HIP_MEMORY_SCOPE_WORKGROUP);
            if (old == 3u) __hip_atomic_store((gu32*)p.ws + word, tag, RLX_AGENT); }
    };
    if (wid < 3) {
        const f32x4 cg0 = csm[0], cg1 = csm[1], cu0 = csm[2], cu1 = csm[3];
#pragma unroll
        for (int mt = 0; mt < 8; ++mt) {
            const f32x2 st = ST[16 * mt + fr];
            const float r2 = st.y * st.y;
            const f32x4 h0 = (acc[0][mt] - st.x * cg0) * (acc[2][mt] - st.x * cu0) * r2;
            const f32x4 h1 = (acc[1][mt] - st.x * cg1) * (acc[3][mt] - st.x * cu1) * r2;
            st_sc1(rs, (int)WS_H + ((((r * 8 + mt) * 48 + 3 * c + wc) * 64 + lane) << 4), pack8(h0, h1));
        }
    } else if (wid == 3) {
#pragma unroll
        for (int q = 0; q < 2; ++q)
#pragma unroll
            for (int mt = 0; mt < 8; ++mt) *(LAS f32x4*)(L + L_RAW + ((q * 8 + mt) * 64 + lane) * 16) = acc[2 + q][mt];
        WAIT_LGKM0();
        flg[0] = tag;
        const f32x4 cg0 = csm[0], cu0 = csm[1];
#pragma unroll
        for (int mt = 0; mt < 8; ++mt) {
            const f32x2 st = ST[16 * mt + fr];
            const float r2 = st.y * st.y;
            const f32x4 h0 = (acc[0][mt] - st.x * cg0) * (acc[1][mt] - st.x * cu0) * r2;
            st_sc1_64(rs, (int)WS_H2 + ((((r * 8 + mt) * 16 + c) * 64 + lane) << 3), u32x2{pk_bf16(h0[0], h0[1]), pk_bf16(h0[2], h0[3])});
        }
    } else {
        const int mt = wid - 4;
        const int kq_ = fr >> 2, s2l = fr & 3;
        __builtin_amdgcn_s_setprio(3);
#pragma unroll 1
        for (unsigned spins = 0; flg[0] != tag && spins < (1u << 20); ++spins) __builtin_amdgcn_s_sleep(1);
        asm volatile("" ::: "memory");
        u32x4 are, aim;
        {
            const int m0 = 32 * s2l + 8 * g, tl = 2 * s2l + (g >> 1), ln = 4 * mt + kq_ + 32 * (g & 1);
            const f32x4 r0 = *(LAS f32x4*)(L + L_RAW + (tl * 64 + ln) * 16), r1 = *(LAS f32x4*)(L + L_RAW + (tl * 64 + ln + 16) * 16);
            const f32x4 i0 = *(LAS f32x4*)(L + L_RAW + ((8 + tl) * 64 + ln) * 16), i1 = *(LAS f32x4*)(L + L_RAW + ((8 + tl) * 64 + ln + 16) * 16);
            const f32x4 sa = *(LAS f32x4*)(ST + m0), sb = *(LAS f32x4*)(ST + m0 + 2), sc = *(LAS f32x4*)(ST + m0 + 4), sd = *(LAS f32x4*)(ST + m0 + 6);
            const float csf = (c == 0 && mt == 0 && kq_ == 0) ? 512.0f : 0.0f;
            are = u32x4{pk_bf16((r0[0] - sa[0] * csf) * sa[1], (r0[1] - sa[2] * csf) * sa[3]), pk_bf16((r0[2] - sb[0] * csf) * sb[1], (r0[3] - sb[2] * csf) * sb[3]),
                        pk_bf16((r1[0] - sc[0] * csf) * sc[1], (r1[1] - sc[2] * csf) * sc[3]), pk_bf16((r1[2] - sd[0] * csf) * sd[1], (r1[3] - sd[2] * csf) * sd[3])};
            aim = u32x4{pk_bf16(i0[0] * sa[1], i0[1] * sa[3]), pk_bf16(i0[2] * sb[1], i0[3] * sb[3]), pk_bf16(i1[0] * sc[1], i1[1] * sc[3]), pk_bf16(i1[2] * sd[1], i1[3] * sd[3])};
        }
        const f32x4 z = {0.f, 0.f, 0.f, 0.f};
        const bool special = (c == 0 && mt == 0 && g == 0);
#pragma unroll
        for (int jt = 0; jt < 2; ++jt) {
            const f32x4 Prc = mfma(are, eaC[jt], z), Pis = mfma(aim, eaS[jt], z), Prs = mfma(are, eaS[jt], z), Pic = mfma(aim, eaC[jt], z);
            const int j1 = 16 * jt + fr;
            float pr[4], qi[4], pr2[4], qi2[4];
#pragma unroll
            for (int e = 0; e < 4; ++e) {
                const float cw = tcA[jt][e], sw = tsA[jt][e];
                const float ar = special ? Prc[e] : Prc[e] + Pis[e], ai = special ? -Prs[e] : Pic[e] - Prs[e];
                pr[e] = ar * cw + ai * sw; qi[e] = -(ai * cw - ar * sw);
                const float br = Pic[e], bi2 = -Pis[e];
                pr2[e] = br * cw + bi2 * sw; qi2[e] = -(bi2 * cw - br * sw);
            }
            const int U = (j1 >> 2) * 64 + 4 * c + mt, lp = 16 * (r & 3) + 4 * g + (j1 & 3);
            st_sc1(rs, (int)WS_AP + (((U * 4 + (r >> 2)) * 64 + lp) << 4), pack8(pr, qi));
            if (special) st_sc1(rs, (int)WS_AX + (((((j1 >> 2) * 4 + (r >> 2)) * 4 + (r & 3)) * 4 + (j1 & 3)) << 4), pack8(pr2, qi2));
        }
        __builtin_amdgcn_s_setprio(0);
        wait_vm<0>();
        signal(1, W_FA + (c * 16 + r) * FSTR);
#pragma unroll
        for (int st = 0; st < 3; ++st) p3_issue(p, Lb, mt, cc, st, wv, fv);
    }
#pragma unroll
    for (int e = 0; e < 4; ++e) { const unsigned o = (unsigned)((fj1 + 32 * (16 * w2 + 4 * g + e)) * DF + fk) * 2u; gl16(fw[e], p.wr, o); gl16(fwi[e], p.wi, o); }
    unsigned fx[8];
    {
        const unsigned o0 = (unsigned)((fj1 + 32 * (16 * w2 + 4 * g)) * DF + 256) * 2u;
        fx_load(fx, o0, o0 + 32u * DF * 2u, o0 + 64u * DF * 2u, o0 + 96u * DF * 2u, p.wr, p.wi, k4);
    }
    if (wid < 4) {
        if (k4 == 0) wait_vm<16>(); else wait_vm<8>();
        signal(2, W_FH + (r * 16 + c) * FSTR);
#pragma unroll
        for (int st = 3; st < 6; ++st) p3_issue(p, Lb, wid, cc, st, wv, fv);
#pragma unroll 1
        for (unsigned spins = 0; flg[1] < 4u && spins < (1u << 22); ++spins) __builtin_amdgcn_s_sleep(1);
        asm volatile("" ::: "memory");
#pragma unroll
        for (int st = 6; st < 9; ++st) p3_issue(p, Lb, wid, cc, st, wv, fv);
    }
    STAMP(5);
    {
        const int kg = wid & 3, rh = wid >> 2;
        u32x4 E1[4], E2[4];
#pragma unroll
        for (int t = 0; t < 4; ++t) E1[t] = frag_cs(16 * w2 + fr, 16 * t + 4 * g, 64);
#pragma unroll
        for (int i = 0; i < 4; ++i) E2[i] = frag_cs(16 * (i >> 1) + fr, 16 * (i & 1) + 4 * g, 32);
        float tc[4], tsn[4];
#pragma unroll
        for (int e = 0; e < 4; ++e) { const float th = (float)((4 * jq + e) * (16 * w2 + fr)) * (1.0f / 2048.0f); tc[e] = cosr(th); tsn[e] = sinr(th); }
        P3Ctx cx;
        {
            const int qq = fr >> 2, pp = lane & 3, row = 32 * kg + 4 * g + qq, sw = 2 * ((row >> 1) & 3);
#pragma unroll
            for (int dt = 0; dt < 4; ++dt) cx.woff[dt] = row * 128 + (((2 * dt + (pp >> 1)) ^ sw) << 4) + 8 * (pp & 1);
            cx.hbase = (unsigned)WS_H + (unsigned)(((r3 * 8 + 4 * h3 + 2 * rh) * 48 + kg) * 64 + lane) * 16u;
            cx.h2base = (unsigned)WS_H2 + (unsigned)(((r3 * 8 + 4 * h3 + 2 * rh) * 16 + 2 * kg) * 64 + lane) * 8u;
            cx.tbase = (unsigned)WS_T + (unsigned)((((4 * r3 + 2 * h3 + rh) * 16 + 4 * kg) * 2) * 64 + lane) * 16u;
        }
        SB();
        STAMP(6);
        if (wid == 0) poll16(ws, W_FA + (kq >> 1) * 16 * FSTR, tag, lane);
        if (wid == 1) poll16(ws, W_FH + r3 * 16 * FSTR, tag, lane);
        BAR();
        STAMP(7);
        const int U = jq * 64 + k4;
        u32x4 Bf[4], Bx[4];
        u32x4 hb[HD][2]; u32x2 hh[2][4];
#pragma unroll
        for (int t = 0; t < 4; ++t) gl128_sc1(Bf[t], ws, (unsigned)WS_AP + (unsigned)((U * 4 + t) * 64 + lane) * 16u);
        {
            const unsigned xo = (unsigned)WS_AX + (unsigned)((jq * 16 + g) * 4 + (fr & 3)) * 16u;
            asm volatile("s_cmp_lg_u32 %6, 0\n\ts_cbranch_scc1 .Lnox%=\n\t"
                         "global_load_dwordx4 %0, %4, %5 sc1\n\tglobal_load_dwordx4 %1, %4, %5 offset:256 sc1\n\t"
                         "global_load_dwordx4 %2, %4, %5 offset:512 sc1\n\tglobal_load_dwordx4 %3, %4, %5 offset:768 sc1\n"
                         ".Lnox%=:" : "=&v"(Bx[0]), "=&v"(Bx[1]), "=&v"(Bx[2]), "=&v"(Bx[3]) : "v"(xo), "s"(ws), "s"(k4) : "memory", "scc");
        }
        static_for<0, HD>([&](auto Sx) { constexpr int o = Sx.v;
            gl128_sc1(hb[o][0], ws, cx.hbase + o * 4096u); gl128_sc1(hb[o][1], ws, cx.hbase + 48u * 1024u + o * 4096u); });
        wait_vm<2 * HD>();
        pin(Bf[0]); pin(Bf[1]); pin(Bf[2]); pin(Bf[3]); pin(Bx[0]); pin(Bx[1]); pin(Bx[2]); pin(Bx[3]);
        pin(fw[0]); pin(fw[1]); pin(fw[2]); pin(fw[3]); pin(fwi[0]); pin(fwi[1]); pin(fwi[2]); pin(fwi[3]);
        pin(fx[0]); pin(fx[1]); pin(fx[2]); pin(fx[3]); pin(fx[4]); pin(fx[5]); pin(fx[6]); pin(fx[7]);
        STAMP(8);
        {
            f32x4 a1 = {0.f, 0.f, 0.f, 0.f}, a2 = a1;
#pragma unroll
            for (int t = 0; t < 4; ++t) { a1 = mfma(E1[t], Bf[t], a1); a2 = mfma(E1[t], swap_neg(Bf[t]), a2); }
            constexpr float SC = 2.0f / (2048.0f * 512.0f);
            const float sc = (k4 == 0 && (fr >> 2) == 0) ? 0.5f * SC : SC;
            float yr[4], yi[4];
#pragma unroll
            for (int e = 0; e < 4; ++e) { const float xr = a1[e], xi = -a2[e], wre = bf_lo(fw[e]), wim = bf_lo(fwi[e]);
                yr[e] = (xr * wre - xi * wim) * sc; yi[e] = (xr * wim + xi * wre) * sc; }
            if (k4 == 0) {
                f32x4 n1 = {0.f, 0.f, 0.f, 0.f}, n2 = n1;
#pragma unroll
                for (int t = 0; t < 4; ++t) { n1 = mfma(E1[t], Bx[t], n1); n2 = mfma(E1[t], swap_neg(Bx[t]), n2); }
                float xr2[4], xi2[4];
#pragma unroll
                for (int e = 0; e < 4; ++e) { const float xr = n1[e], xi = -n2[e], wre = bf_lo(fx[e]), wim = bf_lo(fx[4 + e]);
                    xr2[e] = (xr * wre - xi * wim) * (0.5f * SC); xi2[e] = (xr * wim + xi * wre) * (0.5f * SC); }
                *(LAS u32x4*)(L + L_YX + (w2 * 64 + lane) * 16) = pack8(xr2, xi2);
            }
            *(LAS u32x4*)(L + L_XI + ((u2 * 4 + w2) * 64 + lane) * 16) = pack8(yr, yi);
        }
        WAIT_LGKM0(); BAR();
        u32x4 wf[2][4];
        p3_rdw(L, cx, kg, lane, 0, wf[0]);
        {
            f32x4 t1 = {0.f, 0.f, 0.f, 0.f}, t2 = t1;
#pragma unroll
            for (int t = 0; t < 4; ++t) { const u32x4 y = lds128(L, L_XI + ((u2 * 4 + t) * 64 + lane) * 16);
                t1 = mfma(y, E1[t], t1); t2 = mfma(swap_neg(y), E1[t], t2); }
            const int s2 = 16 * w2 + fr, ko = 4 * k4 + g;
            float tr[4], ti[4];
#pragma unroll
            for (int e = 0; e < 4; ++e) { tr[e] = t1[e] * tc[e] - t2[e] * tsn[e]; ti[e] = t2[e] * tc[e] + t1[e] * tsn[e]; }
            st_sc1(rs, (int)WS_T + ((((s2 * 16 + (ko >> 4)) * 2 + (jq >> 2)) * 64 + (jq & 3) * 16 + (ko & 15)) << 4), pack8(tr, ti));
            if (k4 == 0) {
                f32x4 x1 = {0.f, 0.f, 0.f, 0.f}, x2 = x1;
#pragma unroll
                for (int t = 0; t < 4; ++t) { const u32x4 y = lds128(L, L_YX + (t * 64 + lane) * 16);
                    x1 = mfma(y, E1[t], x1); x2 = mfma(swap_neg(y), E1[t], x2); }
#pragma unroll
                for (int e = 0; e < 4; ++e) { tr[e] = x1[e] * tc[e] - x2[e] * tsn[e]; ti[e] = x2[e] * tc[e] + x1[e] * tsn[e]; }
                if (g == 0) st_sc1(rs, (int)WS_TX + ((s2 * 8 + jq) << 4), pack8(tr, ti));
            }
        }
        u32x4 xres; gl128(xres, (const unsigned char*)p.x + ((size_t)panel_row(r3, 64 * h3 + (tid >> 3)) * 512 + 64 * cc + 8 * (tid & 7)) * 2);
        STAMP(9);
        f32x4 acc3[4][2];
#pragma unroll
        for (int dt = 0; dt < 4; ++dt) { acc3[dt][0] = f32x4{0.f, 0.f, 0.f, 0.f}; acc3[dt][1] = acc3[dt][0]; }
        unsigned ftf; u32x4 tx[2];
        static_for<0, 14>([&](auto Sx) {
            constexpr int s = Sx.v;
            wait_vm<p3_wait(s)>();
            if constexpr (s == 12 || s == 13) { pin(hh[s - 12][0]); pin(hh[s - 12][1]); pin(hh[s - 12][2]); pin(hh[s - 12][3]); }
            else { pin(hb[s % HD][0]); pin(hb[s % HD][1]); }
            if constexpr (s == FTC) {
                STAMP(12);
                pin(ftf);
                if (wid == 0) {
                    if (!__all(ftf == (tag << 8) + 16u)) {
#pragma unroll 1
                        for (unsigned spins = 0;; ++spins) {
                            __builtin_amdgcn_s_sleep(PSLEEP);
                            unsigned f2; gl32_sc1(f2, ws, (unsigned)(W_CT + (lane & 15) * 32) * 4u); wait_vm<0>(); pin(f2);
                            if (__all(f2 == (tag << 8) + 16u)) break;
                            if (spins > SPIN_CAP) { give_up(ws, lane); break; }
                        }
                    }
                    if (b == 0 && lane == 0) __hip_atomic_store((gu32*)p.ws, tag, RLX_AGENT);
                }
                STAMP(13);
            }
            if constexpr (s % 3 == 0 && s > 0) {
                WAIT_LGKM0(); BAR();
                if constexpr (s == 3) { STAMP(4); if (tid == 0) __hip_atomic_fetch_add((gu32*)p.ws + W_CT + (kq >> 1) * 32, 1u, RLX_AGENT); }
                p3_rdw(L, cx, kg, lane, s, wf[s & 1]);
            }
            TS(10 + s);
            if constexpr (s + 1 < 14 && (s + 1) % 3 != 0) p3_rdw(L, cx, kg, lane, s + 1, wf[(s + 1) & 1]);
            u32x4 b0, b1;
            if constexpr (s == 12 || s == 13) { b0 = u32x4{hh[s - 12][0].x, hh[s - 12][0].y, hh[s - 12][1].x, hh[s - 12][1].y}; b1 = u32x4{hh[s - 12][2].x, hh[s - 12][2].y, hh[s - 12][3].x, hh[s - 12][3].y}; }
            else { b0 = hb[s % HD][0]; b1 = hb[s % HD][1]; }
#pragma unroll
            for (int dt = 0; dt < 4; ++dt) { acc3[dt][0] = mfma(wf[s & 1][dt], b0, acc3[dt][0]); acc3[dt][1] = mfma(wf[s & 1][dt], b1, acc3[dt][1]); }
            SB();
            static_for<HD, 18>([&](auto Ox) {
                constexpr int o = Ox.v;
                if constexpr (p3_issue_at(o) == s) {
                    if constexpr (o < 12) { gl128_sc1(hb[o % HD][0], ws, cx.hbase + o * 4096u); gl128_sc1(hb[o % HD][1], ws, cx.hbase + 48u * 1024u + o * 4096u); }
                    else if constexpr (o < 14) {
                        gl64_sc1(hh[o - 12][0], ws, cx.h2base + (o - 12) * 4096u); gl64_sc1(hh[o - 12][1], ws, cx.h2base + (o - 12) * 4096u + 512u);
                        gl64_sc1(hh[o - 12][2], ws, cx.h2base + 8192u + (o - 12) * 4096u); gl64_sc1(hh[o - 12][3], ws, cx.h2base + 8192u + (o - 12) * 4096u + 512u); }
                    else { gl128_sc1(hb[o % HD][0], ws, cx.tbase + (o - 14) * 2048u); gl128_sc1(hb[o % HD][1], ws, cx.tbase + (o - 14) * 2048u + 1024u);
                        if constexpr (o == 14) { const unsigned txo = (unsigned)WS_TX + (unsigned)((4 * r3 + 2 * h3 + rh) * 8 + g) * 16u; gl128_sc1(tx[0], ws, txo); gl128_sc1(tx[1], ws, txo + 64u); } }
                }
            });
            if constexpr (s >= 3 && s + 6 < 18) p3_issue2(p, Lb, wid, cc, s + 6, wv, fv);
            if constexpr (s == FTS) gl32_sc1(ftf, ws, wid == 0 ? (unsigned)(W_CT + (lane & 15) * 32) * 4u : cx.hbase - lane * 16u);
            SB();
        });
        wait_vm<0>();
        static_for<14, 18>([&](auto Ox) { pin(hb[Ox.v % HD][0]); pin(hb[Ox.v % HD][1]); });
        pin(tx[0]); pin(tx[1]); pin(xres);
        WAIT_LGKM0(); BAR();
        TS(24);
        {
            u32x4 wz[2][4];
            p3_rdw(L, cx, kg, lane, 14, wz[0]); p3_rdw(L, cx, kg, lane, 15, wz[1]);
            u32x4 zf[4][2];
            static_for<0, 4>([&](auto Zx) {
                constexpr int z = Zx.v;
                const u32x4 T0 = hb[(14 + z) % HD][0], T1 = hb[(14 + z) % HD][1];
                u32x4 T0s = swap_neg(T0), T1s = swap_neg(T1);
                if constexpr (z == 0) {
                    if (kg == 0 && fr == 0) { T0s = tx[0]; T1s = tx[1]; }
                }
                f32x4 zr[2], zi[2];
#pragma unroll
                for (int st = 0; st < 2; ++st) { const f32x4 z0 = {0.f, 0.f, 0.f, 0.f}; zr[st] = mfma(T0, E2[st * 2 + 0], z0); zi[st] = mfma(T0s, E2[st * 2 + 0], z0); }
#pragma unroll
                for (int st = 0; st < 2; ++st) { zr[st] = mfma(T1, E2[st * 2 + 1], zr[st]); zi[st] = mfma(T1s, E2[st * 2 + 1], zi[st]); }
#pragma unroll
                for (int st = 0; st < 2; ++st) zf[z][st] = pack8(zr[st], zi[st]);
            });
#pragma unroll
            for (int z = 0; z < 2; ++z)
#pragma unroll
                for (int dt = 0; dt < 4; ++dt) { acc3[dt][0] = mfma(wz[z][dt], zf[z][0], acc3[dt][0]); acc3[dt][1] = mfma(wz[z][dt], zf[z][1], acc3[dt][1]); }
            p3_rdw(L, cx, kg, lane, 16, wz[0]); p3_rdw(L, cx, kg, lane, 17, wz[1]);
#pragma unroll
            for (int z = 0; z < 2; ++z)
#pragma unroll
                for (int dt = 0; dt < 4; ++dt) { acc3[dt][0] = mfma(wz[z][dt], zf[2 + z][0], acc3[dt][0]); acc3[dt][1] = mfma(wz[z][dt], zf[2 + z][1], acc3[dt][1]); }
        }
        STAMP(10);
        TS(25);
        WAIT_LGKM0(); BAR();
        constexpr int RP = 272;
#pragma unroll
        for (int dt = 0; dt < 4; ++dt)
#pragma unroll
            for (int st = 0; st < 2; ++st)
                *(LAS f32x4*)(L + (kg * 64 + 32 * rh + 16 * st + fr) * RP + (16 * dt + 4 * g) * 4) = acc3[dt][st];
        WAIT_LGKM0(); BAR();
        const int mm = tid >> 3, cg = tid & 7;
        f32x4 v0 = {0.f, 0.f, 0.f, 0.f}, v1 = v0;
#pragma unroll
        for (int q = 0; q < 4; ++q) { v0 += *(LAS f32x4*)(L + (q * 64 + mm) * RP + cg * 32); v1 += *(LAS f32x4*)(L + (q * 64 + mm) * RP + cg * 32 + 16); }
        const size_t o = (size_t)panel_row(r3, 64 * h3 + mm) * 512 + 64 * cc + 8 * cg;
        {
            const u32x4 ov = u32x4{pk_bf16(bf_lo(xres.x) + v0[0], bf_hi(xres.x) + v0[1]), pk_bf16(bf_lo(xres.y) + v0[2], bf_hi(xres.y) + v0[3]),
                                   pk_bf16(bf_lo(xres.z) + v1[0], bf_hi(xres.z) + v1[1]), pk_bf16(bf_lo(xres.w) + v1[2], bf_hi(xres.w) + v1[3])};
            __builtin_nontemporal_store(ov, (GAS u32x4*)(p.out + o));
        }
    }
    STAMP(11);
}

extern "C" void kernel_launch(void* const* d_in, const int* in_sizes, int n_in, void* d_out, int out_size, void* d_ws, size_t ws_size,
                              hipStream_t stream) {
    Params p;
    p.x = (const uint16_t*)d_in[0]; p.wr = (const uint16_t*)d_in[1]; p.wi = (const uint16_t*)d_in[2];
    p.wg = (const uint16_t*)d_in[4]; p.wu = (const uint16_t*)d_in[5]; p.wd = (const uint16_t*)d_in[6];
    p.out = (uint16_t*)d_out; p.ws = (unsigned char*)d_ws;
    (void)hipFuncSetAttribute((const void*)k_fused, hipFuncAttributeMaxDynamicSharedMemorySize, LDS_BYTES);
    k_fused<<<256, NT, LDS_BYTES, stream>>>(p);
}
```

```cpp
#define OUT_NT
#include <hip/hip_runtime.h>
#include <stdint.h>

#define LAS __attribute__((address_space(3)))
#define GAS __attribute__((address_space(1)))
#define DI __device__ __forceinline__
typedef __bf16 bf8 __attribute__((ext_vector_type(8)));
typedef __bf16 bf2 __attribute__((ext_vector_type(2)));
typedef short s16x4 __attribute__((ext_vector_type(4)));
typedef short s16x8 __attribute__((ext_vector_type(8)));
typedef float f32x4 __attribute__((ext_vector_type(4)));
typedef float f32x2 __attribute__((ext_vector_type(2)));
typedef unsigned u32x4 __attribute__((ext_vector_type(4)));
typedef unsigned u32x2 __attribute__((ext_vector_type(2)));
typedef GAS unsigned gu32;
typedef GAS unsigned long long gu64;
#define RLX_AGENT __ATOMIC_RELAXED, __HIP_MEMORY_SCOPE_AGENT

namespace tabs {
constexpr double PI = 3.14159265358979323846264338327950288;
constexpr double cos_q(double x) {
    double x2 = x * x, t = 1.0, s = 1.0;
    for (int i = 1; i <= 14; ++i) { t *= -x2 / ((2 * i - 1) * (2 * i)); s += t; }
    return s;
}
constexpr uint16_t f2bf(float f) {
    uint32_t u = __builtin_bit_cast(uint32_t, f);
    u += 0x7FFFu + ((u >> 16) & 1u);
    return (uint16_t)(u >> 16);
}
struct Base { uint16_t v[512];
    constexpr Base() : v{} {
        for (int m = 0; m <= 128; ++m) {
            double c = (m == 128) ? 0.0 : cos_q(2.0 * PI * m / 512.0);
            uint16_t b = f2bf((float)c);
            uint16_t nb = (m == 128) ? b : (uint16_t)(b ^ 0x8000u);
            v[m] = b; v[(512 - m) & 511] = b;
            v[256 - m] = nb; v[(256 + m) & 511] = nb;
        }
    }
};
constexpr Base base{};
constexpr uint16_t cosb(int m) { return base.v[m & 511]; }
constexpr uint16_t nsinb(int m) { return base.v[(m + 128) & 511]; }
constexpr uint16_t sinb(int m) { return base.v[(m + 384) & 511]; }
struct Chunk { uint16_t v[16384]; };
constexpr Chunk mk_fwd(int c) {
    Chunk r{};
    for (int d = 0; d < 512; ++d)
        for (int n = 0; n < 32; ++n) { const int k = 16 * c + (n & 15);
            r.v[d * 32 + n] = base.v[((n >> 4) ? (k == 0 ? d * 256 : d * k + 128) : d * k) & 511]; }
    return r;
}
constexpr Chunk mk_inv(int blk) {
    Chunk r{};
    const int cc = blk >> 1;
    for (int e = 0; e < 16384; e += 8) {
        const int l = (e >> 3) & 63, dt = (e >> 9) & 3, kg = (e >> 11) & 3, s = ((blk & 1) << 1) + (e >> 13);
        const int kt = 4 * kg + s, d = 64 * cc + 16 * dt + (l & 15), k0 = 16 * kt + 4 * (l >> 4);
        for (int j = 0; j < 4; ++j) { const int k = k0 + j;
            r.v[e + j] = base.v[(k * d) & 511];
            r.v[e + 4 + j] = base.v[(k == 0 ? d * 256 : k * d + 128) & 511]; }
    }
    return r;
}
template <int I> constexpr Chunk fwd_c = mk_fwd(I);
template <int I> constexpr Chunk inv_c = mk_inv(I);
struct Big { Chunk c[16]; };
}
#define C16(N) {{tabs::N<0>, tabs::N<1>, tabs::N<2>, tabs::N<3>, tabs::N<4>, tabs::N<5>, tabs::N<6>, tabs::N<7>, \
                 tabs::N<8>, tabs::N<9>, tabs::N<10>, tabs::N<11>, tabs::N<12>, tabs::N<13>, tabs::N<14>, tabs::N<15>}}
alignas(256) static __device__ const tabs::Big g_fwd = C16(fwd_c);
alignas(256) static __device__ const tabs::Big g_inv = C16(inv_c);

constexpr int S = 2048, D = 512, F = 1792, DF = 257, NT = 512;
constexpr size_t WS_CTRL = 0;
constexpr size_t WS_STAT = 98304;
constexpr size_t WS_AP = 131072;
constexpr size_t WS_AX = WS_AP + (2u << 20);
constexpr size_t WS_TX = WS_AX + 8192;
constexpr size_t WS_T = WS_AX + 65536;
constexpr size_t WS_H = WS_T + (2u << 20);
constexpr size_t WS_H2 = WS_H + (size_t)16 * 8 * 48 * 1024;
constexpr size_t WS_END = WS_H2 + (size_t)16 * 8 * 16 * 512;
#define FSTR 32
#define PSLEEP 8
constexpr int W_CT = 1024, W_TMO = 2048, W_FA = 4096, W_FH = W_FA + 256 * 32;
constexpr unsigned SPIN_CAP = 1u << 16;
constexpr int CODE_PF_BYTES = 37 * 1024;

struct Params {
    const uint16_t* x; const uint16_t* wr; const uint16_t* wi;
    const uint16_t* wg; const uint16_t* wu; const uint16_t* wd;
    uint16_t* out; unsigned char* ws;
};

template <int I> struct IC { static constexpr int v = I; };
template <int B, int E, class Fn> DI void static_for(Fn&& f) { if constexpr (B < E) { f(IC<B>{}); static_for<B + 1, E>(f); } }
DI f32x4 mfma(u32x4 a, u32x4 b, f32x4 c) {
    return __builtin_amdgcn_mfma_f32_16x16x32_bf16(__builtin_bit_cast(bf8, a), __builtin_bit_cast(bf8, b), c, 0, 0, 0);
}
DI unsigned pk_bf16(float lo, float hi) { bf2 p; p[0] = (__bf16)lo; p[1] = (__bf16)hi; return __builtin_bit_cast(unsigned, p); }
DI float bf_lo(unsigned u) { return __builtin_bit_cast(float, u << 16); }
DI float bf_hi(unsigned u) { return __builtin_bit_cast(float, u & 0xffff0000u); }
DI float bfr(float x) { return bf_lo(pk_bf16(x, x)); }
DI u32x4 lds128(LAS unsigned char* L, int off) { return *(LAS u32x4*)(L + off); }
DI u32x2 lds_tr(LAS unsigned char* L, int off) {
    return __builtin_bit_cast(u32x2, __builtin_amdgcn_ds_read_tr16_b64_v4i16((LAS s16x4*)(L + off)));
}
DI u32x4 lds_tr2(LAS unsigned char* L, int off0, int off1) {
    const u32x2 a = lds_tr(L, off0), b = lds_tr(L, off1);
    return u32x4{a.x, a.y, b.x, b.y};
}
DI u32x4 swap_neg(u32x4 v) { return u32x4{v.z, v.w, v.x ^ 0x80008000u, v.y ^ 0x80008000u}; }
DI u32x4 pack8(const float (&a)[4], const float (&b)[4]) { return u32x4{pk_bf16(a[0], a[1]), pk_bf16(a[2], a[3]), pk_bf16(b[0], b[1]), pk_bf16(b[2], b[3])}; }
DI u32x4 pack8(f32x4 a, f32x4 b) { return u32x4{pk_bf16(a[0], a[1]), pk_bf16(a[2], a[3]), pk_bf16(b[0], b[1]), pk_bf16(b[2], b[3])}; }
DI void glds16(const void* gsrc, unsigned lds_addr) {
    asm volatile("s_mov_b32 m0, %1\n\ts_nop 0\n\tglobal_load_lds_dwordx4 %0, off" :: "v"(gsrc), "s"(lds_addr) : "memory");
}
DI void glds16s(const void* sbase, unsigned voff, unsigned lds_addr) {
    asm volatile("s_mov_b32 m0, %2\n\ts_nop 0\n\tglobal_load_lds_dwordx4 %0, %1" :: "v"(voff), "s"(sbase), "s"(lds_addr) : "memory");
}
DI void glds4(const void* gsrc, unsigned lds_addr) {
    asm volatile("s_mov_b32 m0, %1\n\ts_nop 0\n\tglobal_load_lds_dword %0, off" :: "v"(gsrc), "s"(lds_addr) : "memory");
}
DI void gl128(u32x4& d, const void* p) { asm volatile("global_load_dwordx4 %0, %1, off" : "=v"(d) : "v"(p) : "memory"); }
DI void gl128_sc1(u32x4& d, const void* base, unsigned off) { asm volatile("global_load_dwordx4 %0, %1, %2 sc1" : "=v"(d) : "v"(off), "s"(base) : "memory"); }
DI void gl64_sc1(u32x2& d, const void* base, unsigned off) { asm volatile("global_load_dwordx2 %0, %1, %2 sc1" : "=v"(d) : "v"(off), "s"(base) : "memory"); }
DI void gl32_sc1(unsigned& d, const void* base, unsigned off) { asm volatile("global_load_dword %0, %1, %2 sc1" : "=v"(d) : "v"(off), "s"(base) : "memory"); }
DI void gl16(unsigned& d, const void* base, unsigned off) { asm volatile("global_load_ushort %0, %1, %2" : "=v"(d) : "v"(off), "s"(base) : "memory"); }
template <int N> DI void wait_vm() { static_assert(N >= 0 && N < 64, "vmcnt"); asm volatile("s_waitcnt vmcnt(%0)" :: "n"(N) : "memory"); }
template <class T> DI void pin(T& v) { asm volatile("" : "+v"(v)); }
#define WAIT_LGKM0() asm volatile("s_waitcnt lgkmcnt(0)" ::: "memory")
#define BAR() do { asm volatile("" ::: "memory"); __builtin_amdgcn_s_barrier(); asm volatile("" ::: "memory"); } while (0)
#define SB() __builtin_amdgcn_sched_barrier(0)
DI void st_sc1(__amdgpu_buffer_rsrc_t rs, int off, u32x4 v) { __builtin_amdgcn_raw_buffer_store_b128(v, rs, off, 0, 16); }
DI void st_sc1_64(__amdgpu_buffer_rsrc_t rs, int off, u32x2 v) { __builtin_amdgcn_raw_buffer_store_b64(v, rs, off, 0, 16); }
template <int CTRL> DI float dpp_add(float v) {
    return v + __builtin_bit_cast(float, __builtin_amdgcn_update_dpp(0, __builtin_bit_cast(int, v), CTRL, 0xf, 0xf, true));
}
DI float wave_sum(float v) {
    v = dpp_add<0xB1>(v);
    v = dpp_add<0x4E>(v);
    v = dpp_add<0x141>(v);
    v = dpp_add<0x140>(v);
    const int vi = __builtin_bit_cast(int, v);
    return (__builtin_bit_cast(float, __builtin_amdgcn_readlane(vi, 0)) + __builtin_bit_cast(float, __builtin_amdgcn_readlane(vi, 16))) +
           (__builtin_bit_cast(float, __builtin_amdgcn_readlane(vi, 32)) + __builtin_bit_cast(float, __builtin_amdgcn_readlane(vi, 48)));
}
DI void give_up(const unsigned char* ws, int lane) { if (lane == 0) __hip_atomic_store((gu32*)ws + W_TMO, 1u, RLX_AGENT); }
DI void poll16(const unsigned char* ws, int word, unsigned tag, int lane) {
#pragma unroll 1
    for (unsigned spins = 0;; ++spins) {
        unsigned f; gl32_sc1(f, ws, (unsigned)(word + (lane & 15) * FSTR) * 4u); wait_vm<0>(); pin(f);
        if (__all(f == tag)) break;
        if (spins > SPIN_CAP) { give_up(ws, lane); break; }
        __builtin_amdgcn_s_sleep(PSLEEP);
    }
}
DI void fx_load(unsigned (&w)[8], unsigned o0, unsigned o1, unsigned o2, unsigned o3, const void* wr, const void* wi, int k4) {
    asm volatile("s_cmp_lg_u32 %14, 0\n\ts_cbranch_scc1 .Lnofx%=\n\t"
                 "global_load_ushort %0, %8, %12\n\tglobal_load_ushort %1, %9, %12\n\tglobal_load_ushort %2, %10, %12\n\tglobal_load_ushort %3, %11, %12\n\t"
                 "global_load_ushort %4, %8, %13\n\tglobal_load_ushort %5, %9, %13\n\tglobal_load_ushort %6, %10, %13\n\tglobal_load_ushort %7, %11, %13\n"
                 ".Lnofx%=:" : "=&v"(w[0]), "=&v"(w[1]), "=&v"(w[2]), "=&v"(w[3]), "=&v"(w[4]), "=&v"(w[5]), "=&v"(w[6]), "=&v"(w[7])
                 : "v"(o0), "v"(o1), "v"(o2), "v"(o3), "s"(wr), "s"(wi), "s"(k4) : "memory", "scc");
}
DI float cosr(float x) { return __builtin_amdgcn_cosf(x); }
DI float sinr(float x) { return __builtin_amdgcn_sinf(x); }
DI u32x4 frag_cs(int a, int b0, int n) {
    float c[4], sn[4];
#pragma unroll
    for (int j = 0; j < 4; ++j) { const float th = (float)((a * (b0 + j)) & (n - 1)) * (1.0f / n); c[j] = cosr(th); sn[j] = -sinr(th); }
    return pack8(c, sn);
}

#define STAMP(i) do {} while (0)
#define TS(i) do {} while (0)

constexpr int P1_STAGE = 49152, P1_BOFF = 16384;
constexpr int L_MISC = 147456;
constexpr int L_XI = L_MISC;
constexpr int L_ST = L_MISC + 8704;
constexpr int L_YX = L_ST + 1024;
constexpr int L_SCR = L_YX + 4096;
constexpr int L_FLG = L_SCR + 256;
constexpr int L_END = L_FLG + 16;
static_assert(L_END <= 163840, "lds");
constexpr int LDS_BYTES = 163840;
constexpr int L_RAW = 98304;
constexpr int P3_SLOT = 16384;
DI constexpr int p3_slot(int s) { return (s % 9) * P3_SLOT; }

DI int panel_row(int r, int m) { return 64 * (m & 31) + 4 * r + (m >> 5); }
DI int p3_ks(int s, int kg) { const int n = 4 * s + kg; if (s >= 12) return 7 * (n - 48) + 3; const int p = n / 6, o = n - 6 * p; return 7 * p + o + (o >= 3); }

struct LdSrc { unsigned a[4]; const unsigned char* b[8]; long sB64; const unsigned char* t; long tadv; };
DI void p1_issue(LdSrc& s, const unsigned char* x, unsigned Lb, int lw, int j) {
    const unsigned sb = Lb + (j % 3) * P1_STAGE;
#pragma unroll
    for (int i = 0; i < 4; ++i) glds16s(x + j * 128, s.a[i], sb + (4 * lw + i) * 1024);
#pragma unroll
    for (int u = 0; u < 8; ++u) { const int i = (lw & 1) + 2 * (u & 1) + 4 * (lw >> 1) + 8 * ((u >> 1) & 1) + 16 * (u >> 2);
        glds16(s.b[u], sb + P1_BOFF + i * 1024); s.b[u] += s.sB64; }
}
DI void p1_touch(LdSrc& s, unsigned Lb) { glds4(s.t, Lb + L_SCR); s.t += s.tadv; }
DI void p3_issue(const Params& p, unsigned Lb, int lw, int cc, int s, unsigned wv, unsigned fv) {
    const unsigned sb = Lb + p3_slot(s) + lw * 4096;
    if (s < 14) { const unsigned char* base = (const unsigned char*)p.wd + (size_t)p3_ks(s, lw) * 32768;
#pragma unroll
        for (int i = 0; i < 4; ++i) glds16s(base + i * 8192, wv, sb + i * 1024); }
    else { const unsigned char* base = (const unsigned char*)&g_inv + cc * 65536 + (s - 14) * 16384 + lw * 4096;
#pragma unroll
        for (int i = 0; i < 4; ++i) glds16s(base + i * 1024, fv, sb + i * 1024); }
}

DI void p3_issue2(const Params& p, unsigned Lb, int w, int cc, int s, unsigned wv, unsigned fv) {
    const unsigned sb = Lb + p3_slot(s) + w * 2048;
    if (s < 14) { const unsigned char* base = (const unsigned char*)p.wd + (size_t)p3_ks(s, w >> 1) * 32768 + (w & 1) * 16384;
        glds16s(base, wv, sb); glds16s(base + 8192, wv, sb + 1024); }
    else { const unsigned char* base = (const unsigned char*)&g_inv + cc * 65536 + (s - 14) * 16384 + w * 2048;
        glds16s(base, fv, sb); glds16s(base + 1024, fv, sb + 1024); }
}

template <bool DFTW>
DI void p1_compute(LAS unsigned char* L, unsigned Lb, int wc, int lane, f32x4 (&acc)[4][8], f32x4 (&csm)[4], unsigned (&tsv)[32]) {
    asm volatile("" : "+v"(lane));
    const int fr = lane & 15, g = lane >> 4;
    const int qq = fr >> 2, p = lane & 3;
    int aoff0 = fr * 128 + (((0 + g) ^ (fr & 7)) << 4);
    int aoff1 = fr * 128 + (((4 + g) ^ (fr & 7)) << 4);
    const int sw = 2 * qq + 8 * (g & 1);
    int boff[4];
#pragma unroll
    for (int q = 0; q < 4; ++q) boff[q] = P1_BOFF + (8 * g + qq) * 512 + (((8 * wc + 2 * q + (p >> 1)) ^ sw) << 4) + 8 * (p & 1);
    u32x4 xa[2][4], wb[2][4];
    auto rdx = [&](int ks, int mh, int i) { xa[mh][i] = lds128(L, (ks ? aoff1 : aoff0) + (4 * mh + i) * 2048); };
    auto rdw = [&](int ks, int q) { const int o = boff[q] + ks * 16384; wb[ks][q] = lds_tr2(L, o, o + 2048); };
    auto rebase = [&](int d) { aoff0 += d; aoff1 += d; boff[0] += d; boff[1] += d; boff[2] += d; boff[3] += d;
        asm volatile("" : "+v"(aoff0), "+v"(aoff1), "+v"(boff[0]), "+v"(boff[1]), "+v"(boff[2]), "+v"(boff[3])); };
    auto step = [&](int ks, int mh, bool more) {
#pragma unroll
        for (int i = 0; i < 16; ++i) {
            const int q = i >> 2, mt = i & 3;
            if (DFTW && q >= 2) acc[q][4 * mh + mt] = mfma(xa[mh][mt], wb[ks][q], acc[q][4 * mh + mt]);
            else acc[q][4 * mh + mt] = mfma(wb[ks][q], xa[mh][mt], acc[q][4 * mh + mt]);
            if (more) {
                if (mh == 0) { if (i >= 1 && i <= 4) rdx(ks, 1, i - 1); }
                else { if (i < 4) rdx(ks ^ 1, 0, i); else if (i < 8) rdw(ks ^ 1, i - 4); }
            }
            if (mh == 0 && (i & 3) == 3) { const int d = i >> 2; if (!DFTW || d < 2) csm[d] = mfma(wb[ks][d], u32x4{0x3F803F80u, 0x3F803F80u, 0x3F803F80u, 0x3F803F80u}, csm[d]); }
            SB();
        }
    };
    BAR();
    TS(0);
#pragma unroll
    for (int i = 0; i < 4; ++i) { rdx(0, 0, i); rdw(0, i); }
    SB();
    static_for<0, 8>([&](auto J) {
        constexpr int j = J.v;
        step(0, 0, true);
        step(0, 1, true);
        step(1, 0, true);
        if constexpr (j < 7) { WAIT_LGKM0(); BAR(); TS(j + 1); rebase((j + 1) % 3 == 0 ? -2 * P1_STAGE : P1_STAGE);
            SB(); }
        step(1, 1, j < 7);
    });
    TS(8);
}

constexpr int HD = 5;
constexpr int FTS = 10, FTC = 12;
constexpr int p3_issue_at(int o) { return o < 14 ? o - HD : FTC; }
constexpr int p3_opcnt(int o) { return (o >= 12 && o <= 14) ? 4 : 2; }
enum { K_OP, K_TST, K_W, K_FT };
constexpr int p3_after(int kind, int idx, int s_now) {
    int n = -1;
    auto hit = [&](bool is, int cnt) { if (is) n = 0; else if (n >= 0) n += cnt; };
    for (int o = 0; o < HD; ++o) hit(kind == K_OP && idx == o, 2);
    hit(kind == K_TST, 1); hit(false, 1);
    for (int s = 0; s < s_now; ++s) {
        for (int o = HD; o < 18; ++o) if (p3_issue_at(o) == s) hit(kind == K_OP && idx == o, p3_opcnt(o));
        if (s >= 3 && s + 6 < 18) hit(kind == K_W && idx == s + 6, 2);
        if (s == FTS) hit(kind == K_FT, 1);
    }
    return n;
}
constexpr int cmin(int a, int b) { return a < b ? a : b; }
constexpr int p3_wait(int s) {
    int n = p3_after(K_OP, s, s);
    if (s == 3) n = cmin(n, p3_after(K_TST, 0, s));
    if (s % 3 == 0 && s >= 9) n = cmin(n, p3_after(K_W, s + 2, s));
    if (s == FTC) n = cmin(n, p3_after(K_FT, 0, s));
    return n;
}
static_assert(p3_wait(0) == 2 * (HD - 1) + 2 && p3_wait(3) == 1 + 6, "queue model");

struct P3Ctx { unsigned hbase, h2base, tbase; int woff[4]; };
DI void p3_rdw(LAS unsigned char* L, const P3Ctx& c, int kg, int lane, int s, u32x4 (&wf)[4]) {
#pragma unroll
    for (int dt = 0; dt < 4; ++dt) {
        if (s < 14) wf[dt] = lds_tr2(L, p3_slot(s) + c.woff[dt], p3_slot(s) + c.woff[dt] + 2048);
        else wf[dt] = lds128(L, p3_slot(s) + (kg * 4 + dt) * 1024 + lane * 16);
    }
}

__global__ void __launch_bounds__(NT, 2) k_fused(Params p) {
    extern __shared__ __attribute__((aligned(16))) unsigned char smem[];
    LAS unsigned char* L = (LAS unsigned char*)smem;
    const unsigned Lb = (unsigned)(size_t)L;
    const int wid = __builtin_amdgcn_readfirstlane(threadIdx.x >> 6);
    const int wc = wid & 3;
    const int b = blockIdx.x, xcd = b & 7, bi = b >> 3;
    const int r = (xcd & 1) * 8 + (bi & 7), c = (xcd >> 1) * 4 + (bi >> 3);
    const int jq = b & 7, kq = b >> 3;
    const int cc = (xcd & 1) * 4 + (bi & 3), hp = (xcd >> 1) * 8 + (bi >> 2), r3 = hp >> 1, h3 = hp & 1;
    const unsigned char* ws = p.ws;
    unsigned tsv[32];
#pragma unroll
    for (int i = 0; i < 32; ++i) tsv[i] = 0;
    STAMP(0);
    TS(31);
    const int odd = c & 1;
    const int u2 = wid >> 2, w2 = wid & 3, k4 = 2 * kq + u2;
    LAS f32x2* ST = (LAS f32x2*)(L + L_ST);
    f32x4 acc[4][8], csm[4];
    u32x4 eaC[2], eaS[2]; float tcA[2][4], tsA[2][4];
#pragma unroll
    for (int i = 0; i < 4; ++i) csm[i] = f32x4{0.f, 0.f, 0.f, 0.f};
    unsigned tag;
    {
    const int tid = threadIdx.x, lane = tid & 63;
    if (wid >= 4) {
        const int lw = wid - 4;
        unsigned epoch; gl32_sc1(epoch, ws, 0u);
        LdSrc ls;
        {
            const int idx = lw * 22 + (lane < 22 ? lane : 0);
            if (idx < 32) { ls.t = (const unsigned char*)p.x + (size_t)panel_row(r, 32 * (c & 3) + idx) * 1024; ls.tadv = 128; }
            else { const int i = idx - 32, row = 8 * (r & 7) + i / 7, it = i % 7;
                if (it == 6) { ls.t = (const unsigned char*)&g_fwd + c * 32768 + row * 64; ls.tadv = 64 * 64; }
                else { const int it3 = it % 3; ls.t = (const unsigned char*)(it < 3 ? p.wg : p.wu) + (size_t)row * (F * 2) + c * 224 + (it3 == 2 ? 220 : it3 * 128); ls.tadv = 64L * F * 2; } }
        }
        p1_touch(ls, Lb);
        SB();
#pragma unroll
        for (int i = 0; i < 4; ++i) { const int m = 8 * (4 * lw + i) + (lane >> 3); ls.a[i] = (unsigned)panel_row(r, m) * 1024u + (((lane & 7) ^ (lane >> 3)) << 4); }
        {
            const int b0 = lw & 1, b2 = lw >> 1;
            const int swz = 2 * (2 * b0 + (lane >> 5)) + 8 * b2;
            const int ch = (lane & 31) ^ swz, nt = ch >> 1, half = ch & 1, cw = nt >> 2, q = nt & 3;
            const unsigned char* base; int col, strideB;
            if (cw < 3) { const int ft = odd + 2 * cw + (q & 1); base = (const unsigned char*)(q < 2 ? p.wg : p.wu); col = 112 * c + 16 * ft + 8 * half; strideB = F * 2; }
            else if (q < 2) { const int ft = odd ? 0 : 6; base = (const unsigned char*)(q == 0 ? p.wg : p.wu); col = 112 * c + 16 * ft + 8 * half; strideB = F * 2; }
            else { base = (const unsigned char*)&g_fwd + c * 32768; col = 16 * (q - 2) + 8 * half; strideB = 64; }
            const unsigned char* b00 = base + (size_t)(2 * b0 + 8 * b2 + (lane >> 5)) * strideB + col * 2;
#pragma unroll
            for (int u = 0; u < 8; ++u) ls.b[u] = b00 + (size_t)(4 * (u & 1) + 16 * ((u >> 1) & 1) + 32 * (u >> 2)) * strideB;
            ls.sB64 = (long)strideB * 64;
        }
        p1_issue(ls, (const unsigned char*)p.x, Lb, lw, 0);
        p1_touch(ls, Lb); p1_touch(ls, Lb); p1_touch(ls, Lb);
        p1_issue(ls, (const unsigned char*)p.x, Lb, lw, 1);
        STAMP(14);
        if (wid == 4 && lane < 4) *((volatile LAS unsigned*)(L + L_FLG) + lane) = 0u;
        wait_vm<15>(); pin(epoch); tag = epoch + 1u;
        if (r == 0 && tid == 256) __hip_atomic_store((gu32*)p.ws + W_CT + c * 32, tag << 8, RLX_AGENT);
        WAIT_LGKM0();
        STAMP(15);
        BAR();
        p1_touch(ls, Lb);
        p1_issue(ls, (const unsigned char*)p.x, Lb, lw, 2);
        SB();
        const unsigned char *st0, *st1;
        {
            auto wline = [&](int i) { const int st = i >> 2, ln = (bi >> 2) * 16 + lw * 4 + (i & 3);
                return st < 14 ? (const unsigned char*)p.wd + (size_t)(32 * p3_ks(st, ln >> 5) + (ln & 31)) * 1024 + cc * 128
                               : (const unsigned char*)&g_inv + (size_t)cc * 65536 + (st - 14) * 16384 + ln * 128; };
            st0 = wline(lane);
            const int fi = (bi * 4 + lw) * 20 + (lane >= 8 && lane < 28 ? lane - 8 : 0);
            const int arr = fi / 1280, rr = (fi % 1280) / 5, ll = (fi % 1280) % 5;
            const int jrow = 4 * jq + (rr & 3) + 32 * (rr >> 2);
            st1 = lane < 8 ? wline(64 + lane) : (const unsigned char*)(arr ? p.wi : p.wr) + (size_t)jrow * (DF * 2) + min(ll * 128, 510);
        }
        u32x2 gran; const unsigned goff = (unsigned)WS_STAT + (unsigned)(r * 128 + (tid & 127)) * 8u;
        static_for<0, 7>([&](auto J) {
            constexpr int j = J.v;
            if constexpr (j < 4) wait_vm<13>(); else if constexpr (j == 4) wait_vm<12>();
            else if constexpr (j == 5) wait_vm<15>(); else wait_vm<3>();
            BAR();
            if constexpr (j < 3) p1_touch(ls, Lb);
            if constexpr (j + 3 < 8) p1_issue(ls, (const unsigned char*)p.x, Lb, lw, j + 3);
            if constexpr (j == 4) {
                gl64_sc1(gran, ws, goff);
                glds4(st0, Lb + L_SCR); glds4(st1, Lb + L_SCR);
            }
        });
        wait_vm<2>();
        pin(gran);
        if (lw < 2) {
            if (!__all(gran.y == tag)) {
#pragma unroll 1
                for (unsigned spins = 0;; ++spins) {
                    gl64_sc1(gran, ws, goff); wait_vm<0>(); pin(gran);
                    if (__all(gran.y == tag)) break;
                    if (spins > SPIN_CAP) { give_up(ws, lane); break; }
                    __builtin_amdgcn_s_sleep(1);
                }
            }
            ST[tid & 127] = f32x2{bf_hi(gran.x), 1.0f / bf_lo(gran.x)};
        }
        {
            const int fr = lane & 15, g = lane >> 4;
#pragma unroll
            for (int jt = 0; jt < 2; ++jt) {
                float cv[8], sv[8];
#pragma unroll
                for (int j = 0; j < 8; ++j) { const float th = (float)(((16 * jt + fr) * (8 * g + j)) & 31) * (1.0f / 32.0f); cv[j] = cosr(th); sv[j] = sinr(th); }
                eaC[jt] = u32x4{pk_bf16(cv[0], cv[1]), pk_bf16(cv[2], cv[3]), pk_bf16(cv[4], cv[5]), pk_bf16(cv[6], cv[7])};
                eaS[jt] = u32x4{pk_bf16(sv[0], sv[1]), pk_bf16(sv[2], sv[3]), pk_bf16(sv[4], sv[5]), pk_bf16(sv[6], sv[7])};
#pragma unroll
                for (int e = 0; e < 4; ++e) { const float th = (float)((16 * jt + fr) * (4 * r + e)) * (1.0f / 2048.0f); tcA[jt][e] = cosr(th); tsA[jt][e] = sinr(th); }
            }
        }
#pragma unroll
        for (int q = 0; q < 4; ++q)
#pragma unroll
            for (int mt = 0; mt < 8; ++mt) acc[q][mt] = f32x4{0.f, 0.f, 0.f, 0.f};
    } else {
        if (wid == 0) {
            const unsigned char* pc = (const unsigned char*)__builtin_amdgcn_s_getpc();
            const int ln = bi * 10 + (lane < 10 ? lane : 0);
            glds4(pc - ((size_t)pc & 127) + (size_t)min(ln * 128, CODE_PF_BYTES), Lb + L_SCR);
        }
        if (wid == 1) {
            const int q = lane & 7, a = (lane >> 3) & 3;
            const unsigned char* base = a == 0 ? (const unsigned char*)p.x : a == 1 ? (const unsigned char*)p.wg : a == 2 ? (const unsigned char*)p.wu : (const unsigned char*)&g_fwd;
            glds4(base + (size_t)(a == 3 ? (q & 1) : (a == 0 ? q : min(q, 6))) * 262144, Lb + L_SCR);
        }
        unsigned epoch; gl32_sc1(epoch, ws, 0u);
        u32x4 xrow[2];
#pragma unroll
        for (int i = 0; i < 2; ++i) gl128(xrow[i], (const unsigned char*)p.x + (size_t)panel_row(r, 8 * c + 2 * wid + i) * 1024 + lane * 16);
        wait_vm<0>(); pin(epoch); pin(xrow[0]); pin(xrow[1]);
        tag = epoch + 1u;
#pragma unroll
        for (int i = 0; i < 2; ++i) {
            float v[8];
#pragma unroll
            for (int e = 0; e < 4; ++e) { v[2 * e] = bf_lo(xrow[i][e]); v[2 * e + 1] = bf_hi(xrow[i][e]); }
            float s = ((v[0] + v[1]) + (v[2] + v[3])) + ((v[4] + v[5]) + (v[6] + v[7]));
            s = wave_sum(s);
            const float mean = s * (1.0f / 512.0f);
            float q2 = 0.f;
#pragma unroll
            for (int e = 0; e < 8; ++e) { const float d = v[e] - mean; q2 += d * d; }
            q2 = wave_sum(q2);
            const float var = q2 * (1.0f / 512.0f);
            const float m_bf = bfr(mean), v_bf = bfr(var), s_bf = bfr(sqrtf(bfr(v_bf + 1e-5f)));
            const unsigned val = (__builtin_bit_cast(unsigned, m_bf) & 0xffff0000u) | (__builtin_bit_cast(unsigned, s_bf) >> 16);
            if (lane == 0) __hip_atomic_store((gu64*)(p.ws + WS_STAT) + (r * 128 + 8 * c + 2 * wid + i), ((unsigned long long)tag << 32) | val, RLX_AGENT);
        }
#pragma unroll
        for (int q = 0; q < 4; ++q)
#pragma unroll
            for (int mt = 0; mt < 8; ++mt) acc[q][mt] = f32x4{0.f, 0.f, 0.f, 0.f};
        STAMP(1);
        if (wid < 3) p1_compute<false>(L, Lb, wc, lane, acc, csm, tsv); else p1_compute<true>(L, Lb, wc, lane, acc, csm, tsv);
        STAMP(2);
    }
    }
    int tz = threadIdx.x; asm volatile("" : "+v"(tz));
    const int tid = tz, lane = tz & 63, fr = lane & 15, g = lane >> 4;
    const __amdgpu_buffer_rsrc_t rs = __builtin_amdgcn_make_buffer_rsrc((void*)p.ws, 0, (int)WS_END, 0x00020000);
    const unsigned wv = (unsigned)(lane >> 3) * 1024u + cc * 128u + (unsigned)(((lane & 7) ^ (2 * ((lane >> 4) & 3))) << 4), fv = lane * 16u;
    volatile LAS unsigned* flg = (volatile LAS unsigned*)(L + L_FLG);
    WAIT_LGKM0(); BAR();
    STAMP(3);
    const int fj1 = 4 * jq + (fr & 3), fk = 4 * k4 + (fr >> 2);
    unsigned fw[4], fwi[4];
    auto signal = [&](int cnt, int word) {
        if (lane == 0) { const unsigned old = __hip_atomic_fetch_add((LAS unsigned*)(L + L_FLG) + cnt, 1u, __ATOMIC_RELAXED, __HIP_MEMORY_SCOPE_WORKGROUP);
            if (old == 3u) __hip_atomic_store((gu32*)p.ws + word, tag, RLX_AGENT); }
    };
    if (wid < 3) {
        const f32x4 cg0 = csm[0], cg1 = csm[1], cu0 = csm[2], cu1 = csm[3];
#pragma unroll
        for (int mt = 0; mt < 8; ++mt) {
            const f32x2 st = ST[16 * mt + fr];
            const float r2 = st.y * st.y;
            const f32x4 h0 = (acc[0][mt] - st.x * cg0) * (acc[2][mt] - st.x * cu0) * r2;
            const f32x4 h1 = (acc[1][mt] - st.x * cg1) * (acc[3][mt] - st.x * cu1) * r2;
            st_sc1(rs, (int)WS_H + ((((r * 8 + mt) * 48 + 3 * c + wc) * 64 + lane) << 4), pack8(h0, h1));
        }
    } else if (wid == 3) {
#pragma unroll
        for (int q = 0; q < 2; ++q)
#pragma unroll
            for (int mt = 0; mt < 8; ++mt) *(LAS f32x4*)(L + L_RAW + ((q * 8 + mt) * 64 + lane) * 16) = acc[2 + q][mt];
        WAIT_LGKM0();
        flg[0] = 1u;
        const f32x4 cg0 = csm[0], cu0 = csm[1];
#pragma unroll
        for (int mt = 0; mt < 8; ++mt) {
            const f32x2 st = ST[16 * mt + fr];
            const float r2 = st.y * st.y;
            const f32x4 h0 = (acc[0][mt] - st.x * cg0) * (acc[1][mt] - st.x * cu0) * r2;
            st_sc1_64(rs, (int)WS_H2 + ((((r * 8 + mt) * 16 + c) * 64 + lane) << 3), u32x2{pk_bf16(h0[0], h0[1]), pk_bf16(h0[2], h0[3])});
        }
    } else {
        const int mt = wid - 4;
        const int kq_ = fr >> 2, s2l = fr & 3;
        __builtin_amdgcn_s_setprio(3);
#pragma unroll 1
        for (unsigned spins = 0; flg[0] == 0u && spins < (1u << 20); ++spins) __builtin_amdgcn_s_sleep(1);
        asm volatile("" ::: "memory");
        u32x4 are, aim;
        {
            const int m0 = 32 * s2l + 8 * g, tl = 2 * s2l + (g >> 1), ln = 4 * mt + kq_ + 32 * (g & 1);
            const f32x4 r0 = *(LAS f32x4*)(L + L_RAW + (tl * 64 + ln) * 16), r1 = *(LAS f32x4*)(L + L_RAW + (tl * 64 + ln + 16) * 16);
            const f32x4 i0 = *(LAS f32x4*)(L + L_RAW + ((8 + tl) * 64 + ln) * 16), i1 = *(LAS f32x4*)(L + L_RAW + ((8 + tl) * 64 + ln + 16) * 16);
            const f32x4 sa = *(LAS f32x4*)(ST + m0), sb = *(LAS f32x4*)(ST + m0 + 2), sc = *(LAS f32x4*)(ST + m0 + 4), sd = *(LAS f32x4*)(ST + m0 + 6);
            const float csf = (c == 0 && mt == 0 && kq_ == 0) ? 512.0f : 0.0f;
            are = u32x4{pk_bf16((r0[0] - sa[0] * csf) * sa[1], (r0[1] - sa[2] * csf) * sa[3]), pk_bf16((r0[2] - sb[0] * csf) * sb[1], (r0[3] - sb[2] * csf) * sb[3]),
                        pk_bf16((r1[0] - sc[0] * csf) * sc[1], (r1[1] - sc[2] * csf) * sc[3]), pk_bf16((r1[2] - sd[0] * csf) * sd[1], (r1[3] - sd[2] * csf) * sd[3])};
            aim = u32x4{pk_bf16(i0[0] * sa[1], i0[1] * sa[3]), pk_bf16(i0[2] * sb[1], i0[3] * sb[3]), pk_bf16(i1[0] * sc[1], i1[1] * sc[3]), pk_bf16(i1[2] * sd[1], i1[3] * sd[3])};
        }
        const f32x4 z = {0.f, 0.f, 0.f, 0.f};
        const bool special = (c == 0 && mt == 0 && g == 0);
#pragma unroll
        for (int jt = 0; jt < 2; ++jt) {
            const f32x4 Prc = mfma(are, eaC[jt], z), Pis = mfma(aim, eaS[jt], z), Prs = mfma(are, eaS[jt], z), Pic = mfma(aim, eaC[jt], z);
            const int j1 = 16 * jt + fr;
            float pr[4], qi[4], pr2[4], qi2[4];
#pragma unroll
            for (int e = 0; e < 4; ++e) {
                const float cw = tcA[jt][e], sw = tsA[jt][e];
                const float ar = special ? Prc[e] : Prc[e] + Pis[e], ai = special ? -Prs[e] : Pic[e] - Prs[e];
                pr[e] = ar * cw + ai * sw; qi[e] = -(ai * cw - ar * sw);
                const float br = Pic[e], bi2 = -Pis[e];
                pr2[e] = br * cw + bi2 * sw; qi2[e] = -(bi2 * cw - br * sw);
            }
            const int U = (j1 >> 2) * 64 + 4 * c + mt, lp = 16 * (r & 3) + 4 * g + (j1 & 3);
            st_sc1(rs, (int)WS_AP + (((U * 4 + (r >> 2)) * 64 + lp) << 4), pack8(pr, qi));
            if (special) st_sc1(rs, (int)WS_AX + (((((j1 >> 2) * 4 + (r >> 2)) * 4 + (r & 3)) * 4 + (j1 & 3)) << 4), pack8(pr2, qi2));
        }
        __builtin_amdgcn_s_setprio(0);
        wait_vm<0>();
        signal(1, W_FA + (c * 16 + r) * FSTR);
#pragma unroll
        for (int st = 0; st < 3; ++st) p3_issue(p, Lb, mt, cc, st, wv, fv);
    }
#pragma unroll
    for (int e = 0; e < 4; ++e) { const unsigned o = (unsigned)((fj1 + 32 * (16 * w2 + 4 * g + e)) * DF + fk) * 2u; gl16(fw[e], p.wr, o); gl16(fwi[e], p.wi, o); }
    unsigned fx[8];
    {
        const unsigned o0 = (unsigned)((fj1 + 32 * (16 * w2 + 4 * g)) * DF + 256) * 2u;
        fx_load(fx, o0, o0 + 32u * DF * 2u, o0 + 64u * DF * 2u, o0 + 96u * DF * 2u, p.wr, p.wi, k4);
    }
    if (wid < 4) {
        if (k4 == 0) wait_vm<16>(); else wait_vm<8>();
        signal(2, W_FH + (r * 16 + c) * FSTR);
#pragma unroll
        for (int st = 3; st < 6; ++st) p3_issue(p, Lb, wid, cc, st, wv, fv);
#pragma unroll 1
        for (unsigned spins = 0; flg[1] < 4u && spins < (1u << 22); ++spins) __builtin_amdgcn_s_sleep(1);
        asm volatile("" ::: "memory");
#pragma unroll
        for (int st = 6; st < 9; ++st) p3_issue(p, Lb, wid, cc, st, wv, fv);
    }
    STAMP(5);
    {
        const int kg = wid & 3, rh = wid >> 2;
        u32x4 E1[4], E2[4];
#pragma unroll
        for (int t = 0; t < 4; ++t) E1[t] = frag_cs(16 * w2 + fr, 16 * t + 4 * g, 64);
#pragma unroll
        for (int i = 0; i < 4; ++i) E2[i] = frag_cs(16 * (i >> 1) + fr, 16 * (i & 1) + 4 * g, 32);
        float tc[4], tsn[4];
#pragma unroll
        for (int e = 0; e < 4; ++e) { const float th = (float)((4 * jq + e) * (16 * w2 + fr)) * (1.0f / 2048.0f); tc[e] = cosr(th); tsn[e] = sinr(th); }
        P3Ctx cx;
        {
            const int qq = fr >> 2, pp = lane & 3, row = 32 * kg + 4 * g + qq, sw = 2 * ((row >> 1) & 3);
#pragma unroll
            for (int dt = 0; dt < 4; ++dt) cx.woff[dt] = row * 128 + (((2 * dt + (pp >> 1)) ^ sw) << 4) + 8 * (pp & 1);
            cx.hbase = (unsigned)WS_H + (unsigned)(((r3 * 8 + 4 * h3 + 2 * rh) * 48 + kg) * 64 + lane) * 16u;
            cx.h2base = (unsigned)WS_H2 + (unsigned)(((r3 * 8 + 4 * h3 + 2 * rh) * 16 + 2 * kg) * 64 + lane) * 8u;
            cx.tbase = (unsigned)WS_T + (unsigned)((((4 * r3 + 2 * h3 + rh) * 16 + 4 * kg) * 2) * 64 + lane) * 16u;
        }
        SB();
        STAMP(6);
        if (wid == 0) poll16(ws, W_FA + (kq >> 1) * 16 * FSTR, tag, lane);
        if (wid == 1) poll16(ws, W_FH + r3 * 16 * FSTR, tag, lane);
        BAR();
        STAMP(7);
        const int U = jq * 64 + k4;
        u32x4 Bf[4], Bx[4];
        u32x4 hb[HD][2]; u32x2 hh[2][4];
#pragma unroll
        for (int t = 0; t < 4; ++t) gl128_sc1(Bf[t], ws, (unsigned)WS_AP + (unsigned)((U * 4 + t) * 64 + lane) * 16u);
        {
            const unsigned xo = (unsigned)WS_AX + (unsigned)((jq * 16 + g) * 4 + (fr & 3)) * 16u;
            asm volatile("s_cmp_lg_u32 %6, 0\n\ts_cbranch_scc1 .Lnox%=\n\t"
                         "global_load_dwordx4 %0, %4, %5 sc1\n\tglobal_load_dwordx4 %1, %4, %5 offset:256 sc1\n\t"
                         "global_load_dwordx4 %2, %4, %5 offset:512 sc1\n\tglobal_load_dwordx4 %3, %4, %5 offset:768 sc1\n"
                         ".Lnox%=:" : "=&v"(Bx[0]), "=&v"(Bx[1]), "=&v"(Bx[2]), "=&v"(Bx[3]) : "v"(xo), "s"(ws), "s"(k4) : "memory", "scc");
        }
        static_for<0, HD>([&](auto Sx) { constexpr int o = Sx.v;
            gl128_sc1(hb[o][0], ws, cx.hbase + o * 4096u); gl128_sc1(hb[o][1], ws, cx.hbase + 48u * 1024u + o * 4096u); });
        wait_vm<2 * HD>();
        pin(Bf[0]); pin(Bf[1]); pin(Bf[2]); pin(Bf[3]); pin(Bx[0]); pin(Bx[1]); pin(Bx[2]); pin(Bx[3]);
        pin(fw[0]); pin(fw[1]); pin(fw[2]); pin(fw[3]); pin(fwi[0]); pin(fwi[1]); pin(fwi[2]); pin(fwi[3]);
        pin(fx[0]); pin(fx[1]); pin(fx[2]); pin(fx[3]); pin(fx[4]); pin(fx[5]); pin(fx[6]); pin(fx[7]);
        STAMP(8);
        {
            f32x4 a1 = {0.f, 0.f, 0.f, 0.f}, a2 = a1;
#pragma unroll
            for (int t = 0; t < 4; ++t) { a1 = mfma(E1[t], Bf[t], a1); a2 = mfma(E1[t], swap_neg(Bf[t]), a2); }
            constexpr float SC = 2.0f / (2048.0f * 512.0f);
            const float sc = (k4 == 0 && (fr >> 2) == 0) ? 0.5f * SC : SC;
            float yr[4], yi[4];
#pragma unroll
            for (int e = 0; e < 4; ++e) { const float xr = a1[e], xi = -a2[e], wre = bf_lo(fw[e]), wim = bf_lo(fwi[e]);
                yr[e] = (xr * wre - xi * wim) * sc; yi[e] = (xr * wim + xi * wre) * sc; }
            if (k4 == 0) {
                f32x4 n1 = {0.f, 0.f, 0.f, 0.f}, n2 = n1;
#pragma unroll
                for (int t = 0; t < 4; ++t) { n1 = mfma(E1[t], Bx[t], n1); n2 = mfma(E1[t], swap_neg(Bx[t]), n2); }
                float xr2[4], xi2[4];
#pragma unroll
                for (int e = 0; e < 4; ++e) { const float xr = n1[e], xi = -n2[e], wre = bf_lo(fx[e]), wim = bf_lo(fx[4 + e]);
                    xr2[e] = (xr * wre - xi * wim) * (0.5f * SC); xi2[e] = (xr * wim + xi * wre) * (0.5f * SC); }
                *(LAS u32x4*)(L + L_YX + (w2 * 64 + lane) * 16) = pack8(xr2, xi2);
            }
            *(LAS u32x4*)(L + L_XI + ((u2 * 4 + w2) * 64 + lane) * 16) = pack8(yr, yi);
        }
        WAIT_LGKM0(); BAR();
        u32x4 wf[2][4];
        p3_rdw(L, cx, kg, lane, 0, wf[0]);
        {
            f32x4 t1 = {0.f, 0.f, 0.f, 0.f}, t2 = t1;
#pragma unroll
            for (int t = 0; t < 4; ++t) { const u32x4 y = lds128(L, L_XI + ((u2 * 4 + t) * 64 + lane) * 16);
                t1 = mfma(y, E1[t], t1); t2 = mfma(swap_neg(y), E1[t], t2); }
            const int s2 = 16 * w2 + fr, ko = 4 * k4 + g;
            float tr[4], ti[4];
#pragma unroll
            for (int e = 0; e < 4; ++e) { tr[e] = t1[e] * tc[e] - t2[e] * tsn[e]; ti[e] = t2[e] * tc[e] + t1[e] * tsn[e]; }
            st_sc1(rs, (int)WS_T + ((((s2 * 16 + (ko >> 4)) * 2 + (jq >> 2)) * 64 + (jq & 3) * 16 + (ko & 15)) << 4), pack8(tr, ti));
            if (k4 == 0) {
                f32x4 x1 = {0.f, 0.f, 0.f, 0.f}, x2 = x1;
#pragma unroll
                for (int t = 0; t < 4; ++t) { const u32x4 y = lds128(L, L_YX + (t * 64 + lane) * 16);
                    x1 = mfma(y, E1[t], x1); x2 = mfma(swap_neg(y), E1[t], x2); }
#pragma unroll
                for (int e = 0; e < 4; ++e) { tr[e] = x1[e] * tc[e] - x2[e] * tsn[e]; ti[e] = x2[e] * tc[e] + x1[e] * tsn[e]; }
                if (g == 0) st_sc1(rs, (int)WS_TX + ((s2 * 8 + jq) << 4), pack8(tr, ti));
            }
        }
        u32x4 xres; gl128(xres, (const unsigned char*)p.x + ((size_t)panel_row(r3, 64 * h3 + (tid >> 3)) * 512 + 64 * cc + 8 * (tid & 7)) * 2);
        STAMP(9);
        f32x4 acc3[4][2];
#pragma unroll
        for (int dt = 0; dt < 4; ++dt) { acc3[dt][0] = f32x4{0.f, 0.f, 0.f, 0.f}; acc3[dt][1] = acc3[dt][0]; }
        unsigned ftf; u32x4 tx[2];
        static_for<0, 14>([&](auto Sx) {
            constexpr int s = Sx.v;
            wait_vm<p3_wait(s)>();
            if constexpr (s == 12 || s == 13) { pin(hh[s - 12][0]); pin(hh[s - 12][1]); pin(hh[s - 12][2]); pin(hh[s - 12][3]); }
            else { pin(hb[s % HD][0]); pin(hb[s % HD][1]); }
            if constexpr (s == FTC) {
                STAMP(12);
                pin(ftf);
                if (wid == 0) {
                    if (!__all(ftf == (tag << 8) + 16u)) {
#pragma unroll 1
                        for (unsigned spins = 0;; ++spins) {
                            __builtin_amdgcn_s_sleep(PSLEEP);
                            unsigned f2; gl32_sc1(f2, ws, (unsigned)(W_CT + (lane & 15) * 32) * 4u); wait_vm<0>(); pin(f2);
                            if (__all(f2 == (tag << 8) + 16u)) break;
                            if (spins > SPIN_CAP) { give_up(ws, lane); break; }
                        }
                    }
                    if (b == 0 && lane == 0) __hip_atomic_store((gu32*)p.ws, tag, RLX_AGENT);
                }
                STAMP(13);
            }
            if constexpr (s % 3 == 0 && s > 0) {
                WAIT_LGKM0(); BAR();
                if constexpr (s == 3) { STAMP(4); if (tid == 0) __hip_atomic_fetch_add((gu32*)p.ws + W_CT + (kq >> 1) * 32, 1u, RLX_AGENT); }
                p3_rdw(L, cx, kg, lane, s, wf[s & 1]);
            }
            TS(10 + s);
            if constexpr (s + 1 < 14 && (s + 1) % 3 != 0) p3_rdw(L, cx, kg, lane, s + 1, wf[(s + 1) & 1]);
            u32x4 b0, b1;
            if constexpr (s == 12 || s == 13) { b0 = u32x4{hh[s - 12][0].x, hh[s - 12][0].y, hh[s - 12][1].x, hh[s - 12][1].y}; b1 = u32x4{hh[s - 12][2].x, hh[s - 12][2].y, hh[s - 12][3].x, hh[s - 12][3].y}; }
            else { b0 = hb[s % HD][0]; b1 = hb[s % HD][1]; }
#pragma unroll
            for (int dt = 0; dt < 4; ++dt) { acc3[dt][0] = mfma(wf[s & 1][dt], b0, acc3[dt][0]); acc3[dt][1] = mfma(wf[s & 1][dt], b1, acc3[dt][1]); }
            SB();
            static_for<HD, 18>([&](auto Ox) {
                constexpr int o = Ox.v;
                if constexpr (p3_issue_at(o) == s) {
                    if constexpr (o < 12) { gl128_sc1(hb[o % HD][0], ws, cx.hbase + o * 4096u); gl128_sc1(hb[o % HD][1], ws, cx.hbase + 48u * 1024u + o * 4096u); }
                    else if constexpr (o < 14) {
                        gl64_sc1(hh[o - 12][0], ws, cx.h2base + (o - 12) * 4096u); gl64_sc1(hh[o - 12][1], ws, cx.h2base + (o - 12) * 4096u + 512u);
                        gl64_sc1(hh[o - 12][2], ws, cx.h2base + 8192u + (o - 12) * 4096u); gl64_sc1(hh[o - 12][3], ws, cx.h2base + 8192u + (o - 12) * 4096u + 512u); }
                    else { gl128_sc1(hb[o % HD][0], ws, cx.tbase + (o - 14) * 2048u); gl128_sc1(hb[o % HD][1], ws, cx.tbase + (o - 14) * 2048u + 1024u);
                        if constexpr (o == 14) { const unsigned txo = (unsigned)WS_TX + (unsigned)((4 * r3 + 2 * h3 + rh) * 8 + g) * 16u; gl128_sc1(tx[0], ws, txo); gl128_sc1(tx[1], ws, txo + 64u); } }
                }
            });
            if constexpr (s >= 3 && s + 6 < 18) p3_issue2(p, Lb, wid, cc, s + 6, wv, fv);
            if constexpr (s == FTS) gl32_sc1(ftf, ws, wid == 0 ? (unsigned)(W_CT + (lane & 15) * 32) * 4u : cx.hbase - lane * 16u);
            SB();
        });
        wait_vm<0>();
        static_for<14, 18>([&](auto Ox) { pin(hb[Ox.v % HD][0]); pin(hb[Ox.v % HD][1]); });
        pin(tx[0]); pin(tx[1]); pin(xres);
        WAIT_LGKM0(); BAR();
        TS(24);
        {
            u32x4 wz[2][4];
            p3_rdw(L, cx, kg, lane, 14, wz[0]); p3_rdw(L, cx, kg, lane, 15, wz[1]);
            u32x4 zf[4][2];
            static_for<0, 4>([&](auto Zx) {
                constexpr int z = Zx.v;
                const u32x4 T0 = hb[(14 + z) % HD][0], T1 = hb[(14 + z) % HD][1];
                u32x4 T0s = swap_neg(T0), T1s = swap_neg(T1);
                if constexpr (z == 0) {
                    if (kg == 0 && fr == 0) { T0s = tx[0]; T1s = tx[1]; }
                }
                f32x4 zr[2], zi[2];
#pragma unroll
                for (int st = 0; st < 2; ++st) { const f32x4 z0 = {0.f, 0.f, 0.f, 0.f}; zr[st] = mfma(T0, E2[st * 2 + 0], z0); zi[st] = mfma(T0s, E2[st * 2 + 0], z0); }
#pragma unroll
                for (int st = 0; st < 2; ++st) { zr[st] = mfma(T1, E2[st * 2 + 1], zr[st]); zi[st] = mfma(T1s, E2[st * 2 + 1], zi[st]); }
#pragma unroll
                for (int st = 0; st < 2; ++st) zf[z][st] = pack8(zr[st], zi[st]);
            });
#pragma unroll
            for (int z = 0; z < 2; ++z)
#pragma unroll
                for (int dt = 0; dt < 4; ++dt) { acc3[dt][0] = mfma(wz[z][dt], zf[z][0], acc3[dt][0]); acc3[dt][1] = mfma(wz[z][dt], zf[z][1], acc3[dt][1]); }
            p3_rdw(L, cx, kg, lane, 16, wz[0]); p3_rdw(L, cx, kg, lane, 17, wz[1]);
#pragma unroll
            for (int z = 0; z < 2; ++z)
#pragma unroll
                for (int dt = 0; dt < 4; ++dt) { acc3[dt][0] = mfma(wz[z][dt], zf[2 + z][0], acc3[dt][0]); acc3[dt][1] = mfma(wz[z][dt], zf[2 + z][1], acc3[dt][1]); }
        }
        STAMP(10);
        TS(25);
        WAIT_LGKM0(); BAR();
        constexpr int RP = 272;
#pragma unroll
        for (int dt = 0; dt < 4; ++dt)
#pragma unroll
            for (int st = 0; st < 2; ++st)
                *(LAS f32x4*)(L + (kg * 64 + 32 * rh + 16 * st + fr) * RP + (16 * dt + 4 * g) * 4) = acc3[dt][st];
        WAIT_LGKM0(); BAR();
        const int mm = tid >> 3, cg = tid & 7;
        f32x4 v0 = {0.f, 0.f, 0.f, 0.f}, v1 = v0;
#pragma unroll
        for (int q = 0; q < 4; ++q) { v0 += *(LAS f32x4*)(L + (q * 64 + mm) * RP + cg * 32); v1 += *(LAS f32x4*)(L + (q * 64 + mm) * RP + cg * 32 + 16); }
        const size_t o = (size_t)panel_row(r3, 64 * h3 + mm) * 512 + 64 * cc + 8 * cg;
        {
            const u32x4 ov = u32x4{pk_bf16(bf_lo(xres.x) + v0[0], bf_hi(xres.x) + v0[1]), pk_bf16(bf_lo(xres.y) + v0[2], bf_hi(xres.y) + v0[3]),
                                   pk_bf16(bf_lo(xres.z) + v1[0], bf_hi(xres.z) + v1[1]), pk_bf16(bf_lo(xres.w) + v1[2], bf_hi(xres.w) + v1[3])};
            __builtin_nontemporal_store(ov, (GAS u32x4*)(p.out + o));
        }
    }
    STAMP(11);
}

extern "C" void kernel_launch(void* const* d_in, const int* in_sizes, int n_in, void* d_out, int out_size, void* d_ws, size_t ws_size,
                              hipStream_t stream) {
    Params p;
    p.x = (const uint16_t*)d_in[0]; p.wr = (const uint16_t*)d_in[1]; p.wi = (const uint16_t*)d_in[2];
    p.wg = (const uint16_t*)d_in[4]; p.wu = (const uint16_t*)d_in[5]; p.wd = (const uint16_t*)d_in[6];
    p.out = (uint16_t*)d_out; p.ws = (unsigned char*)d_ws;
    (void)hipFuncSetAttribute((const void*)k_fused, hipFuncAttributeMaxDynamicSharedMemorySize, LDS_BYTES);
    k_fused<<<256, NT, LDS_BYTES, stream>>>(p);
}
```

```cpp
#define OUT_NT
#include <hip/hip_runtime.h>
#include <stdint.h>

#define LAS __attribute__((address_space(3)))
#define GAS __attribute__((address_space(1)))
#define DI __device__ __forceinline__
typedef __bf16 bf8 __attribute__((ext_vector_type(8)));
typedef __bf16 bf2 __attribute__((ext_vector_type(2)));
typedef short s16x4 __attribute__((ext_vector_type(4)));
typedef short s16x8 __attribute__((ext_vector_type(8)));
typedef float f32x4 __attribute__((ext_vector_type(4)));
typedef float f32x2 __attribute__((ext_vector_type(2)));
typedef unsigned u32x4 __attribute__((ext_vector_type(4)));
typedef unsigned u32x2 __attribute__((ext_vector_type(2)));
typedef GAS unsigned gu32;
typedef GAS unsigned long long gu64;
#define RLX_AGENT __ATOMIC_RELAXED, __HIP_MEMORY_SCOPE_AGENT

namespace tabs {
constexpr double PI = 3.14159265358979323846264338327950288;
constexpr double cos_q(double x) {
    double x2 = x * x, t = 1.0, s = 1.0;
    for (int i = 1; i <= 14; ++i) { t *= -x2 / ((2 * i - 1) * (2 * i)); s += t; }
    return s;
}
constexpr uint16_t f2bf(float f) {
    uint32_t u = __builtin_bit_cast(uint32_t, f);
    u += 0x7FFFu + ((u >> 16) & 1u);
    return (uint16_t)(u >> 16);
}
struct Base { uint16_t v[512];
    constexpr Base() : v{} {
        for (int m = 0; m <= 128; ++m) {
            double c = (m == 128) ? 0.0 : cos_q(2.0 * PI * m / 512.0);
            uint16_t b = f2bf((float)c);
            uint16_t nb = (m == 128) ? b : (uint16_t)(b ^ 0x8000u);
            v[m] = b; v[(512 - m) & 511] = b;
            v[256 - m] = nb; v[(256 + m) & 511] = nb;
        }
    }
};
constexpr Base base{};
constexpr uint16_t cosb(int m) { return base.v[m & 511]; }
constexpr uint16_t nsinb(int m) { return base.v[(m + 128) & 511]; }
constexpr uint16_t sinb(int m) { return base.v[(m + 384) & 511]; }
struct Chunk { uint16_t v[16384]; };
constexpr Chunk mk_fwd(int c) {
    Chunk r{};
    for (int d = 0; d < 512; ++d)
        for (int n = 0; n < 32; ++n) { const int k = 16 * c + (n & 15);
            r.v[d * 32 + n] = base.v[((n >> 4) ? (k == 0 ? d * 256 : d * k + 128) : d * k) & 511]; }
    return r;
}
constexpr Chunk mk_inv(int blk) {
    Chunk r{};
    const int cc = blk >> 1;
    for (int e = 0; e < 16384; e += 8) {
        const int l = (e >> 3) & 63, dt = (e >> 9) & 3, kg = (e >> 11) & 3, s = ((blk & 1) << 1) + (e >> 13);
        const int kt = 4 * kg + s, d = 64 * cc + 16 * dt + (l & 15), k0 = 16 * kt + 4 * (l >> 4);
        for (int j = 0; j < 4; ++j) { const int k = k0 + j;
            r.v[e + j] = base.v[(k * d) & 511];
            r.v[e + 4 + j] = base.v[(k == 0 ? d * 256 : k * d + 128) & 511]; }
    }
    return r;
}
template <int I> constexpr Chunk fwd_c = mk_fwd(I);
template <int I> constexpr Chunk inv_c = mk_inv(I);
struct Big { Chunk c[16]; };
}
#define C16(N) {{tabs::N<0>, tabs::N<1>, tabs::N<2>, tabs::N<3>, tabs::N<4>, tabs::N<5>, tabs::N<6>, tabs::N<7>, \
                 tabs::N<8>, tabs::N<9>, tabs::N<10>, tabs::N<11>, tabs::N<12>, tabs::N<13>, tabs::N<14>, tabs::N<15>}}
alignas(256) static __device__ const tabs::Big g_fwd = C16(fwd_c);
alignas(256) static __device__ const tabs::Big g_inv = C16(inv_c);

constexpr int S = 2048, D = 512, F = 1792, DF = 257, NT = 512;
constexpr size_t WS_CTRL = 0;
constexpr size_t WS_STAT = 98304;
constexpr size_t WS_AP = 131072;
constexpr size_t WS_AX = WS_AP + (2u << 20);
constexpr size_t WS_TX = WS_AX + 8192;
constexpr size_t WS_T = WS_AX + 65536;
constexpr size_t WS_H = WS_T + (2u << 20);
constexpr size_t WS_H2 = WS_H + (size_t)16 * 8 * 48 * 1024;
constexpr size_t WS_END = WS_H2 + (size_t)16 * 8 * 16 * 512;
#define FSTR 32
#define PSLEEP 8
constexpr int W_CT = 1024, W_TMO = 2048, W_FA = 4096, W_FH = W_FA + 256 * 32;
constexpr unsigned SPIN_CAP = 1u << 16;
constexpr int CODE_PF_BYTES = 37 * 1024;

struct Params {
    const uint16_t* x; const uint16_t* wr; const uint16_t* wi;
    const uint16_t* wg; const uint16_t* wu; const uint16_t* wd;
    uint16_t* out; unsigned char* ws;
};

template <int I> struct IC { static constexpr int v = I; };
template <int B, int E, class Fn> DI void static_for(Fn&& f) { if constexpr (B < E) { f(IC<B>{}); static_for<B + 1, E>(f); } }
DI f32x4 mfma(u32x4 a, u32x4 b, f32x4 c) {
    return __builtin_amdgcn_mfma_f32_16x16x32_bf16(__builtin_bit_cast(bf8, a), __builtin_bit_cast(bf8, b), c, 0, 0, 0);
}
DI unsigned pk_bf16(float lo, float hi) { bf2 p; p[0] = (__bf16)lo; p[1] = (__bf16)hi; return __builtin_bit_cast(unsigned, p); }
DI float bf_lo(unsigned u) { return __builtin_bit_cast(float, u << 16); }
DI float bf_hi(unsigned u) { return __builtin_bit_cast(float, u & 0xffff0000u); }
DI float bfr(float x) { return bf_lo(pk_bf16(x, x)); }
DI u32x4 lds128(LAS unsigned char* L, int off) { return *(LAS u32x4*)(L + off); }
DI u32x2 lds_tr(LAS unsigned char* L, int off) {
    return __builtin_bit_cast(u32x2, __builtin_amdgcn_ds_read_tr16_b64_v4i16((LAS s16x4*)(L + off)));
}
DI u32x4 lds_tr2(LAS unsigned char* L, int off0, int off1) {
    const u32x2 a = lds_tr(L, off0), b = lds_tr(L, off1);
    return u32x4{a.x, a.y, b.x, b.y};
}
DI u32x4 swap_neg(u32x4 v) { return u32x4{v.z, v.w, v.x ^ 0x80008000u, v.y ^ 0x80008000u}; }
DI u32x4 pack8(const float (&a)[4], const float (&b)[4]) { return u32x4{pk_bf16(a[0], a[1]), pk_bf16(a[2], a[3]), pk_bf16(b[0], b[1]), pk_bf16(b[2], b[3])}; }
DI u32x4 pack8(f32x4 a, f32x4 b) { return u32x4{pk_bf16(a[0], a[1]), pk_bf16(a[2], a[3]), pk_bf16(b[0], b[1]), pk_bf16(b[2], b[3])}; }
DI void glds16(const void* gsrc, unsigned lds_addr) {
    asm volatile("s_mov_b32 m0, %1\n\ts_nop 0\n\tglobal_load_lds_dwordx4 %0, off" :: "v"(gsrc), "s"(lds_addr) : "memory");
}
DI void glds16s(const void* sbase, unsigned voff, unsigned lds_addr) {
    asm volatile("s_mov_b32 m0, %2\n\ts_nop 0\n\tglobal_load_lds_dwordx4 %0, %1" :: "v"(voff), "s"(sbase), "s"(lds_addr) : "memory");
}
DI void glds4(const void* gsrc, unsigned lds_addr) {
    asm volatile("s_mov_b32 m0, %1\n\ts_nop 0\n\tglobal_load_lds_dword %0, off" :: "v"(gsrc), "s"(lds_addr) : "memory");
}
DI void gl128(u32x4& d, const void* p) { asm volatile("global_load_dwordx4 %0, %1, off" : "=v"(d) : "v"(p) : "memory"); }
DI void gl128_sc1(u32x4& d, const void* base, unsigned off) { asm volatile("global_load_dwordx4 %0, %1, %2 sc1" : "=v"(d) : "v"(off), "s"(base) : "memory"); }
DI void gl64_sc1(u32x2& d, const void* base, unsigned off) { asm volatile("global_load_dwordx2 %0, %1, %2 sc1" : "=v"(d) : "v"(off), "s"(base) : "memory"); }
DI void gl32_sc1(unsigned& d, const void* base, unsigned off) { asm volatile("global_load_dword %0, %1, %2 sc1" : "=v"(d) : "v"(off), "s"(base) : "memory"); }
DI void gl16(unsigned& d, const void* base, unsigned off) { asm volatile("global_load_ushort %0, %1, %2" : "=v"(d) : "v"(off), "s"(base) : "memory"); }
template <int N> DI void wait_vm() { static_assert(N >= 0 && N < 64, "vmcnt"); asm volatile("s_waitcnt vmcnt(%0)" :: "n"(N) : "memory"); }
template <class T> DI void pin(T& v) { asm volatile("" : "+v"(v)); }
#define WAIT_LGKM0() asm volatile("s_waitcnt lgkmcnt(0)" ::: "memory")
#define BAR() do { asm volatile("" ::: "memory"); __builtin_amdgcn_s_barrier(); asm volatile("" ::: "memory"); } while (0)
#define SB() __builtin_amdgcn_sched_barrier(0)
DI void st_sc1(__amdgpu_buffer_rsrc_t rs, int off, u32x4 v) { __builtin_amdgcn_raw_buffer_store_b128(v, rs, off, 0, 16); }
DI void st_sc1_64(__amdgpu_buffer_rsrc_t rs, int off, u32x2 v) { __builtin_amdgcn_raw_buffer_store_b64(v, rs, off, 0, 16); }
template <int CTRL> DI float dpp_add(float v) {
    return v + __builtin_bit_cast(float, __builtin_amdgcn_update_dpp(0, __builtin_bit_cast(int, v), CTRL, 0xf, 0xf, true));
}
DI float wave_sum(float v) {
    v = dpp_add<0xB1>(v);
    v = dpp_add<0x4E>(v);
    v = dpp_add<0x141>(v);
    v = dpp_add<0x140>(v);
    const int vi = __builtin_bit_cast(int, v);
    return (__builtin_bit_cast(float, __builtin_amdgcn_readlane(vi, 0)) + __builtin_bit_cast(float, __builtin_amdgcn_readlane(vi, 16))) +
           (__builtin_bit_cast(float, __builtin_amdgcn_readlane(vi, 32)) + __builtin_bit_cast(float, __builtin_amdgcn_readlane(vi, 48)));
}
DI void give_up(const unsigned char* ws, int lane) { if (lane == 0) __hip_atomic_store((gu32*)ws + W_TMO, 1u, RLX_AGENT); }
DI void poll16(const unsigned char* ws, int word, unsigned tag, int lane) {
#pragma unroll 1
    for (unsigned spins = 0;; ++spins) {
        unsigned f; gl32_sc1(f, ws, (unsigned)(word + (lane & 15) * FSTR) * 4u); wait_vm<0>(); pin(f);
        if (__all(f == tag)) break;
        if (spins > SPIN_CAP) { give_up(ws, lane); break; }
        __builtin_amdgcn_s_sleep(PSLEEP);
    }
}
DI void fx_load(unsigned (&w)[8], unsigned o0, unsigned o1, unsigned o2, unsigned o3, const void* wr, const void* wi, int k4) {
    asm volatile("s_cmp_lg_u32 %14, 0\n\ts_cbranch_scc1 .Lnofx%=\n\t"
                 "global_load_ushort %0, %8, %12\n\tglobal_load_ushort %1, %9, %12\n\tglobal_load_ushort %2, %10, %12\n\tglobal_load_ushort %3, %11, %12\n\t"
                 "global_load_ushort %4, %8, %13\n\tglobal_load_ushort %5, %9, %13\n\tglobal_load_ushort %6, %10, %13\n\tglobal_load_ushort %7, %11, %13\n"
                 ".Lnofx%=:" : "=&v"(w[0]), "=&v"(w[1]), "=&v"(w[2]), "=&v"(w[3]), "=&v"(w[4]), "=&v"(w[5]), "=&v"(w[6]), "=&v"(w[7])
                 : "v"(o0), "v"(o1), "v"(o2), "v"(o3), "s"(wr), "s"(wi), "s"(k4) : "memory", "scc");
}
DI float cosr(float x) { return __builtin_amdgcn_cosf(x); }
DI float sinr(float x) { return __builtin_amdgcn_sinf(x); }
DI u32x4 frag_cs(int a, int b0, int n) {
    float c[4], sn[4];
#pragma unroll
    for (int j = 0; j < 4; ++j) { const float th = (float)((a * (b0 + j)) & (n - 1)) * (1.0f / n); c[j] = cosr(th); sn[j] = -sinr(th); }
    return pack8(c, sn);
}

#define STAMP(i) do {} while (0)
#define TS(i) do {} while (0)

constexpr int P1_STAGE = 49152, P1_BOFF = 16384;
constexpr int L_MISC = 147456;
constexpr int L_XI = L_MISC;
constexpr int L_ST = L_MISC + 8704;
constexpr int L_YX = L_ST + 1024;
constexpr int L_SCR = L_YX + 4096;
constexpr int L_FLG = L_SCR + 256;
constexpr int L_END = L_FLG + 16;
static_assert(L_END <= 163840, "lds");
constexpr int LDS_BYTES = 163840;
constexpr int L_RAW = 98304;
constexpr int P3_SLOT = 16384;
DI constexpr int p3_slot(int s) { return (s % 9) * P3_SLOT; }

DI int panel_row(int r, int m) { return 64 * (m & 31) + 4 * r + (m >> 5); }
DI int p3_ks(int s, int kg) { const int n = 4 * s + kg; if (s >= 12) return 7 * (n - 48) + 3; const int p = n / 6, o = n - 6 * p; return 7 * p + o + (o >= 3); }

struct LdSrc { unsigned a[4]; const unsigned char* b[8]; long sB64; const unsigned char* t; long tadv; };
DI void p1_issue(LdSrc& s, const unsigned char* x, unsigned Lb, int lw, int j) {
    const unsigned sb = Lb + (j % 3) * P1_STAGE;
#pragma unroll
    for (int i = 0; i < 4; ++i) glds16s(x + j * 128, s.a[i], sb + (4 * lw + i) * 1024);
#pragma unroll
    for (int u = 0; u < 8; ++u) { const int i = (lw & 1) + 2 * (u & 1) + 4 * (lw >> 1) + 8 * ((u >> 1) & 1) + 16 * (u >> 2);
        glds16(s.b[u], sb + P1_BOFF + i * 1024); s.b[u] += s.sB64; }
}
DI void p1_touch(LdSrc& s, unsigned Lb) { glds4(s.t, Lb + L_SCR); s.t += s.tadv; }
DI void p3_issue(const Params& p, unsigned Lb, int lw, int cc, int s, unsigned wv, unsigned fv) {
    const unsigned sb = Lb + p3_slot(s) + lw * 4096;
    if (s < 14) { const unsigned char* base = (const unsigned char*)p.wd + (size_t)p3_ks(s, lw) * 32768;
#pragma unroll
        for (int i = 0; i < 4; ++i) glds16s(base + i * 8192, wv, sb + i * 1024); }
    else { const unsigned char* base = (const unsigned char*)&g_inv + cc * 65536 + (s - 14) * 16384 + lw * 4096;
#pragma unroll
        for (int i = 0; i < 4; ++i) glds16s(base + i * 1024, fv, sb + i * 1024); }
}

DI void p3_issue2(const Params& p, unsigned Lb, int w, int cc, int s, unsigned wv, unsigned fv) {
    const unsigned sb = Lb + p3_slot(s) + w * 2048;
    if (s < 14) { const unsigned char* base = (const unsigned char*)p.wd + (size_t)p3_ks(s, w >> 1) * 32768 + (w & 1) * 16384;
        glds16s(base, wv, sb); glds16s(base + 8192, wv, sb + 1024); }
    else { const unsigned char* base = (const unsigned char*)&g_inv + cc * 65536 + (s - 14) * 16384 + w * 2048;
        glds16s(base, fv, sb); glds16s(base + 1024, fv, sb + 1024); }
}

template <bool DFTW>
DI void p1_compute(LAS unsigned char* L, unsigned Lb, int wc, int lane, f32x4 (&acc)[4][8], f32x4 (&csm)[4], unsigned (&tsv)[32]) {
    asm volatile("" : "+v"(lane));
    const int fr = lane & 15, g = lane >> 4;
    const int qq = fr >> 2, p = lane & 3;
    int aoff0 = fr * 128 + (((0 + g) ^ (fr & 7)) << 4);
    int aoff1 = fr * 128 + (((4 + g) ^ (fr & 7)) << 4);
    const int sw = 2 * qq + 8 * (g & 1);
    int boff[4];
#pragma unroll
    for (int q = 0; q < 4; ++q) boff[q] = P1_BOFF + (8 * g + qq) * 512 + (((8 * wc + 2 * q + (p >> 1)) ^ sw) << 4) + 8 * (p & 1);
    u32x4 xa[2][4], wb[2][4];
    auto rdx = [&](int ks, int mh, int i) { xa[mh][i] = lds128(L, (ks ? aoff1 : aoff0) + (4 * mh + i) * 2048); };
    auto rdw = [&](int ks, int q) { const int o = boff[q] + ks * 16384; wb[ks][q] = lds_tr2(L, o, o + 2048); };
    auto rebase = [&](int d) { aoff0 += d; aoff1 += d; boff[0] += d; boff[1] += d; boff[2] += d; boff[3] += d;
        asm volatile("" : "+v"(aoff0), "+v"(aoff1), "+v"(boff[0]), "+v"(boff[1]), "+v"(boff[2]), "+v"(boff[3])); };
    auto step = [&](int ks, int mh, bool more) {
#pragma unroll
        for (int i = 0; i < 16; ++i) {
            const int q = i >> 2, mt = i & 3;
            if (DFTW && q >= 2) acc[q][4 * mh + mt] = mfma(xa[mh][mt], wb[ks][q], acc[q][4 * mh + mt]);
            else acc[q][4 * mh + mt] = mfma(wb[ks][q], xa[mh][mt], acc[q][4 * mh + mt]);
            if (more) {
                if (mh == 0) { if (i >= 1 && i <= 4) rdx(ks, 1, i - 1); }
                else { if (i < 4) rdx(ks ^ 1, 0, i); else if (i < 8) rdw(ks ^ 1, i - 4); }
            }
            if (mh == 0 && (i & 3) == 3) { const int d = i >> 2; if (!DFTW || d < 2) csm[d] = mfma(wb[ks][d], u32x4{0x3F803F80u, 0x3F803F80u, 0x3F803F80u, 0x3F803F80u}, csm[d]); }
            SB();
        }
    };
    BAR();
    TS(0);
#pragma unroll
    for (int i = 0; i < 4; ++i) { rdx(0, 0, i); rdw(0, i); }
    SB();
    static_for<0, 8>([&](auto J) {
        constexpr int j = J.v;
        step(0, 0, true);
        step(0, 1, true);
        step(1, 0, true);
        if constexpr (j < 7) { WAIT_LGKM0(); BAR(); TS(j + 1); rebase((j + 1) % 3 == 0 ? -2 * P1_STAGE : P1_STAGE);
            SB(); }
        step(1, 1, j < 7);
    });
    TS(8);
}

constexpr int HD = 5;
constexpr int FTS = 10, FTC = 12;
constexpr int p3_issue_at(int o) { return o < 14 ? o - HD : FTC; }
constexpr int p3_opcnt(int o) { return (o >= 12 && o <= 14) ? 4 : 2; }
enum { K_OP, K_TST, K_W, K_FT };
constexpr int p3_after(int kind, int idx, int s_now) {
    int n = -1;
    auto hit = [&](bool is, int cnt) { if (is) n = 0; else if (n >= 0) n += cnt; };
    for (int o = 0; o < HD; ++o) hit(kind == K_OP && idx == o, 2);
    hit(kind == K_TST, 1); hit(false, 1);
    for (int s = 0; s < s_now; ++s) {
        for (int o = HD; o < 18; ++o) if (p3_issue_at(o) == s) hit(kind == K_OP && idx == o, p3_opcnt(o));
        if (s >= 3 && s + 6 < 18) hit(kind == K_W && idx == s + 6, 2);
        if (s == FTS) hit(kind == K_FT, 1);
    }
    return n;
}
constexpr int cmin(int a, int b) { return a < b ? a : b; }
constexpr int p3_wait(int s) {
    int n = p3_after(K_OP, s, s);
    if (s == 3) n = cmin(n, p3_after(K_TST, 0, s));
    if (s % 3 == 0 && s >= 9) n = cmin(n, p3_after(K_W, s + 2, s));
    if (s == FTC) n = cmin(n, p3_after(K_FT, 0, s));
    return n;
}
static_assert(p3_wait(0) == 2 * (HD - 1) + 2 && p3_wait(3) == 1 + 6, "queue model");

struct P3Ctx { unsigned hbase, h2base, tbase; int woff[4]; };
DI void p3_rdw(LAS unsigned char* L, const P3Ctx& c, int kg, int lane, int s, u32x4 (&wf)[4]) {
#pragma unroll
    for (int dt = 0; dt < 4; ++dt) {
        if (s < 14) wf[dt] = lds_tr2(L, p3_slot(s) + c.woff[dt], p3_slot(s) + c.woff[dt] + 2048);
        else wf[dt] = lds128(L, p3_slot(s) + (kg * 4 + dt) * 1024 + lane * 16);
    }
}

__global__ void __launch_bounds__(NT, 2) k_fused(Params p) {
    extern __shared__ __attribute__((aligned(16))) unsigned char smem[];
    LAS unsigned char* L = (LAS unsigned char*)smem;
    const unsigned Lb = (unsigned)(size_t)L;
    const int wid = __builtin_amdgcn_readfirstlane(threadIdx.x >> 6);
    const int wc = wid & 3;
    const int b = blockIdx.x, xcd = b & 7, bi = b >> 3;
    const int r = (xcd & 1) * 8 + (bi & 7), c = (xcd >> 1) * 4 + (bi >> 3);
    const int jq = b & 7, kq = b >> 3;
    const int cc = (xcd & 1) * 4 + (bi & 3), hp = (xcd >> 1) * 8 + (bi >> 2), r3 = hp >> 1, h3 = hp & 1;
    const unsigned char* ws = p.ws;
    unsigned tsv[32];
#pragma unroll
    for (int i = 0; i < 32; ++i) tsv[i] = 0;
    STAMP(0);
    TS(31);
    const int odd = c & 1;
    const int u2 = wid >> 2, w2 = wid & 3, k4 = 2 * kq + u2;
    LAS f32x2* ST = (LAS f32x2*)(L + L_ST);
    f32x4 acc[4][8], csm[4];
    u32x4 eaC[2], eaS[2]; float tcA[2][4], tsA[2][4];
#pragma unroll
    for (int i = 0; i < 4; ++i) csm[i] = f32x4{0.f, 0.f, 0.f, 0.f};
    unsigned tag;
    {
    const int tid = threadIdx.x, lane = tid & 63;
    if (wid >= 4) {
        const int lw = wid - 4;
        unsigned epoch; gl32_sc1(epoch, ws, 0u);
        LdSrc ls;
        {
            const int idx = lw * 22 + (lane < 22 ? lane : 0);
            if (idx < 32) { ls.t = (const unsigned char*)p.x + (size_t)panel_row(r, 32 * (c & 3) + idx) * 1024; ls.tadv = 128; }
            else { const int i = idx - 32, row = 8 * (r & 7) + i / 7, it = i % 7;
                if (it == 6) { ls.t = (const unsigned char*)&g_fwd + c * 32768 + row * 64; ls.tadv = 64 * 64; }
                else { const int it3 = it % 3; ls.t = (const unsigned char*)(it < 3 ? p.wg : p.wu) + (size_t)row * (F * 2) + c * 224 + (it3 == 2 ? 220 : it3 * 128); ls.tadv = 64L * F * 2; } }
        }
        p1_touch(ls, Lb);
        SB();
#pragma unroll
        for (int i = 0; i < 4; ++i) { const int m = 8 * (4 * lw + i) + (lane >> 3); ls.a[i] = (unsigned)panel_row(r, m) * 1024u + (((lane & 7) ^ (lane >> 3)) << 4); }
        {
            const int b0 = lw & 1, b2 = lw >> 1;
            const int swz = 2 * (2 * b0 + (lane >> 5)) + 8 * b2;
            const int ch = (lane & 31) ^ swz, nt = ch >> 1, half = ch & 1, cw = nt >> 2, q = nt & 3;
            const unsigned char* base; int col, strideB;
            if (cw < 3) { const int ft = odd + 2 * cw + (q & 1); base = (const unsigned char*)(q < 2 ? p.wg : p.wu); col = 112 * c + 16 * ft + 8 * half; strideB = F * 2; }
            else if (q < 2) { const int ft = odd ? 0 : 6; base = (const unsigned char*)(q == 0 ? p.wg : p.wu); col = 112 * c + 16 * ft + 8 * half; strideB = F * 2; }
            else { base = (const unsigned char*)&g_fwd + c * 32768; col = 16 * (q - 2) + 8 * half; strideB = 64; }
            const unsigned char* b00 = base + (size_t)(2 * b0 + 8 * b2 + (lane >> 5)) * strideB + col * 2;
#pragma unroll
            for (int u = 0; u < 8; ++u) ls.b[u] = b00 + (size_t)(4 * (u & 1) + 16 * ((u >> 1) & 1) + 32 * (u >> 2)) * strideB;
            ls.sB64 = (long)strideB * 64;
        }
        p1_issue(ls, (const unsigned char*)p.x, Lb, lw, 0);
        p1_touch(ls, Lb); p1_touch(ls, Lb); p1_touch(ls, Lb);
        p1_issue(ls, (const unsigned char*)p.x, Lb, lw, 1);
        STAMP(14);
        if (wid == 4 && lane < 4) *((volatile LAS unsigned*)(L + L_FLG) + lane) = 0u;
        wait_vm<15>(); pin(epoch); tag = epoch + 1u;
        if (r == 0 && tid == 256) __hip_atomic_store((gu32*)p.ws + W_CT + c * 32, tag << 8, RLX_AGENT);
        WAIT_LGKM0();
        STAMP(15);
        BAR();
        p1_touch(ls, Lb);
        p1_issue(ls, (const unsigned char*)p.x, Lb, lw, 2);
        SB();
        const unsigned char *st0, *st1;
        {
            auto wline = [&](int i) { const int st = i >> 2, ln = (bi >> 2) * 16 + lw * 4 + (i & 3);
                return st < 14 ? (const unsigned char*)p.wd + (size_t)(32 * p3_ks(st, ln >> 5) + (ln & 31)) * 1024 + cc * 128
                               : (const unsigned char*)&g_inv + (size_t)cc * 65536 + (st - 14) * 16384 + ln * 128; };
            st0 = wline(lane);
            const int fi = (bi * 4 + lw) * 20 + (lane >= 8 && lane < 28 ? lane - 8 : 0);
            const int arr = fi / 1280, rr = (fi % 1280) / 5, ll = (fi % 1280) % 5;
            const int jrow = 4 * jq + (rr & 3) + 32 * (rr >> 2);
            st1 = lane < 8 ? wline(64 + lane) : (const unsigned char*)(arr ? p.wi : p.wr) + (size_t)jrow * (DF * 2) + min(ll * 128, 510);
        }
        u32x2 gran; const unsigned goff = (unsigned)WS_STAT + (unsigned)(r * 128 + (tid & 127)) * 8u;
        static_for<0, 7>([&](auto J) {
            constexpr int j = J.v;
            if constexpr (j < 4) wait_vm<13>(); else if constexpr (j == 4) wait_vm<12>();
            else if constexpr (j == 5) wait_vm<15>(); else wait_vm<3>();
            BAR();
            if constexpr (j < 3) p1_touch(ls, Lb);
            if constexpr (j + 3 < 8) p1_issue(ls, (const unsigned char*)p.x, Lb, lw, j + 3);
            if constexpr (j == 4) {
                gl64_sc1(gran, ws, goff);
                glds4(st0, Lb + L_SCR); glds4(st1, Lb + L_SCR);
            }
        });
        wait_vm<2>();
        pin(gran);
        if (lw < 2) {
            if (!__all(gran.y == tag)) {
#pragma unroll 1
                for (unsigned spins = 0;; ++spins) {
                    gl64_sc1(gran, ws, goff); wait_vm<0>(); pin(gran);
                    if (__all(gran.y == tag)) break;
                    if (spins > SPIN_CAP) { give_up(ws, lane); break; }
                    __builtin_amdgcn_s_sleep(1);
                }
            }
            ST[tid & 127] = f32x2{bf_hi(gran.x), 1.0f / bf_lo(gran.x)};
        }
        {
            const int fr = lane & 15, g = lane >> 4;
#pragma unroll
            for (int jt = 0; jt < 2; ++jt) {
                float cv[8], sv[8];
#pragma unroll
                for (int j = 0; j < 8; ++j) { const float th = (float)(((16 * jt + fr) * (8 * g + j)) & 31) * (1.0f / 32.0f); cv[j] = cosr(th); sv[j] = sinr(th); }
                eaC[jt] = u32x4{pk_bf16(cv[0], cv[1]), pk_bf16(cv[2], cv[3]), pk_bf16(cv[4], cv[5]), pk_bf16(cv[6], cv[7])};
                eaS[jt] = u32x4{pk_bf16(sv[0], sv[1]), pk_bf16(sv[2], sv[3]), pk_bf16(sv[4], sv[5]), pk_bf16(sv[6], sv[7])};
#pragma unroll
                for (int e = 0; e < 4; ++e) { const float th = (float)((16 * jt + fr) * (4 * r + e)) * (1.0f / 2048.0f); tcA[jt][e] = cosr(th); tsA[jt][e] = sinr(th); }
            }
        }
#pragma unroll
        for (int q = 0; q < 4; ++q)
#pragma unroll
            for (int mt = 0; mt < 8; ++mt) acc[q][mt] = f32x4{0.f, 0.f, 0.f, 0.f};
    } else {
        if (wid == 0) {
            const unsigned char* pc = (const unsigned char*)__builtin_amdgcn_s_getpc();
            const int ln = bi * 10 + (lane < 10 ? lane : 0);
            glds4(pc - ((size_t)pc & 127) + (size_t)min(ln * 128, CODE_PF_BYTES), Lb + L_SCR);
        }
        if (wid == 1 || wid == 2) {
            const unsigned char* a;
            if (wid == 1) a = lane < 32 ? (const unsigned char*)p.x + (size_t)lane * 65536 : (const unsigned char*)p.wg + (size_t)min(lane - 32, 27) * 65536;
            else a = lane < 32 ? (const unsigned char*)p.wu + (size_t)min(lane, 27) * 65536 : (const unsigned char*)&g_fwd + (size_t)(lane & 7) * 65536;
            glds4(a, Lb + L_SCR);
        }
        unsigned epoch; gl32_sc1(epoch, ws, 0u);
        u32x4 xrow[2];
#pragma unroll
        for (int i = 0; i < 2; ++i) gl128(xrow[i], (const unsigned char*)p.x + (size_t)panel_row(r, 8 * c + 2 * wid + i) * 1024 + lane * 16);
        wait_vm<0>(); pin(epoch); pin(xrow[0]); pin(xrow[1]);
        tag = epoch + 1u;
#pragma unroll
        for (int i = 0; i < 2; ++i) {
            float v[8];
#pragma unroll
            for (int e = 0; e < 4; ++e) { v[2 * e] = bf_lo(xrow[i][e]); v[2 * e + 1] = bf_hi(xrow[i][e]); }
            float s = ((v[0] + v[1]) + (v[2] + v[3])) + ((v[4] + v[5]) + (v[6] + v[7]));
            s = wave_sum(s);
            const float mean = s * (1.0f / 512.0f);
            float q2 = 0.f;
#pragma unroll
            for (int e = 0; e < 8; ++e) { const float d = v[e] - mean; q2 += d * d; }
            q2 = wave_sum(q2);
            const float var = q2 * (1.0f / 512.0f);
            const float m_bf = bfr(mean), v_bf = bfr(var), s_bf = bfr(sqrtf(bfr(v_bf + 1e-5f)));
            const unsigned val = (__builtin_bit_cast(unsigned, m_bf) & 0xffff0000u) | (__builtin_bit_cast(unsigned, s_bf) >> 16);
            if (lane == 0) __hip_atomic_store((gu64*)(p.ws + WS_STAT) + (r * 128 + 8 * c + 2 * wid + i), ((unsigned long long)tag << 32) | val, RLX_AGENT);
        }
#pragma unroll
        for (int q = 0; q < 4; ++q)
#pragma unroll
            for (int mt = 0; mt < 8; ++mt) acc[q][mt] = f32x4{0.f, 0.f, 0.f, 0.f};
        STAMP(1);
        if (wid < 3) p1_compute<false>(L, Lb, wc, lane, acc, csm, tsv); else p1_compute<true>(L, Lb, wc, lane, acc, csm, tsv);
        STAMP(2);
    }
    }
    int tz = threadIdx.x; asm volatile("" : "+v"(tz));
    const int tid = tz, lane = tz & 63, fr = lane & 15, g = lane >> 4;
    const __amdgpu_buffer_rsrc_t rs = __builtin_amdgcn_make_buffer_rsrc((void*)p.ws, 0, (int)WS_END, 0x00020000);
    const unsigned wv = (unsigned)(lane >> 3) * 1024u + cc * 128u + (unsigned)(((lane & 7) ^ (2 * ((lane >> 4) & 3))) << 4), fv = lane * 16u;
    volatile LAS unsigned* flg = (volatile LAS unsigned*)(L + L_FLG);
    WAIT_LGKM0(); BAR();
    STAMP(3);
    const int fj1 = 4 * jq + (fr & 3), fk = 4 * k4 + (fr >> 2);
    unsigned fw[4], fwi[4];
    auto signal = [&](int cnt, int word) {
        if (lane == 0) { const unsigned old = __hip_atomic_fetch_add((LAS unsigned*)(L + L_FLG) + cnt, 1u, __ATOMIC_RELAXED, __HIP_MEMORY_SCOPE_WORKGROUP);
            if (old == 3u) __hip_atomic_store((gu32*)p.ws + word, tag, RLX_AGENT); }
    };
    if (wid < 3) {
        const f32x4 cg0 = csm[0], cg1 = csm[1], cu0 = csm[2], cu1 = csm[3];
#pragma unroll
        for (int mt = 0; mt < 8; ++mt) {
            const f32x2 st = ST[16 * mt + fr];
            const float r2 = st.y * st.y;
            const f32x4 h0 = (acc[0][mt] - st.x * cg0) * (acc[2][mt] - st.x * cu0) * r2;
            const f32x4 h1 = (acc[1][mt] - st.x * cg1) * (acc[3][mt] - st.x * cu1) * r2;
            st_sc1(rs, (int)WS_H + ((((r * 8 + mt) * 48 + 3 * c + wc) * 64 + lane) << 4), pack8(h0, h1));
        }
    } else if (wid == 3) {
#pragma unroll
        for (int q = 0; q < 2; ++q)
#pragma unroll
            for (int mt = 0; mt < 8; ++mt) *(LAS f32x4*)(L + L_RAW + ((q * 8 + mt) * 64 + lane) * 16) = acc[2 + q][mt];
        WAIT_LGKM0();
        flg[0] = 1u;
        const f32x4 cg0 = csm[0], cu0 = csm[1];
#pragma unroll
        for (int mt = 0; mt < 8; ++mt) {
            const f32x2 st = ST[16 * mt + fr];
            const float r2 = st.y * st.y;
            const f32x4 h0 = (acc[0][mt] - st.x * cg0) * (acc[1][mt] - st.x * cu0) * r2;
            st_sc1_64(rs, (int)WS_H2 + ((((r * 8 + mt) * 16 + c) * 64 + lane) << 3), u32x2{pk_bf16(h0[0], h0[1]), pk_bf16(h0[2], h0[3])});
        }
    } else {
        const int mt = wid - 4;
        const int kq_ = fr >> 2, s2l = fr & 3;
        __builtin_amdgcn_s_setprio(3);
#pragma unroll 1
        for (unsigned spins = 0; flg[0] == 0u && spins < (1u << 20); ++spins) __builtin_amdgcn_s_sleep(1);
        asm volatile("" ::: "memory");
        u32x4 are, aim;
        {
            const int m0 = 32 * s2l + 8 * g, tl = 2 * s2l + (g >> 1), ln = 4 * mt + kq_ + 32 * (g & 1);
            const f32x4 r0 = *(LAS f32x4*)(L + L_RAW + (tl * 64 + ln) * 16), r1 = *(LAS f32x4*)(L + L_RAW + (tl * 64 + ln + 16) * 16);
            const f32x4 i0 = *(LAS f32x4*)(L + L_RAW + ((8 + tl) * 64 + ln) * 16), i1 = *(LAS f32x4*)(L + L_RAW + ((8 + tl) * 64 + ln + 16) * 16);
            const f32x4 sa = *(LAS f32x4*)(ST + m0), sb = *(LAS f32x4*)(ST + m0 + 2), sc = *(LAS f32x4*)(ST + m0 + 4), sd = *(LAS f32x4*)(ST + m0 + 6);
            const float csf = (c == 0 && mt == 0 && kq_ == 0) ? 512.0f : 0.0f;
            are = u32x4{pk_bf16((r0[0] - sa[0] * csf) * sa[1], (r0[1] - sa[2] * csf) * sa[3]), pk_bf16((r0[2] - sb[0] * csf) * sb[1], (r0[3] - sb[2] * csf) * sb[3]),
                        pk_bf16((r1[0] - sc[0] * csf) * sc[1], (r1[1] - sc[2] * csf) * sc[3]), pk_bf16((r1[2] - sd[0] * csf) * sd[1], (r1[3] - sd[2] * csf) * sd[3])};
            aim = u32x4{pk_bf16(i0[0] * sa[1], i0[1] * sa[3]), pk_bf16(i0[2] * sb[1], i0[3] * sb[3]), pk_bf16(i1[0] * sc[1], i1[1] * sc[3]), pk_bf16(i1[2] * sd[1], i1[3] * sd[3])};
        }
        const f32x4 z = {0.f, 0.f, 0.f, 0.f};
        const bool special = (c == 0 && mt == 0 && g == 0);
#pragma unroll
        for (int jt = 0; jt < 2; ++jt) {
            const f32x4 Prc = mfma(are, eaC[jt], z), Pis = mfma(aim, eaS[jt], z), Prs = mfma(are, eaS[jt], z), Pic = mfma(aim, eaC[jt], z);
            const int j1 = 16 * jt + fr;
            float pr[4], qi[4], pr2[4], qi2[4];
#pragma unroll
            for (int e = 0; e < 4; ++e) {
                const float cw = tcA[jt][e], sw = tsA[jt][e];
                const float ar = special ? Prc[e] : Prc[e] + Pis[e], ai = special ? -Prs[e] : Pic[e] - Prs[e];
                pr[e] = ar * cw + ai * sw; qi[e] = -(ai * cw - ar * sw);
                const float br = Pic[e], bi2 = -Pis[e];
                pr2[e] = br * cw + bi2 * sw; qi2[e] = -(bi2 * cw - br * sw);
            }
            const int U = (j1 >> 2) * 64 + 4 * c + mt, lp = 16 * (r & 3) + 4 * g + (j1 & 3);
            st_sc1(rs, (int)WS_AP + (((U * 4 + (r >> 2)) * 64 + lp) << 4), pack8(pr, qi));
            if (special) st_sc1(rs, (int)WS_AX + (((((j1 >> 2) * 4 + (r >> 2)) * 4 + (r & 3)) * 4 + (j1 & 3)) << 4), pack8(pr2, qi2));
        }
        __builtin_amdgcn_s_setprio(0);
        wait_vm<0>();
        signal(1, W_FA + (c * 16 + r) * FSTR);
#pragma unroll
        for (int st = 0; st < 3; ++st) p3_issue(p, Lb, mt, cc, st, wv, fv);
    }
#pragma unroll
    for (int e = 0; e < 4; ++e) { const unsigned o = (unsigned)((fj1 + 32 * (16 * w2 + 4 * g + e)) * DF + fk) * 2u; gl16(fw[e], p.wr, o); gl16(fwi[e], p.wi, o); }
    unsigned fx[8];
    {
        const unsigned o0 = (unsigned)((fj1 + 32 * (16 * w2 + 4 * g)) * DF + 256) * 2u;
        fx_load(fx, o0, o0 + 32u * DF * 2u, o0 + 64u * DF * 2u, o0 + 96u * DF * 2u, p.wr, p.wi, k4);
    }
    if (wid < 4) {
        if (k4 == 0) wait_vm<16>(); else wait_vm<8>();
        signal(2, W_FH + (r * 16 + c) * FSTR);
#pragma unroll
        for (int st = 3; st < 6; ++st) p3_issue(p, Lb, wid, cc, st, wv, fv);
#pragma unroll 1
        for (unsigned spins = 0; flg[1] < 4u && spins < (1u << 22); ++spins) __builtin_amdgcn_s_sleep(1);
        asm volatile("" ::: "memory");
#pragma unroll
        for (int st = 6; st < 9; ++st) p3_issue(p, Lb, wid, cc, st, wv, fv);
    }
    STAMP(5);
    {
        const int kg = wid & 3, rh = wid >> 2;
        u32x4 E1[4], E2[4];
#pragma unroll
        for (int t = 0; t < 4; ++t) E1[t] = frag_cs(16 * w2 + fr, 16 * t + 4 * g, 64);
#pragma unroll
        for (int i = 0; i < 4; ++i) E2[i] = frag_cs(16 * (i >> 1) + fr, 16 * (i & 1) + 4 * g, 32);
        float tc[4], tsn[4];
#pragma unroll
        for (int e = 0; e < 4; ++e) { const float th = (float)((4 * jq + e) * (16 * w2 + fr)) * (1.0f / 2048.0f); tc[e] = cosr(th); tsn[e] = sinr(th); }
        P3Ctx cx;
        {
            const int qq = fr >> 2, pp = lane & 3, row = 32 * kg + 4 * g + qq, sw = 2 * ((row >> 1) & 3);
#pragma unroll
            for (int dt = 0; dt < 4; ++dt) cx.woff[dt] = row * 128 + (((2 * dt + (pp >> 1)) ^ sw) << 4) + 8 * (pp & 1);
            cx.hbase = (unsigned)WS_H + (unsigned)(((r3 * 8 + 4 * h3 + 2 * rh) * 48 + kg) * 64 + lane) * 16u;
            cx.h2base = (unsigned)WS_H2 + (unsigned)(((r3 * 8 + 4 * h3 + 2 * rh) * 16 + 2 * kg) * 64 + lane) * 8u;
            cx.tbase = (unsigned)WS_T + (unsigned)((((4 * r3 + 2 * h3 + rh) * 16 + 4 * kg) * 2) * 64 + lane) * 16u;
        }
        SB();
        STAMP(6);
        if (wid == 0) poll16(ws, W_FA + (kq >> 1) * 16 * FSTR, tag, lane);
        if (wid == 1) poll16(ws, W_FH + r3 * 16 * FSTR, tag, lane);
        BAR();
        STAMP(7);
        const int U = jq * 64 + k4;
        u32x4 Bf[4], Bx[4];
        u32x4 hb[HD][2]; u32x2 hh[2][4];
#pragma unroll
        for (int t = 0; t < 4; ++t) gl128_sc1(Bf[t], ws, (unsigned)WS_AP + (unsigned)((U * 4 + t) * 64 + lane) * 16u);
        {
            const unsigned xo = (unsigned)WS_AX + (unsigned)((jq * 16 + g) * 4 + (fr & 3)) * 16u;
            asm volatile("s_cmp_lg_u32 %6, 0\n\ts_cbranch_scc1 .Lnox%=\n\t"
                         "global_load_dwordx4 %0, %4, %5 sc1\n\tglobal_load_dwordx4 %1, %4, %5 offset:256 sc1\n\t"
                         "global_load_dwordx4 %2, %4, %5 offset:512 sc1\n\tglobal_load_dwordx4 %3, %4, %5 offset:768 sc1\n"
                         ".Lnox%=:" : "=&v"(Bx[0]), "=&v"(Bx[1]), "=&v"(Bx[2]), "=&v"(Bx[3]) : "v"(xo), "s"(ws), "s"(k4) : "memory", "scc");
        }
        static_for<0, HD>([&](auto Sx) { constexpr int o = Sx.v;
            gl128_sc1(hb[o][0], ws, cx.hbase + o * 4096u); gl128_sc1(hb[o][1], ws, cx.hbase + 48u * 1024u + o * 4096u); });
        wait_vm<2 * HD>();
        pin(Bf[0]); pin(Bf[1]); pin(Bf[2]); pin(Bf[3]); pin(Bx[0]); pin(Bx[1]); pin(Bx[2]); pin(Bx[3]);
        pin(fw[0]); pin(fw[1]); pin(fw[2]); pin(fw[3]); pin(fwi[0]); pin(fwi[1]); pin(fwi[2]); pin(fwi[3]);
        pin(fx[0]); pin(fx[1]); pin(fx[2]); pin(fx[3]); pin(fx[4]); pin(fx[5]); pin(fx[6]); pin(fx[7]);
        STAMP(8);
        {
            f32x4 a1 = {0.f, 0.f, 0.f, 0.f}, a2 = a1;
#pragma unroll
            for (int t = 0; t < 4; ++t) { a1 = mfma(E1[t], Bf[t], a1); a2 = mfma(E1[t], swap_neg(Bf[t]), a2); }
            constexpr float SC = 2.0f / (2048.0f * 512.0f);
            const float sc = (k4 == 0 && (fr >> 2) == 0) ? 0.5f * SC : SC;
            float yr[4], yi[4];
#pragma unroll
            for (int e = 0; e < 4; ++e) { const float xr = a1[e], xi = -a2[e], wre = bf_lo(fw[e]), wim = bf_lo(fwi[e]);
                yr[e] = (xr * wre - xi * wim) * sc; yi[e] = (xr * wim + xi * wre) * sc; }
            if (k4 == 0) {
                f32x4 n1 = {0.f, 0.f, 0.f, 0.f}, n2 = n1;
#pragma unroll
                for (int t = 0; t < 4; ++t) { n1 = mfma(E1[t], Bx[t], n1); n2 = mfma(E1[t], swap_neg(Bx[t]), n2); }
                float xr2[4], xi2[4];
#pragma unroll
                for (int e = 0; e < 4; ++e) { const float xr = n1[e], xi = -n2[e], wre = bf_lo(fx[e]), wim = bf_lo(fx[4 + e]);
                    xr2[e] = (xr * wre - xi * wim) * (0.5f * SC); xi2[e] = (xr * wim + xi * wre) * (0.5f * SC); }
                *(LAS u32x4*)(L + L_YX + (w2 * 64 + lane) * 16) = pack8(xr2, xi2);
            }
            *(LAS u32x4*)(L + L_XI + ((u2 * 4 + w2) * 64 + lane) * 16) = pack8(yr, yi);
        }
        WAIT_LGKM0(); BAR();
        u32x4 wf[2][4];
        p3_rdw(L, cx, kg, lane, 0, wf[0]);
        {
            f32x4 t1 = {0.f, 0.f, 0.f, 0.f}, t2 = t1;
#pragma unroll
            for (int t = 0; t < 4; ++t) { const u32x4 y = lds128(L, L_XI + ((u2 * 4 + t) * 64 + lane) * 16);
                t1 = mfma(y, E1[t], t1); t2 = mfma(swap_neg(y), E1[t], t2); }
            const int s2 = 16 * w2 + fr, ko = 4 * k4 + g;
            float tr[4], ti[4];
#pragma unroll
            for (int e = 0; e < 4; ++e) { tr[e] = t1[e] * tc[e] - t2[e] * tsn[e]; ti[e] = t2[e] * tc[e] + t1[e] * tsn[e]; }
            st_sc1(rs, (int)WS_T + ((((s2 * 16 + (ko >> 4)) * 2 + (jq >> 2)) * 64 + (jq & 3) * 16 + (ko & 15)) << 4), pack8(tr, ti));
            if (k4 == 0) {
                f32x4 x1 = {0.f, 0.f, 0.f, 0.f}, x2 = x1;
#pragma unroll
                for (int t = 0; t < 4; ++t) { const u32x4 y = lds128(L, L_YX + (t * 64 + lane) * 16);
                    x1 = mfma(y, E1[t], x1); x2 = mfma(swap_neg(y), E1[t], x2); }
#pragma unroll
                for (int e = 0; e < 4; ++e) { tr[e] = x1[e] * tc[e] - x2[e] * tsn[e]; ti[e] = x2[e] * tc[e] + x1[e] * tsn[e]; }
                if (g == 0) st_sc1(rs, (int)WS_TX + ((s2 * 8 + jq) << 4), pack8(tr, ti));
            }
        }
        u32x4 xres; gl128(xres, (const unsigned char*)p.x + ((size_t)panel_row(r3, 64 * h3 + (tid >> 3)) * 512 + 64 * cc + 8 * (tid & 7)) * 2);
        STAMP(9);
        f32x4 acc3[4][2];
#pragma unroll
        for (int dt = 0; dt < 4; ++dt) { acc3[dt][0] = f32x4{0.f, 0.f, 0.f, 0.f}; acc3[dt][1] = acc3[dt][0]; }
        unsigned ftf; u32x4 tx[2];
        static_for<0, 14>([&](auto Sx) {
            constexpr int s = Sx.v;
            wait_vm<p3_wait(s)>();
            if constexpr (s == 12 || s == 13) { pin(hh[s - 12][0]); pin(hh[s - 12][1]); pin(hh[s - 12][2]); pin(hh[s - 12][3]); }
            else { pin(hb[s % HD][0]); pin(hb[s % HD][1]); }
            if constexpr (s == FTC) {
                STAMP(12);
                pin(ftf);
                if (wid == 0) {
                    if (!__all(ftf == (tag << 8) + 16u)) {
#pragma unroll 1
                        for (unsigned spins = 0;; ++spins) {
                            __builtin_amdgcn_s_sleep(PSLEEP);
                            unsigned f2; gl32_sc1(f2, ws, (unsigned)(W_CT + (lane & 15) * 32) * 4u); wait_vm<0>(); pin(f2);
                            if (__all(f2 == (tag << 8) + 16u)) break;
                            if (spins > SPIN_CAP) { give_up(ws, lane); break; }
                        }
                    }
                    if (b == 0 && lane == 0) __hip_atomic_store((gu32*)p.ws, tag, RLX_AGENT);
                }
                STAMP(13);
            }
            if constexpr (s % 3 == 0 && s > 0) {
                WAIT_LGKM0(); BAR();
                if constexpr (s == 3) { STAMP(4); if (tid == 0) __hip_atomic_fetch_add((gu32*)p.ws + W_CT + (kq >> 1) * 32, 1u, RLX_AGENT); }
                p3_rdw(L, cx, kg, lane, s, wf[s & 1]);
            }
            TS(10 + s);
            if constexpr (s + 1 < 14 && (s + 1) % 3 != 0) p3_rdw(L, cx, kg, lane, s + 1, wf[(s + 1) & 1]);
            u32x4 b0, b1;
            if constexpr (s == 12 || s == 13) { b0 = u32x4{hh[s - 12][0].x, hh[s - 12][0].y, hh[s - 12][1].x, hh[s - 12][1].y}; b1 = u32x4{hh[s - 12][2].x, hh[s - 12][2].y, hh[s - 12][3].x, hh[s - 12][3].y}; }
            else { b0 = hb[s % HD][0]; b1 = hb[s % HD][1]; }
#pragma unroll
            for (int dt = 0; dt < 4; ++dt) { acc3[dt][0] = mfma(wf[s & 1][dt], b0, acc3[dt][0]); acc3[dt][1] = mfma(wf[s & 1][dt], b1, acc3[dt][1]); }
            SB();
            static_for<HD, 18>([&](auto Ox) {
                constexpr int o = Ox.v;
                if constexpr (p3_issue_at(o) == s) {
                    if constexpr (o < 12) { gl128_sc1(hb[o % HD][0], ws, cx.hbase + o * 4096u); gl128_sc1(hb[o % HD][1], ws, cx.hbase + 48u * 1024u + o * 4096u); }
                    else if constexpr (o < 14) {
                        gl64_sc1(hh[o - 12][0], ws, cx.h2base + (o - 12) * 4096u); gl64_sc1(hh[o - 12][1], ws, cx.h2base + (o - 12) * 4096u + 512u);
                        gl64_sc1(hh[o - 12][2], ws, cx.h2base + 8192u + (o - 12) * 4096u); gl64_sc1(hh[o - 12][3], ws, cx.h2base + 8192u + (o - 12) * 4096u + 512u); }
                    else { gl128_sc1(hb[o % HD][0], ws, cx.tbase + (o - 14) * 2048u); gl128_sc1(hb[o % HD][1], ws, cx.tbase + (o - 14) * 2048u + 1024u);
                        if constexpr (o == 14) { const unsigned txo = (unsigned)WS_TX + (unsigned)((4 * r3 + 2 * h3 + rh) * 8 + g) * 16u; gl128_sc1(tx[0], ws, txo); gl128_sc1(tx[1], ws, txo + 64u); } }
                }
            });
            if constexpr (s >= 3 && s + 6 < 18) p3_issue2(p, Lb, wid, cc, s + 6, wv, fv);
            if constexpr (s == FTS) gl32_sc1(ftf, ws, wid == 0 ? (unsigned)(W_CT + (lane & 15) * 32) * 4u : cx.hbase - lane * 16u);
            SB();
        });
        wait_vm<0>();
        static_for<14, 18>([&](auto Ox) { pin(hb[Ox.v % HD][0]); pin(hb[Ox.v % HD][1]); });
        pin(tx[0]); pin(tx[1]); pin(xres);
        WAIT_LGKM0(); BAR();
        TS(24);
        {
            u32x4 wz[2][4];
            p3_rdw(L, cx, kg, lane, 14, wz[0]); p3_rdw(L, cx, kg, lane, 15, wz[1]);
            u32x4 zf[4][2];
            static_for<0, 4>([&](auto Zx) {
                constexpr int z = Zx.v;
                const u32x4 T0 = hb[(14 + z) % HD][0], T1 = hb[(14 + z) % HD][1];
                u32x4 T0s = swap_neg(T0), T1s = swap_neg(T1);
                if constexpr (z == 0) {
                    if (kg == 0 && fr == 0) { T0s = tx[0]; T1s = tx[1]; }
                }
                f32x4 zr[2], zi[2];
#pragma unroll
                for (int st = 0; st < 2; ++st) { const f32x4 z0 = {0.f, 0.f, 0.f, 0.f}; zr[st] = mfma(T0, E2[st * 2 + 0], z0); zi[st] = mfma(T0s, E2[st * 2 + 0], z0); }
#pragma unroll
                for (int st = 0; st < 2; ++st) { zr[st] = mfma(T1, E2[st * 2 + 1], zr[st]); zi[st] = mfma(T1s, E2[st * 2 + 1], zi[st]); }
#pragma unroll
                for (int st = 0; st < 2; ++st) zf[z][st] = pack8(zr[st], zi[st]);
            });
#pragma unroll
            for (int z = 0; z < 2; ++z)
#pragma unroll
                for (int dt = 0; dt < 4; ++dt) { acc3[dt][0] = mfma(wz[z][dt], zf[z][0], acc3[dt][0]); acc3[dt][1] = mfma(wz[z][dt], zf[z][1], acc3[dt][1]); }
            p3_rdw(L, cx, kg, lane, 16, wz[0]); p3_rdw(L, cx, kg, lane, 17, wz[1]);
#pragma unroll
            for (int z = 0; z < 2; ++z)
#pragma unroll
                for (int dt = 0; dt < 4; ++dt) { acc3[dt][0] = mfma(wz[z][dt], zf[2 + z][0], acc3[dt][0]); acc3[dt][1] = mfma(wz[z][dt], zf[2 + z][1], acc3[dt][1]); }
        }
        STAMP(10);
        TS(25);
        WAIT_LGKM0(); BAR();
        constexpr int RP = 272;
#pragma unroll
        for (int dt = 0; dt < 4; ++dt)
#pragma unroll
            for (int st = 0; st < 2; ++st)
                *(LAS f32x4*)(L + (kg * 64 + 32 * rh + 16 * st + fr) * RP + (16 * dt + 4 * g) * 4) = acc3[dt][st];
        WAIT_LGKM0(); BAR();
        const int mm = tid >> 3, cg = tid & 7;
        f32x4 v0 = {0.f, 0.f, 0.f, 0.f}, v1 = v0;
#pragma unroll
        for (int q = 0; q < 4; ++q) { v0 += *(LAS f32x4*)(L + (q * 64 + mm) * RP + cg * 32); v1 += *(LAS f32x4*)(L + (q * 64 + mm) * RP + cg * 32 + 16); }
        const size_t o = (size_t)panel_row(r3, 64 * h3 + mm) * 512 + 64 * cc + 8 * cg;
        {
            const u32x4 ov = u32x4{pk_bf16(bf_lo(xres.x) + v0[0], bf_hi(xres.x) + v0[1]), pk_bf16(bf_lo(xres.y) + v0[2], bf_hi(xres.y) + v0[3]),
                                   pk_bf16(bf_lo(xres.z) + v1[0], bf_hi(xres.z) + v1[1]), pk_bf16(bf_lo(xres.w) + v1[2], bf_hi(xres.w) + v1[3])};
            __builtin_nontemporal_store(ov, (GAS u32x4*)(p.out + o));
        }
    }
    STAMP(11);
}

extern "C" void kernel_launch(void* const* d_in, const int* in_sizes, int n_in, void* d_out, int out_size, void* d_ws, size_t ws_size,
                              hipStream_t stream) {
    Params p;
    p.x = (const uint16_t*)d_in[0]; p.wr = (const uint16_t*)d_in[1]; p.wi = (const uint16_t*)d_in[2];
    p.wg = (const uint16_t*)d_in[4]; p.wu = (const uint16_t*)d_in[5]; p.wd = (const uint16_t*)d_in[6];
    p.out = (uint16_t*)d_out; p.ws = (unsigned char*)d_ws;
    (void)hipFuncSetAttribute((const void*)k_fused, hipFuncAttributeMaxDynamicSharedMemorySize, LDS_BYTES);
    k_fused<<<256, NT, LDS_BYTES, stream>>>(p);
}
```

```cpp
#define OUT_NT
#include <hip/hip_runtime.h>
#include <stdint.h>

#define LAS __attribute__((address_space(3)))
#define GAS __attribute__((address_space(1)))
#define DI __device__ __forceinline__
typedef __bf16 bf8 __attribute__((ext_vector_type(8)));
typedef __bf16 bf2 __attribute__((ext_vector_type(2)));
typedef short s16x4 __attribute__((ext_vector_type(4)));
typedef short s16x8 __attribute__((ext_vector_type(8)));
typedef float f32x4 __attribute__((ext_vector_type(4)));
typedef float f32x2 __attribute__((ext_vector_type(2)));
typedef unsigned u32x4 __attribute__((ext_vector_type(4)));
typedef unsigned u32x2 __attribute__((ext_vector_type(2)));
typedef GAS unsigned gu32;
typedef GAS unsigned long long gu64;
#define RLX_AGENT __ATOMIC_RELAXED, __HIP_MEMORY_SCOPE_AGENT

namespace tabs {
constexpr double PI = 3.14159265358979323846264338327950288;
constexpr double cos_q(double x) {
    double x2 = x * x, t = 1.0, s = 1.0;
    for (int i = 1; i <= 14; ++i) { t *= -x2 / ((2 * i - 1) * (2 * i)); s += t; }
    return s;
}
constexpr uint16_t f2bf(float f) {
    uint32_t u = __builtin_bit_cast(uint32_t, f);
    u += 0x7FFFu + ((u >> 16) & 1u);
    return (uint16_t)(u >> 16);
}
struct Base { uint16_t v[512];
    constexpr Base() : v{} {
        for (int m = 0; m <= 128; ++m) {
            double c = (m == 128) ? 0.0 : cos_q(2.0 * PI * m / 512.0);
            uint16_t b = f2bf((float)c);
            uint16_t nb = (m == 128) ? b : (uint16_t)(b ^ 0x8000u);
            v[m] = b; v[(512 - m) & 511] = b;
            v[256 - m] = nb; v[(256 + m) & 511] = nb;
        }
    }
};
constexpr Base base{};
constexpr uint16_t cosb(int m) { return base.v[m & 511]; }
constexpr uint16_t nsinb(int m) { return base.v[(m + 128) & 511]; }
constexpr uint16_t sinb(int m) { return base.v[(m + 384) & 511]; }
struct Chunk { uint16_t v[16384]; };
constexpr Chunk mk_fwd(int c) {
    Chunk r{};
    for (int d = 0; d < 512; ++d)
        for (int n = 0; n < 32; ++n) { const int k = 16 * c + (n & 15);
            r.v[d * 32 + n] = base.v[((n >> 4) ? (k == 0 ? d * 256 : d * k + 128) : d * k) & 511]; }
    return r;
}
constexpr Chunk mk_inv(int blk) {
    Chunk r{};
    const int cc = blk >> 1;
    for (int e = 0; e < 16384; e += 8) {
        const int l = (e >> 3) & 63, dt = (e >> 9) & 3, kg = (e >> 11) & 3, s = ((blk & 1) << 1) + (e >> 13);
        const int kt = 4 * kg + s, d = 64 * cc + 16 * dt + (l & 15), k0 = 16 * kt + 4 * (l >> 4);
        for (int j = 0; j < 4; ++j) { const int k = k0 + j;
            r.v[e + j] = base.v[(k * d) & 511];
            r.v[e + 4 + j] = base.v[(k == 0 ? d * 256 : k * d + 128) & 511]; }
    }
    return r;
}
template <int I> constexpr Chunk fwd_c = mk_fwd(I);
template <int I> constexpr Chunk inv_c = mk_inv(I);
struct Big { Chunk c[16]; };
}
#define C16(N) {{tabs::N<0>, tabs::N<1>, tabs::N<2>, tabs::N<3>, tabs::N<4>, tabs::N<5>, tabs::N<6>, tabs::N<7>, \
                 tabs::N<8>, tabs::N<9>, tabs::N<10>, tabs::N<11>, tabs::N<12>, tabs::N<13>, tabs::N<14>, tabs::N<15>}}
alignas(256) static __device__ const tabs::Big g_fwd = C16(fwd_c);
alignas(256) static __device__ const tabs::Big g_inv = C16(inv_c);

constexpr int S = 2048, D = 512, F = 1792, DF = 257, NT = 512;
constexpr size_t WS_CTRL = 0;
constexpr size_t WS_STAT = 98304;
constexpr size_t WS_AP = 131072;
constexpr size_t WS_AX = WS_AP + (2u << 20);
constexpr size_t WS_TX = WS_AX + 8192;
constexpr size_t WS_T = WS_AX + 65536;
constexpr size_t WS_H = WS_T + (2u << 20);
constexpr size_t WS_H2 = WS_H + (size_t)16 * 8 * 48 * 1024;
constexpr size_t WS_END = WS_H2 + (size_t)16 * 8 * 16 * 512;
#define FSTR 32
#define PSLEEP 8
constexpr int W_CT = 1024, W_TMO = 2048, W_FA = 4096, W_FH = W_FA + 256 * 32;
constexpr unsigned SPIN_CAP = 1u << 16;
constexpr int CODE_PF_BYTES = 37 * 1024;

struct Params {
    const uint16_t* x; const uint16_t* wr; const uint16_t* wi;
    const uint16_t* wg; const uint16_t* wu; const uint16_t* wd;
    uint16_t* out; unsigned char* ws;
};

template <int I> struct IC { static constexpr int v = I; };
template <int B, int E, class Fn> DI void static_for(Fn&& f) { if constexpr (B < E) { f(IC<B>{}); static_for<B + 1, E>(f); } }
DI f32x4 mfma(u32x4 a, u32x4 b, f32x4 c) {
    return __builtin_amdgcn_mfma_f32_16x16x32_bf16(__builtin_bit_cast(bf8, a), __builtin_bit_cast(bf8, b), c, 0, 0, 0);
}
DI unsigned pk_bf16(float lo, float hi) { bf2 p; p[0] = (__bf16)lo; p[1] = (__bf16)hi; return __builtin_bit_cast(unsigned, p); }
DI float bf_lo(unsigned u) { return __builtin_bit_cast(float, u << 16); }
DI float bf_hi(unsigned u) { return __builtin_bit_cast(float, u & 0xffff0000u); }
DI float bfr(float x) { return bf_lo(pk_bf16(x, x)); }
DI u32x4 lds128(LAS unsigned char* L, int off) { return *(LAS u32x4*)(L + off); }
DI u32x2 lds_tr(LAS unsigned char* L, int off) {
    return __builtin_bit_cast(u32x2, __builtin_amdgcn_ds_read_tr16_b64_v4i16((LAS s16x4*)(L + off)));
}
DI u32x4 lds_tr2(LAS unsigned char* L, int off0, int off1) {
    const u32x2 a = lds_tr(L, off0), b = lds_tr(L, off1);
    return u32x4{a.x, a.y, b.x, b.y};
}
DI u32x4 swap_neg(u32x4 v) { return u32x4{v.z, v.w, v.x ^ 0x80008000u, v.y ^ 0x80008000u}; }
DI u32x4 pack8(const float (&a)[4], const float (&b)[4]) { return u32x4{pk_bf16(a[0], a[1]), pk_bf16(a[2], a[3]), pk_bf16(b[0], b[1]), pk_bf16(b[2], b[3])}; }
DI u32x4 pack8(f32x4 a, f32x4 b) { return u32x4{pk_bf16(a[0], a[1]), pk_bf16(a[2], a[3]), pk_bf16(b[0], b[1]), pk_bf16(b[2], b[3])}; }
DI void glds16(const void* gsrc, unsigned lds_addr) {
    asm volatile("s_mov_b32 m0, %1\n\ts_nop 0\n\tglobal_load_lds_dwordx4 %0, off" :: "v"(gsrc), "s"(lds_addr) : "memory");
}
DI void glds16s(const void* sbase, unsigned voff, unsigned lds_addr) {
    asm volatile("s_mov_b32 m0, %2\n\ts_nop 0\n\tglobal_load_lds_dwordx4 %0, %1" :: "v"(voff), "s"(sbase), "s"(lds_addr) : "memory");
}
DI void glds4(const void* gsrc, unsigned lds_addr) {
    asm volatile("s_mov_b32 m0, %1\n\ts_nop 0\n\tglobal_load_lds_dword %0, off" :: "v"(gsrc), "s"(lds_addr) : "memory");
}
DI void gl128(u32x4& d, const void* p) { asm volatile("global_load_dwordx4 %0, %1, off" : "=v"(d) : "v"(p) : "memory"); }
DI void gl128_sc1(u32x4& d, const void* base, unsigned off) { asm volatile("global_load_dwordx4 %0, %1, %2 sc1" : "=v"(d) : "v"(off), "s"(base) : "memory"); }
DI void gl64_sc1(u32x2& d, const void* base, unsigned off) { asm volatile("global_load_dwordx2 %0, %1, %2 sc1" : "=v"(d) : "v"(off), "s"(base) : "memory"); }
DI void gl32_sc1(unsigned& d, const void* base, unsigned off) { asm volatile("global_load_dword %0, %1, %2 sc1" : "=v"(d) : "v"(off), "s"(base) : "memory"); }
DI void gl16(unsigned& d, const void* base, unsigned off) { asm volatile("global_load_ushort %0, %1, %2" : "=v"(d) : "v"(off), "s"(base) : "memory"); }
template <int N> DI void wait_vm() { static_assert(N >= 0 && N < 64, "vmcnt"); asm volatile("s_waitcnt vmcnt(%0)" :: "n"(N) : "memory"); }
template <class T> DI void pin(T& v) { asm volatile("" : "+v"(v)); }
#define WAIT_LGKM0() asm volatile("s_waitcnt lgkmcnt(0)" ::: "memory")
#define BAR() do { asm volatile("" ::: "memory"); __builtin_amdgcn_s_barrier(); asm volatile("" ::: "memory"); } while (0)
#define SB() __builtin_amdgcn_sched_barrier(0)
DI void st_sc1(__amdgpu_buffer_rsrc_t rs, int off, u32x4 v) { __builtin_amdgcn_raw_buffer_store_b128(v, rs, off, 0, 16); }
DI void st_sc1_64(__amdgpu_buffer_rsrc_t rs, int off, u32x2 v) { __builtin_amdgcn_raw_buffer_store_b64(v, rs, off, 0, 16); }
template <int CTRL> DI float dpp_add(float v) {
    return v + __builtin_bit_cast(float, __builtin_amdgcn_update_dpp(0, __builtin_bit_cast(int, v), CTRL, 0xf, 0xf, true));
}
DI float wave_sum(float v) {
    v = dpp_add<0xB1>(v);
    v = dpp_add<0x4E>(v);
    v = dpp_add<0x141>(v);
    v = dpp_add<0x140>(v);
    const int vi = __builtin_bit_cast(int, v);
    return (__builtin_bit_cast(float, __builtin_amdgcn_readlane(vi, 0)) + __builtin_bit_cast(float, __builtin_amdgcn_readlane(vi, 16))) +
           (__builtin_bit_cast(float, __builtin_amdgcn_readlane(vi, 32)) + __builtin_bit_cast(float, __builtin_amdgcn_readlane(vi, 48)));
}
DI void give_up(const unsigned char* ws, int lane) { if (lane == 0) __hip_atomic_store((gu32*)ws + W_TMO, 1u, RLX_AGENT); }
DI void poll16(const unsigned char* ws, int word, unsigned tag, int lane) {
#pragma unroll 1
    for (unsigned spins = 0;; ++spins) {
        unsigned f; gl32_sc1(f, ws, (unsigned)(word + (lane & 15) * FSTR) * 4u); wait_vm<0>(); pin(f);
        if (__all(f == tag)) break;
        if (spins > SPIN_CAP) { give_up(ws, lane); break; }
        __builtin_amdgcn_s_sleep(PSLEEP);
    }
}
DI void fx_load(unsigned (&w)[8], unsigned o0, unsigned o1, unsigned o2, unsigned o3, const void* wr, const void* wi, int k4) {
    asm volatile("s_cmp_lg_u32 %14, 0\n\ts_cbranch_scc1 .Lnofx%=\n\t"
                 "global_load_ushort %0, %8, %12\n\tglobal_load_ushort %1, %9, %12\n\tglobal_load_ushort %2, %10, %12\n\tglobal_load_ushort %3, %11, %12\n\t"
                 "global_load_ushort %4, %8, %13\n\tglobal_load_ushort %5, %9, %13\n\tglobal_load_ushort %6, %10, %13\n\tglobal_load_ushort %7, %11, %13\n"
                 ".Lnofx%=:" : "=&v"(w[0]), "=&v"(w[1]), "=&v"(w[2]), "=&v"(w[3]), "=&v"(w[4]), "=&v"(w[5]), "=&v"(w[6]), "=&v"(w[7])
                 : "v"(o0), "v"(o1), "v"(o2), "v"(o3), "s"(wr), "s"(wi), "s"(k4) : "memory", "scc");
}
DI float cosr(float x) { return __builtin_amdgcn_cosf(x); }
DI float sinr(float x) { return __builtin_amdgcn_sinf(x); }
DI u32x4 frag_cs(int a, int b0, int n) {
    float c[4], sn[4];
#pragma unroll
    for (int j = 0; j < 4; ++j) { const float th = (float)((a * (b0 + j)) & (n - 1)) * (1.0f / n); c[j] = cosr(th); sn[j] = -sinr(th); }
    return pack8(c, sn);
}

#define STAMP(i) do {} while (0)
#define TS(i) do {} while (0)

constexpr int P1_STAGE = 49152, P1_BOFF = 16384;
constexpr int L_MISC = 147456;
constexpr int L_XI = L_MISC;
constexpr int L_ST = L_MISC + 8704;
constexpr int L_YX = L_ST + 1024;
constexpr int L_SCR = L_YX + 4096;
constexpr int L_FLG = L_SCR + 256;
constexpr int L_END = L_FLG + 16;
static_assert(L_END <= 163840, "lds");
constexpr int LDS_BYTES = 163840;
constexpr int L_RAW = 98304;
constexpr int P3_SLOT = 16384;
DI constexpr int p3_slot(int s) { return (s % 9) * P3_SLOT; }

DI int panel_row(int r, int m) { return 64 * (m & 31) + 4 * r + (m >> 5); }
DI int p3_ks(int s, int kg) { const int n = 4 * s + kg; if (s >= 12) return 7 * (n - 48) + 3; const int p = n / 6, o = n - 6 * p; return 7 * p + o + (o >= 3); }

struct LdSrc { unsigned a[4]; const unsigned char* b[8]; long sB64; const unsigned char* t; long tadv; };
DI void p1_issue(LdSrc& s, const unsigned char* x, unsigned Lb, int lw, int j) {
    const unsigned sb = Lb + (j % 3) * P1_STAGE;
#pragma unroll
    for (int i = 0; i < 4; ++i) glds16s(x + j * 128, s.a[i], sb + (4 * lw + i) * 1024);
#pragma unroll
    for (int u = 0; u < 8; ++u) { const int i = (lw & 1) + 2 * (u & 1) + 4 * (lw >> 1) + 8 * ((u >> 1) & 1) + 16 * (u >> 2);
        glds16(s.b[u], sb + P1_BOFF + i * 1024); s.b[u] += s.sB64; }
}
DI void p1_touch(LdSrc& s, unsigned Lb) { glds4(s.t, Lb + L_SCR); s.t += s.tadv; }
DI void p3_issue(const Params& p, unsigned Lb, int lw, int cc, int s, unsigned wv, unsigned fv) {
    const unsigned sb = Lb + p3_slot(s) + lw * 4096;
    if (s < 14) { const unsigned char* base = (const unsigned char*)p.wd + (size_t)p3_ks(s, lw) * 32768;
#pragma unroll
        for (int i = 0; i < 4; ++i) glds16s(base + i * 8192, wv, sb + i * 1024); }
    else { const unsigned char* base = (const unsigned char*)&g_inv + cc * 65536 + (s - 14) * 16384 + lw * 4096;
#pragma unroll
        for (int i = 0; i < 4; ++i) glds16s(base + i * 1024, fv, sb + i * 1024); }
}

DI void p3_issue2(const Params& p, unsigned Lb, int w, int cc, int s, unsigned wv, unsigned fv) {
    const unsigned sb = Lb + p3_slot(s) + w * 2048;
    if (s < 14) { const unsigned char* base = (const unsigned char*)p.wd + (size_t)p3_ks(s, w >> 1) * 32768 + (w & 1) * 16384;
        glds16s(base, wv, sb); glds16s(base + 8192, wv, sb + 1024); }
    else { const unsigned char* base = (const unsigned char*)&g_inv + cc * 65536 + (s - 14) * 16384 + w * 2048;
        glds16s(base, fv, sb); glds16s(base + 1024, fv, sb + 1024); }
}

template <bool DFTW>
DI void p1_compute(LAS unsigned char* L, unsigned Lb, int wc, int lane, f32x4 (&acc)[4][8], f32x4 (&csm)[4], unsigned (&tsv)[32]) {
    asm volatile("" : "+v"(lane));
    const int fr = lane & 15, g = lane >> 4;
    const int qq = fr >> 2, p = lane & 3;
    int aoff0 = fr * 128 + (((0 + g) ^ (fr & 7)) << 4);
    int aoff1 = fr * 128 + (((4 + g) ^ (fr & 7)) << 4);
    const int sw = 2 * qq + 8 * (g & 1);
    int boff[4];
#pragma unroll
    for (int q = 0; q < 4; ++q) boff[q] = P1_BOFF + (8 * g + qq) * 512 + (((8 * wc + 2 * q + (p >> 1)) ^ sw) << 4) + 8 * (p & 1);
    u32x4 xa[2][4], wb[2][4];
    auto rdx = [&](int ks, int mh, int i) { xa[mh][i] = lds128(L, (ks ? aoff1 : aoff0) + (4 * mh + i) * 2048); };
    auto rdw = [&](int ks, int q) { const int o = boff[q] + ks * 16384; wb[ks][q] = lds_tr2(L, o, o + 2048); };
    auto rebase = [&](int d) { aoff0 += d; aoff1 += d; boff[0] += d; boff[1] += d; boff[2] += d; boff[3] += d;
        asm volatile("" : "+v"(aoff0), "+v"(aoff1), "+v"(boff[0]), "+v"(boff[1]), "+v"(boff[2]), "+v"(boff[3])); };
    auto step = [&](int ks, int mh, bool more) {
#pragma unroll
        for (int i = 0; i < 16; ++i) {
            const int q = i >> 2, mt = i & 3;
            if (DFTW && q >= 2) acc[q][4 * mh + mt] = mfma(xa[mh][mt], wb[ks][q], acc[q][4 * mh + mt]);
            else acc[q][4 * mh + mt] = mfma(wb[ks][q], xa[mh][mt], acc[q][4 * mh + mt]);
            if (more) {
                if (mh == 0) { if (i >= 1 && i <= 4) rdx(ks, 1, i - 1); }
                else { if (i < 4) rdx(ks ^ 1, 0, i); else if (i < 8) rdw(ks ^ 1, i - 4); }
            }
            if (mh == 0 && (i & 3) == 3) { const int d = i >> 2; if (!DFTW || d < 2) csm[d] = mfma(wb[ks][d], u32x4{0x3F803F80u, 0x3F803F80u, 0x3F803F80u, 0x3F803F80u}, csm[d]); }
            SB();
        }
    };
    BAR();
    TS(0);
#pragma unroll
    for (int i = 0; i < 4; ++i) { rdx(0, 0, i); rdw(0, i); }
    SB();
    static_for<0, 8>([&](auto J) {
        constexpr int j = J.v;
        step(0, 0, true);
        step(0, 1, true);
        step(1, 0, true);
        if constexpr (j < 7) { WAIT_LGKM0(); BAR(); TS(j + 1); rebase((j + 1) % 3 == 0 ? -2 * P1_STAGE : P1_STAGE);
            SB(); }
        step(1, 1, j < 7);
    });
    TS(8);
}

constexpr int HD = 5;
constexpr int FTS = 10, FTC = 12;
constexpr int p3_issue_at(int o) { return o < 14 ? o - HD : FTC; }
constexpr int p3_opcnt(int o) { return (o >= 12 && o <= 14) ? 4 : 2; }
enum { K_OP, K_TST, K_W, K_FT };
constexpr int p3_after(int kind, int idx, int s_now) {
    int n = -1;
    auto hit = [&](bool is, int cnt) { if (is) n = 0; else if (n >= 0) n += cnt; };
    for (int o = 0; o < HD; ++o) hit(kind == K_OP && idx == o, 2);
    hit(kind == K_TST, 1); hit(false, 1);
    for (int s = 0; s < s_now; ++s) {
        for (int o = HD; o < 18; ++o) if (p3_issue_at(o) == s) hit(kind == K_OP && idx == o, p3_opcnt(o));
        if (s >= 3 && s + 6 < 18) hit(kind == K_W && idx == s + 6, 2);
        if (s == FTS) hit(kind == K_FT, 1);
    }
    return n;
}
constexpr int cmin(int a, int b) { return a < b ? a : b; }
constexpr int p3_wait(int s) {
    int n = p3_after(K_OP, s, s);
    if (s == 3) n = cmin(n, p3_after(K_TST, 0, s));
    if (s % 3 == 0 && s >= 9) n = cmin(n, p3_after(K_W, s + 2, s));
    if (s == FTC) n = cmin(n, p3_after(K_FT, 0, s));
    return n;
}
static_assert(p3_wait(0) == 2 * (HD - 1) + 2 && p3_wait(3) == 1 + 6, "queue model");

struct P3Ctx { unsigned hbase, h2base, tbase; int woff[4]; };
DI void p3_rdw(LAS unsigned char* L, const P3Ctx& c, int kg, int lane, int s, u32x4 (&wf)[4]) {
#pragma unroll
    for (int dt = 0; dt < 4; ++dt) {
        if (s < 14) wf[dt] = lds_tr2(L, p3_slot(s) + c.woff[dt], p3_slot(s) + c.woff[dt] + 2048);
        else wf[dt] = lds128(L, p3_slot(s) + (kg * 4 + dt) * 1024 + lane * 16);
    }
}

typedef unsigned u32x16 __attribute__((ext_vector_type(16)));
__global__ void __launch_bounds__(NT, 2) k_fused(Params p_arg) {
    unsigned long long t_entry; asm volatile("s_memrealtime %0\n\ts_waitcnt lgkmcnt(0)" : "=s"(t_entry) :: "memory");
    Params p;
    {
        u32x16 ka; const void* kp = (const void*)__builtin_amdgcn_kernarg_segment_ptr();
        asm volatile("s_load_dwordx16 %0, %1, 0x0\n\ts_waitcnt lgkmcnt(0)" : "=s"(ka) : "s"(kp) : "memory");
        auto P = [&](int i) { return ((unsigned long long)ka[2 * i + 1] << 32) | ka[2 * i]; };
        p.x = (const uint16_t*)P(0); p.wr = (const uint16_t*)P(1); p.wi = (const uint16_t*)P(2); p.wg = (const uint16_t*)P(3); p.wu = (const uint16_t*)P(4); p.wd = (const uint16_t*)P(5);
        p.out = (uint16_t*)P(6); p.ws = (unsigned char*)P(7);
    }
    extern __shared__ __attribute__((aligned(16))) unsigned char smem[];
    LAS unsigned char* L = (LAS unsigned char*)smem;
    const unsigned Lb = (unsigned)(size_t)L;
    const int wid = __builtin_amdgcn_readfirstlane(threadIdx.x >> 6);
    const int wc = wid & 3;
    const int b = blockIdx.x, xcd = b & 7, bi = b >> 3;
    const int r = (xcd & 1) * 8 + (bi & 7), c = (xcd >> 1) * 4 + (bi >> 3);
    const int jq = b & 7, kq = b >> 3;
    const int cc = (xcd & 1) * 4 + (bi & 3), hp = (xcd >> 1) * 8 + (bi >> 2), r3 = hp >> 1, h3 = hp & 1;
    const unsigned char* ws = p.ws;
    unsigned tsv[32];
#pragma unroll
    for (int i = 0; i < 32; ++i) tsv[i] = 0;
    STAMP(0);
    TS(31);
    const int odd = c & 1;
    const int u2 = wid >> 2, w2 = wid & 3, k4 = 2 * kq + u2;
    LAS f32x2* ST = (LAS f32x2*)(L + L_ST);
    f32x4 acc[4][8], csm[4];
    u32x4 eaC[2], eaS[2]; float tcA[2][4], tsA[2][4];
#pragma unroll
    for (int i = 0; i < 4; ++i) csm[i] = f32x4{0.f, 0.f, 0.f, 0.f};
    unsigned tag;
    {
    const int tid = threadIdx.x, lane = tid & 63;
    if (wid >= 4) {
        const int lw = wid - 4;
        unsigned epoch; gl32_sc1(epoch, ws, 0u);
        LdSrc ls;
        {
            const int idx = lw * 22 + (lane < 22 ? lane : 0);
            if (idx < 32) { ls.t = (const unsigned char*)p.x + (size_t)panel_row(r, 32 * (c & 3) + idx) * 1024; ls.tadv = 128; }
            else { const int i = idx - 32, row = 8 * (r & 7) + i / 7, it = i % 7;
                if (it == 6) { ls.t = (const unsigned char*)&g_fwd + c * 32768 + row * 64; ls.tadv = 64 * 64; }
                else { const int it3 = it % 3; ls.t = (const unsigned char*)(it < 3 ? p.wg : p.wu) + (size_t)row * (F * 2) + c * 224 + (it3 == 2 ? 220 : it3 * 128); ls.tadv = 64L * F * 2; } }
        }
        p1_touch(ls, Lb);
        SB();
#pragma unroll
        for (int i = 0; i < 4; ++i) { const int m = 8 * (4 * lw + i) + (lane >> 3); ls.a[i] = (unsigned)panel_row(r, m) * 1024u + (((lane & 7) ^ (lane >> 3)) << 4); }
        {
            const int b0 = lw & 1, b2 = lw >> 1;
            const int swz = 2 * (2 * b0 + (lane >> 5)) + 8 * b2;
            const int ch = (lane & 31) ^ swz, nt = ch >> 1, half = ch & 1, cw = nt >> 2, q = nt & 3;
            const unsigned char* base; int col, strideB;
            if (cw < 3) { const int ft = odd + 2 * cw + (q & 1); base = (const unsigned char*)(q < 2 ? p.wg : p.wu); col = 112 * c + 16 * ft + 8 * half; strideB = F * 2; }
            else if (q < 2) { const int ft = odd ? 0 : 6; base = (const unsigned char*)(q == 0 ? p.wg : p.wu); col = 112 * c + 16 * ft + 8 * half; strideB = F * 2; }
            else { base = (const unsigned char*)&g_fwd + c * 32768; col = 16 * (q - 2) + 8 * half; strideB = 64; }
            const unsigned char* b00 = base + (size_t)(2 * b0 + 8 * b2 + (lane >> 5)) * strideB + col * 2;
#pragma unroll
            for (int u = 0; u < 8; ++u) ls.b[u] = b00 + (size_t)(4 * (u & 1) + 16 * ((u >> 1) & 1) + 32 * (u >> 2)) * strideB;
            ls.sB64 = (long)strideB * 64;
        }
        p1_issue(ls, (const unsigned char*)p.x, Lb, lw, 0);
        p1_touch(ls, Lb); p1_touch(ls, Lb); p1_touch(ls, Lb);
        p1_issue(ls, (const unsigned char*)p.x, Lb, lw, 1);
        STAMP(14);
        if (wid == 4 && lane < 4) *((volatile LAS unsigned*)(L + L_FLG) + lane) = 0u;
        wait_vm<15>(); pin(epoch); tag = epoch + 1u;
        if (r == 0 && tid == 256) __hip_atomic_store((gu32*)p.ws + W_CT + c * 32, tag << 8, RLX_AGENT);
        WAIT_LGKM0();
        STAMP(15);
        BAR();
        p1_touch(ls, Lb);
        p1_issue(ls, (const unsigned char*)p.x, Lb, lw, 2);
        SB();
        const unsigned char *st0, *st1;
        {
            auto wline = [&](int i) { const int st = i >> 2, ln = (bi >> 2) * 16 + lw * 4 + (i & 3);
                return st < 14 ? (const unsigned char*)p.wd + (size_t)(32 * p3_ks(st, ln >> 5) + (ln & 31)) * 1024 + cc * 128
                               : (const unsigned char*)&g_inv + (size_t)cc * 65536 + (st - 14) * 16384 + ln * 128; };
            st0 = wline(lane);
            const int fi = (bi * 4 + lw) * 20 + (lane >= 8 && lane < 28 ? lane - 8 : 0);
            const int arr = fi / 1280, rr = (fi % 1280) / 5, ll = (fi % 1280) % 5;
            const int jrow = 4 * jq + (rr & 3) + 32 * (rr >> 2);
            st1 = lane < 8 ? wline(64 + lane) : (const unsigned char*)(arr ? p.wi : p.wr) + (size_t)jrow * (DF * 2) + min(ll * 128, 510);
        }
        u32x2 gran; const unsigned goff = (unsigned)WS_STAT + (unsigned)(r * 128 + (tid & 127)) * 8u;
        static_for<0, 7>([&](auto J) {
            constexpr int j = J.v;
            if constexpr (j < 4) wait_vm<13>(); else if constexpr (j == 4) wait_vm<12>();
            else if constexpr (j == 5) wait_vm<15>(); else wait_vm<3>();
            BAR();
            if constexpr (j < 3) p1_touch(ls, Lb);
            if constexpr (j + 3 < 8) p1_issue(ls, (const unsigned char*)p.x, Lb, lw, j + 3);
            if constexpr (j == 4) {
                gl64_sc1(gran, ws, goff);
                glds4(st0, Lb + L_SCR); glds4(st1, Lb + L_SCR);
            }
        });
        wait_vm<2>();
        pin(gran);
        if (lw < 2) {
            if (!__all(gran.y == tag)) {
#pragma unroll 1
                for (unsigned spins = 0;; ++spins) {
                    gl64_sc1(gran, ws, goff); wait_vm<0>(); pin(gran);
                    if (__all(gran.y == tag)) break;
                    if (spins > SPIN_CAP) { give_up(ws, lane); break; }
                    __builtin_amdgcn_s_sleep(1);
                }
            }
            ST[tid & 127] = f32x2{bf_hi(gran.x), 1.0f / bf_lo(gran.x)};
        }
        {
            const int fr = lane & 15, g = lane >> 4;
#pragma unroll
            for (int jt = 0; jt < 2; ++jt) {
                float cv[8], sv[8];
#pragma unroll
                for (int j = 0; j < 8; ++j) { const float th = (float)(((16 * jt + fr) * (8 * g + j)) & 31) * (1.0f / 32.0f); cv[j] = cosr(th); sv[j] = sinr(th); }
                eaC[jt] = u32x4{pk_bf16(cv[0], cv[1]), pk_bf16(cv[2], cv[3]), pk_bf16(cv[4], cv[5]), pk_bf16(cv[6], cv[7])};
                eaS[jt] = u32x4{pk_bf16(sv[0], sv[1]), pk_bf16(sv[2], sv[3]), pk_bf16(sv[4], sv[5]), pk_bf16(sv[6], sv[7])};
#pragma unroll
                for (int e = 0; e < 4; ++e) { const float th = (float)((16 * jt + fr) * (4 * r + e)) * (1.0f / 2048.0f); tcA[jt][e] = cosr(th); tsA[jt][e] = sinr(th); }
            }
        }
#pragma unroll
        for (int q = 0; q < 4; ++q)
#pragma unroll
            for (int mt = 0; mt < 8; ++mt) acc[q][mt] = f32x4{0.f, 0.f, 0.f, 0.f};
    } else {
        if (wid == 0) {
            const unsigned char* pc = (const unsigned char*)__builtin_amdgcn_s_getpc();
            const int ln = bi * 10 + (lane < 10 ? lane : 0);
            glds4(pc - ((size_t)pc & 127) + (size_t)min(ln * 128, CODE_PF_BYTES), Lb + L_SCR);
        }
        if (wid == 1 || wid == 2) {
            const unsigned char* a;
            if (wid == 1) a = lane < 32 ? (const unsigned char*)p.x + (size_t)lane * 65536 : (const unsigned char*)p.wg + (size_t)min(lane - 32, 27) * 65536;
            else a = lane < 32 ? (const unsigned char*)p.wu + (size_t)min(lane, 27) * 65536 : (const unsigned char*)&g_fwd + (size_t)(lane & 7) * 65536;
            glds4(a, Lb + L_SCR);
        }
        unsigned epoch; gl32_sc1(epoch, ws, 0u);
        u32x4 xrow[2];
#pragma unroll
        for (int i = 0; i < 2; ++i) gl128(xrow[i], (const unsigned char*)p.x + (size_t)panel_row(r, 8 * c + 2 * wid + i) * 1024 + lane * 16);
        wait_vm<0>(); pin(epoch); pin(xrow[0]); pin(xrow[1]);
        tag = epoch + 1u;
#pragma unroll
        for (int i = 0; i < 2; ++i) {
            float v[8];
#pragma unroll
            for (int e = 0; e < 4; ++e) { v[2 * e] = bf_lo(xrow[i][e]); v[2 * e + 1] = bf_hi(xrow[i][e]); }
            float s = ((v[0] + v[1]) + (v[2] + v[3])) + ((v[4] + v[5]) + (v[6] + v[7]));
            s = wave_sum(s);
            const float mean = s * (1.0f / 512.0f);
            float q2 = 0.f;
#pragma unroll
            for (int e = 0; e < 8; ++e) { const float d = v[e] - mean; q2 += d * d; }
            q2 = wave_sum(q2);
            const float var = q2 * (1.0f / 512.0f);
            const float m_bf = bfr(mean), v_bf = bfr(var), s_bf = bfr(sqrtf(bfr(v_bf + 1e-5f)));
            const unsigned val = (__builtin_bit_cast(unsigned, m_bf) & 0xffff0000u) | (__builtin_bit_cast(unsigned, s_bf) >> 16);
            if (lane == 0) __hip_atomic_store((gu64*)(p.ws + WS_STAT) + (r * 128 + 8 * c + 2 * wid + i), ((unsigned long long)tag << 32) | val, RLX_AGENT);
        }
#pragma unroll
        for (int q = 0; q < 4; ++q)
#pragma unroll
            for (int mt = 0; mt < 8; ++mt) acc[q][mt] = f32x4{0.f, 0.f, 0.f, 0.f};
        STAMP(1);
        if (wid < 3) p1_compute<false>(L, Lb, wc, lane, acc, csm, tsv); else p1_compute<true>(L, Lb, wc, lane, acc, csm, tsv);
        STAMP(2);
    }
    }
    int tz = threadIdx.x; asm volatile("" : "+v"(tz));
    const int tid = tz, lane = tz & 63, fr = lane & 15, g = lane >> 4;
    const __amdgpu_buffer_rsrc_t rs = __builtin_amdgcn_make_buffer_rsrc((void*)p.ws, 0, (int)WS_END, 0x00020000);
    const unsigned wv = (unsigned)(lane >> 3) * 1024u + cc * 128u + (unsigned)(((lane & 7) ^ (2 * ((lane >> 4) & 3))) << 4), fv = lane * 16u;
    volatile LAS unsigned* flg = (volatile LAS unsigned*)(L + L_FLG);
    WAIT_LGKM0(); BAR();
    STAMP(3);
    const int fj1 = 4 * jq + (fr & 3), fk = 4 * k4 + (fr >> 2);
    unsigned fw[4], fwi[4];
    auto signal = [&](int cnt, int word) {
        if (lane == 0) { const unsigned old = __hip_atomic_fetch_add((LAS unsigned*)(L + L_FLG) + cnt, 1u, __ATOMIC_RELAXED, __HIP_MEMORY_SCOPE_WORKGROUP);
            if (old == 3u) __hip_atomic_store((gu32*)p.ws + word, tag, RLX_AGENT); }
    };
    if (wid < 3) {
        const f32x4 cg0 = csm[0], cg1 = csm[1], cu0 = csm[2], cu1 = csm[3];
#pragma unroll
        for (int mt = 0; mt < 8; ++mt) {
            const f32x2 st = ST[16 * mt + fr];
            const float r2 = st.y * st.y;
            const f32x4 h0 = (acc[0][mt] - st.x * cg0) * (acc[2][mt] - st.x * cu0) * r2;
            const f32x4 h1 = (acc[1][mt] - st.x * cg1) * (acc[3][mt] - st.x * cu1) * r2;
            st_sc1(rs, (int)WS_H + ((((r * 8 + mt) * 48 + 3 * c + wc) * 64 + lane) << 4), pack8(h0, h1));
        }
    } else if (wid == 3) {
#pragma unroll
        for (int q = 0; q < 2; ++q)
#pragma unroll
            for (int mt = 0; mt < 8; ++mt) *(LAS f32x4*)(L + L_RAW + ((q * 8 + mt) * 64 + lane) * 16) = acc[2 + q][mt];
        WAIT_LGKM0();
        flg[0] = 1u;
        const f32x4 cg0 = csm[0], cu0 = csm[1];
#pragma unroll
        for (int mt = 0; mt < 8; ++mt) {
            const f32x2 st = ST[16 * mt + fr];
            const float r2 = st.y * st.y;
            const f32x4 h0 = (acc[0][mt] - st.x * cg0) * (acc[1][mt] - st.x * cu0) * r2;
            st_sc1_64(rs, (int)WS_H2 + ((((r * 8 + mt) * 16 + c) * 64 + lane) << 3), u32x2{pk_bf16(h0[0], h0[1]), pk_bf16(h0[2], h0[3])});
        }
    } else {
        const int mt = wid - 4;
        const int kq_ = fr >> 2, s2l = fr & 3;
        __builtin_amdgcn_s_setprio(3);
#pragma unroll 1
        for (unsigned spins = 0; flg[0] == 0u && spins < (1u << 20); ++spins) __builtin_amdgcn_s_sleep(1);
        asm volatile("" ::: "memory");
        u32x4 are, aim;
        {
            const int m0 = 32 * s2l + 8 * g, tl = 2 * s2l + (g >> 1), ln = 4 * mt + kq_ + 32 * (g & 1);
            const f32x4 r0 = *(LAS f32x4*)(L + L_RAW + (tl * 64 + ln) * 16), r1 = *(LAS f32x4*)(L + L_RAW + (tl * 64 + ln + 16) * 16);
            const f32x4 i0 = *(LAS f32x4*)(L + L_RAW + ((8 + tl) * 64 + ln) * 16), i1 = *(LAS f32x4*)(L + L_RAW + ((8 + tl) * 64 + ln + 16) * 16);
            const f32x4 sa = *(LAS f32x4*)(ST + m0), sb = *(LAS f32x4*)(ST + m0 + 2), sc = *(LAS f32x4*)(ST + m0 + 4), sd = *(LAS f32x4*)(ST + m0 + 6);
            const float csf = (c == 0 && mt == 0 && kq_ == 0) ? 512.0f : 0.0f;
            are = u32x4{pk_bf16((r0[0] - sa[0] * csf) * sa[1], (r0[1] - sa[2] * csf) * sa[3]), pk_bf16((r0[2] - sb[0] * csf) * sb[1], (r0[3] - sb[2] * csf) * sb[3]),
                        pk_bf16((r1[0] - sc[0] * csf) * sc[1], (r1[1] - sc[2] * csf) * sc[3]), pk_bf16((r1[2] - sd[0] * csf) * sd[1], (r1[3] - sd[2] * csf) * sd[3])};
            aim = u32x4{pk_bf16(i0[0] * sa[1], i0[1] * sa[3]), pk_bf16(i0[2] * sb[1], i0[3] * sb[3]), pk_bf16(i1[0] * sc[1], i1[1] * sc[3]), pk_bf16(i1[2] * sd[1], i1[3] * sd[3])};
        }
        const f32x4 z = {0.f, 0.f, 0.f, 0.f};
        const bool special = (c == 0 && mt == 0 && g == 0);
#pragma unroll
        for (int jt = 0; jt < 2; ++jt) {
            const f32x4 Prc = mfma(are, eaC[jt], z), Pis = mfma(aim, eaS[jt], z), Prs = mfma(are, eaS[jt], z), Pic = mfma(aim, eaC[jt], z);
            const int j1 = 16 * jt + fr;
            float pr[4], qi[4], pr2[4], qi2[4];
#pragma unroll
            for (int e = 0; e < 4; ++e) {
                const float cw = tcA[jt][e], sw = tsA[jt][e];
                const float ar = special ? Prc[e] : Prc[e] + Pis[e], ai = special ? -Prs[e] : Pic[e] - Prs[e];
                pr[e] = ar * cw + ai * sw; qi[e] = -(ai * cw - ar * sw);
                const float br = Pic[e], bi2 = -Pis[e];
                pr2[e] = br * cw + bi2 * sw; qi2[e] = -(bi2 * cw - br * sw);
            }
            const int U = (j1 >> 2) * 64 + 4 * c + mt, lp = 16 * (r & 3) + 4 * g + (j1 & 3);
            st_sc1(rs, (int)WS_AP + (((U * 4 + (r >> 2)) * 64 + lp) << 4), pack8(pr, qi));
            if (special) st_sc1(rs, (int)WS_AX + (((((j1 >> 2) * 4 + (r >> 2)) * 4 + (r & 3)) * 4 + (j1 & 3)) << 4), pack8(pr2, qi2));
        }
        __builtin_amdgcn_s_setprio(0);
        wait_vm<0>();
        signal(1, W_FA + (c * 16 + r) * FSTR);
#pragma unroll
        for (int st = 0; st < 3; ++st) p3_issue(p, Lb, mt, cc, st, wv, fv);
    }
#pragma unroll
    for (int e = 0; e < 4; ++e) { const unsigned o = (unsigned)((fj1 + 32 * (16 * w2 + 4 * g + e)) * DF + fk) * 2u; gl16(fw[e], p.wr, o); gl16(fwi[e], p.wi, o); }
    unsigned fx[8];
    {
        const unsigned o0 = (unsigned)((fj1 + 32 * (16 * w2 + 4 * g)) * DF + 256) * 2u;
        fx_load(fx, o0, o0 + 32u * DF * 2u, o0 + 64u * DF * 2u, o0 + 96u * DF * 2u, p.wr, p.wi, k4);
    }
    if (wid < 4) {
        if (k4 == 0) wait_vm<16>(); else wait_vm<8>();
        signal(2, W_FH + (r * 16 + c) * FSTR);
#pragma unroll
        for (int st = 3; st < 6; ++st) p3_issue(p, Lb, wid, cc, st, wv, fv);
#pragma unroll 1
        for (unsigned spins = 0; flg[1] < 4u && spins < (1u << 22); ++spins) __builtin_amdgcn_s_sleep(1);
        asm volatile("" ::: "memory");
#pragma unroll
        for (int st = 6; st < 9; ++st) p3_issue(p, Lb, wid, cc, st, wv, fv);
    }
    STAMP(5);
    {
        const int kg = wid & 3, rh = wid >> 2;
        u32x4 E1[4], E2[4];
#pragma unroll
        for (int t = 0; t < 4; ++t) E1[t] = frag_cs(16 * w2 + fr, 16 * t + 4 * g, 64);
#pragma unroll
        for (int i = 0; i < 4; ++i) E2[i] = frag_cs(16 * (i >> 1) + fr, 16 * (i & 1) + 4 * g, 32);
        float tc[4], tsn[4];
#pragma unroll
        for (int e = 0; e < 4; ++e) { const float th = (float)((4 * jq + e) * (16 * w2 + fr)) * (1.0f / 2048.0f); tc[e] = cosr(th); tsn[e] = sinr(th); }
        P3Ctx cx;
        {
            const int qq = fr >> 2, pp = lane & 3, row = 32 * kg + 4 * g + qq, sw = 2 * ((row >> 1) & 3);
#pragma unroll
            for (int dt = 0; dt < 4; ++dt) cx.woff[dt] = row * 128 + (((2 * dt + (pp >> 1)) ^ sw) << 4) + 8 * (pp & 1);
            cx.hbase = (unsigned)WS_H + (unsigned)(((r3 * 8 + 4 * h3 + 2 * rh) * 48 + kg) * 64 + lane) * 16u;
            cx.h2base = (unsigned)WS_H2 + (unsigned)(((r3 * 8 + 4 * h3 + 2 * rh) * 16 + 2 * kg) * 64 + lane) * 8u;
            cx.tbase = (unsigned)WS_T + (unsigned)((((4 * r3 + 2 * h3 + rh) * 16 + 4 * kg) * 2) * 64 + lane) * 16u;
        }
        SB();
        STAMP(6);
        if (wid == 0) poll16(ws, W_FA + (kq >> 1) * 16 * FSTR, tag, lane);
        if (wid == 1) poll16(ws, W_FH + r3 * 16 * FSTR, tag, lane);
        BAR();
        STAMP(7);
        const int U = jq * 64 + k4;
        u32x4 Bf[4], Bx[4];
        u32x4 hb[HD][2]; u32x2 hh[2][4];
#pragma unroll
        for (int t = 0; t < 4; ++t) gl128_sc1(Bf[t], ws, (unsigned)WS_AP + (unsigned)((U * 4 + t) * 64 + lane) * 16u);
        {
            const unsigned xo = (unsigned)WS_AX + (unsigned)((jq * 16 + g) * 4 + (fr & 3)) * 16u;
            asm volatile("s_cmp_lg_u32 %6, 0\n\ts_cbranch_scc1 .Lnox%=\n\t"
                         "global_load_dwordx4 %0, %4, %5 sc1\n\tglobal_load_dwordx4 %1, %4, %5 offset:256 sc1\n\t"
                         "global_load_dwordx4 %2, %4, %5 offset:512 sc1\n\tglobal_load_dwordx4 %3, %4, %5 offset:768 sc1\n"
                         ".Lnox%=:" : "=&v"(Bx[0]), "=&v"(Bx[1]), "=&v"(Bx[2]), "=&v"(Bx[3]) : "v"(xo), "s"(ws), "s"(k4) : "memory", "scc");
        }
        static_for<0, HD>([&](auto Sx) { constexpr int o = Sx.v;
            gl128_sc1(hb[o][0], ws, cx.hbase + o * 4096u); gl128_sc1(hb[o][1], ws, cx.hbase + 48u * 1024u + o * 4096u); });
        wait_vm<2 * HD>();
        pin(Bf[0]); pin(Bf[1]); pin(Bf[2]); pin(Bf[3]); pin(Bx[0]); pin(Bx[1]); pin(Bx[2]); pin(Bx[3]);
        pin(fw[0]); pin(fw[1]); pin(fw[2]); pin(fw[3]); pin(fwi[0]); pin(fwi[1]); pin(fwi[2]); pin(fwi[3]);
        pin(fx[0]); pin(fx[1]); pin(fx[2]); pin(fx[3]); pin(fx[4]); pin(fx[5]); pin(fx[6]); pin(fx[7]);
        STAMP(8);
        {
            f32x4 a1 = {0.f, 0.f, 0.f, 0.f}, a2 = a1;
#pragma unroll
            for (int t = 0; t < 4; ++t) { a1 = mfma(E1[t], Bf[t], a1); a2 = mfma(E1[t], swap_neg(Bf[t]), a2); }
            constexpr float SC = 2.0f / (2048.0f * 512.0f);
            const float sc = (k4 == 0 && (fr >> 2) == 0) ? 0.5f * SC : SC;
            float yr[4], yi[4];
#pragma unroll
            for (int e = 0; e < 4; ++e) { const float xr = a1[e], xi = -a2[e], wre = bf_lo(fw[e]), wim = bf_lo(fwi[e]);
                yr[e] = (xr * wre - xi * wim) * sc; yi[e] = (xr * wim + xi * wre) * sc; }
            if (k4 == 0) {
                f32x4 n1 = {0.f, 0.f, 0.f, 0.f}, n2 = n1;
#pragma unroll
                for (int t = 0; t < 4; ++t) { n1 = mfma(E1[t], Bx[t], n1); n2 = mfma(E1[t], swap_neg(Bx[t]), n2); }
                float xr2[4], xi2[4];
#pragma unroll
                for (int e = 0; e < 4; ++e) { const float xr = n1[e], xi = -n2[e], wre = bf_lo(fx[e]), wim = bf_lo(fx[4 + e]);
                    xr2[e] = (xr * wre - xi * wim) * (0.5f * SC); xi2[e] = (xr * wim + xi * wre) * (0.5f * SC); }
                *(LAS u32x4*)(L + L_YX + (w2 * 64 + lane) * 16) = pack8(xr2, xi2);
            }
            *(LAS u32x4*)(L + L_XI + ((u2 * 4 + w2) * 64 + lane) * 16) = pack8(yr, yi);
        }
        WAIT_LGKM0(); BAR();
        u32x4 wf[2][4];
        p3_rdw(L, cx, kg, lane, 0, wf[0]);
        {
            f32x4 t1 = {0.f, 0.f, 0.f, 0.f}, t2 = t1;
#pragma unroll
            for (int t = 0; t < 4; ++t) { const u32x4 y = lds128(L, L_XI + ((u2 * 4 + t) * 64 + lane) * 16);
                t1 = mfma(y, E1[t], t1); t2 = mfma(swap_neg(y), E1[t], t2); }
            const int s2 = 16 * w2 + fr, ko = 4 * k4 + g;
            float tr[4], ti[4];
#pragma unroll
            for (int e = 0; e < 4; ++e) { tr[e] = t1[e] * tc[e] - t2[e] * tsn[e]; ti[e] = t2[e] * tc[e] + t1[e] * tsn[e]; }
            st_sc1(rs, (int)WS_T + ((((s2 * 16 + (ko >> 4)) * 2 + (jq >> 2)) * 64 + (jq & 3) * 16 + (ko & 15)) << 4), pack8(tr, ti));
            if (k4 == 0) {
                f32x4 x1 = {0.f, 0.f, 0.f, 0.f}, x2 = x1;
#pragma unroll
                for (int t = 0; t < 4; ++t) { const u32x4 y = lds128(L, L_YX + (t * 64 + lane) * 16);
                    x1 = mfma(y, E1[t], x1); x2 = mfma(swap_neg(y), E1[t], x2); }
#pragma unroll
                for (int e = 0; e < 4; ++e) { tr[e] = x1[e] * tc[e] - x2[e] * tsn[e]; ti[e] = x2[e] * tc[e] + x1[e] * tsn[e]; }
                if (g == 0) st_sc1(rs, (int)WS_TX + ((s2 * 8 + jq) << 4), pack8(tr, ti));
            }
        }
        u32x4 xres; gl128(xres, (const unsigned char*)p.x + ((size_t)panel_row(r3, 64 * h3 + (tid >> 3)) * 512 + 64 * cc + 8 * (tid & 7)) * 2);
        STAMP(9);
        f32x4 acc3[4][2];
#pragma unroll
        for (int dt = 0; dt < 4; ++dt) { acc3[dt][0] = f32x4{0.f, 0.f, 0.f, 0.f}; acc3[dt][1] = acc3[dt][0]; }
        unsigned ftf; u32x4 tx[2];
        static_for<0, 14>([&](auto Sx) {
            constexpr int s = Sx.v;
            wait_vm<p3_wait(s)>();
            if constexpr (s == 12 || s == 13) { pin(hh[s - 12][0]); pin(hh[s - 12][1]); pin(hh[s - 12][2]); pin(hh[s - 12][3]); }
            else { pin(hb[s % HD][0]); pin(hb[s % HD][1]); }
            if constexpr (s == FTC) {
                STAMP(12);
                pin(ftf);
                if (wid == 0) {
                    if (!__all(ftf == (tag << 8) + 16u)) {
#pragma unroll 1
                        for (unsigned spins = 0;; ++spins) {
                            __builtin_amdgcn_s_sleep(PSLEEP);
                            unsigned f2; gl32_sc1(f2, ws, (unsigned)(W_CT + (lane & 15) * 32) * 4u); wait_vm<0>(); pin(f2);
                            if (__all(f2 == (tag << 8) + 16u)) break;
                            if (spins > SPIN_CAP) { give_up(ws, lane); break; }
                        }
                    }
                    if (b == 0 && lane == 0) __hip_atomic_store((gu32*)p.ws, tag, RLX_AGENT);
                }
                STAMP(13);
            }
            if constexpr (s % 3 == 0 && s > 0) {
                WAIT_LGKM0(); BAR();
                if constexpr (s == 3) { STAMP(4); if (tid == 0) __hip_atomic_fetch_add((gu32*)p.ws + W_CT + (kq >> 1) * 32, 1u, RLX_AGENT); }
                p3_rdw(L, cx, kg, lane, s, wf[s & 1]);
            }
            TS(10 + s);
            if constexpr (s + 1 < 14 && (s + 1) % 3 != 0) p3_rdw(L, cx, kg, lane, s + 1, wf[(s + 1) & 1]);
            u32x4 b0, b1;
            if constexpr (s == 12 || s == 13) { b0 = u32x4{hh[s - 12][0].x, hh[s - 12][0].y, hh[s - 12][1].x, hh[s - 12][1].y}; b1 = u32x4{hh[s - 12][2].x, hh[s - 12][2].y, hh[s - 12][3].x, hh[s - 12][3].y}; }
            else { b0 = hb[s % HD][0]; b1 = hb[s % HD][1]; }
#pragma unroll
            for (int dt = 0; dt < 4; ++dt) { acc3[dt][0] = mfma(wf[s & 1][dt], b0, acc3[dt][0]); acc3[dt][1] = mfma(wf[s & 1][dt], b1, acc3[dt][1]); }
            SB();
            static_for<HD, 18>([&](auto Ox) {
                constexpr int o = Ox.v;
                if constexpr (p3_issue_at(o) == s) {
                    if constexpr (o < 12) { gl128_sc1(hb[o % HD][0], ws, cx.hbase + o * 4096u); gl128_sc1(hb[o % HD][1], ws, cx.hbase + 48u * 1024u + o * 4096u); }
                    else if constexpr (o < 14) {
                        gl64_sc1(hh[o - 12][0], ws, cx.h2base + (o - 12) * 4096u); gl64_sc1(hh[o - 12][1], ws, cx.h2base + (o - 12) * 4096u + 512u);
                        gl64_sc1(hh[o - 12][2], ws, cx.h2base + 8192u + (o - 12) * 4096u); gl64_sc1(hh[o - 12][3], ws, cx.h2base + 8192u + (o - 12) * 4096u + 512u); }
                    else { gl128_sc1(hb[o % HD][0], ws, cx.tbase + (o - 14) * 2048u); gl128_sc1(hb[o % HD][1], ws, cx.tbase + (o - 14) * 2048u + 1024u);
                        if constexpr (o == 14) { const unsigned txo = (unsigned)WS_TX + (unsigned)((4 * r3 + 2 * h3 + rh) * 8 + g) * 16u; gl128_sc1(tx[0], ws, txo); gl128_sc1(tx[1], ws, txo + 64u); } }
                }
            });
            if constexpr (s >= 3 && s + 6 < 18) p3_issue2(p, Lb, wid, cc, s + 6, wv, fv);
            if constexpr (s == FTS) gl32_sc1(ftf, ws, wid == 0 ? (unsigned)(W_CT + (lane & 15) * 32) * 4u : cx.hbase - lane * 16u);
            SB();
        });
        wait_vm<0>();
        static_for<14, 18>([&](auto Ox) { pin(hb[Ox.v % HD][0]); pin(hb[Ox.v % HD][1]); });
        pin(tx[0]); pin(tx[1]); pin(xres);
        WAIT_LGKM0(); BAR();
        TS(24);
        {
            u32x4 wz[2][4];
            p3_rdw(L, cx, kg, lane, 14, wz[0]); p3_rdw(L, cx, kg, lane, 15, wz[1]);
            u32x4 zf[4][2];
            static_for<0, 4>([&](auto Zx) {
                constexpr int z = Zx.v;
                const u32x4 T0 = hb[(14 + z) % HD][0], T1 = hb[(14 + z) % HD][1];
                u32x4 T0s = swap_neg(T0), T1s = swap_neg(T1);
                if constexpr (z == 0) {
                    if (kg == 0 && fr == 0) { T0s = tx[0]; T1s = tx[1]; }
                }
                f32x4 zr[2], zi[2];
#pragma unroll
                for (int st = 0; st < 2; ++st) { const f32x4 z0 = {0.f, 0.f, 0.f, 0.f}; zr[st] = mfma(T0, E2[st * 2 + 0], z0); zi[st] = mfma(T0s, E2[st * 2 + 0], z0); }
#pragma unroll
                for (int st = 0; st < 2; ++st) { zr[st] = mfma(T1, E2[st * 2 + 1], zr[st]); zi[st] = mfma(T1s, E2[st * 2 + 1], zi[st]); }
#pragma unroll
                for (int st = 0; st < 2; ++st) zf[z][st] = pack8(zr[st], zi[st]);
            });
#pragma unroll
            for (int z = 0; z < 2; ++z)
#pragma unroll
                for (int dt = 0; dt < 4; ++dt) { acc3[dt][0] = mfma(wz[z][dt], zf[z][0], acc3[dt][0]); acc3[dt][1] = mfma(wz[z][dt], zf[z][1], acc3[dt][1]); }
            p3_rdw(L, cx, kg, lane, 16, wz[0]); p3_rdw(L, cx, kg, lane, 17, wz[1]);
#pragma unroll
            for (int z = 0; z < 2; ++z)
#pragma unroll
                for (int dt = 0; dt < 4; ++dt) { acc3[dt][0] = mfma(wz[z][dt], zf[2 + z][0], acc3[dt][0]); acc3[dt][1] = mfma(wz[z][dt], zf[2 + z][1], acc3[dt][1]); }
        }
        STAMP(10);
        TS(25);
        WAIT_LGKM0(); BAR();
        constexpr int RP = 272;
#pragma unroll
        for (int dt = 0; dt < 4; ++dt)
#pragma unroll
            for (int st = 0; st < 2; ++st)
                *(LAS f32x4*)(L + (kg * 64 + 32 * rh + 16 * st + fr) * RP + (16 * dt + 4 * g) * 4) = acc3[dt][st];
        WAIT_LGKM0(); BAR();
        const int mm = tid >> 3, cg = tid & 7;
        f32x4 v0 = {0.f, 0.f, 0.f, 0.f}, v1 = v0;
#pragma unroll
        for (int q = 0; q < 4; ++q) { v0 += *(LAS f32x4*)(L + (q * 64 + mm) * RP + cg * 32); v1 += *(LAS f32x4*)(L + (q * 64 + mm) * RP + cg * 32 + 16); }
        const size_t o = (size_t)panel_row(r3, 64 * h3 + mm) * 512 + 64 * cc + 8 * cg;
        {
        const u32x4 ov = u32x4{pk_bf16(bf_lo(xres.x) + v0[0], bf_hi(xres.x) + v0[1]), pk_bf16(bf_lo(xres.y) + v0[2], bf_hi(xres.y) + v0[3]),
                                   pk_bf16(bf_lo(xres.z) + v1[0], bf_hi(xres.z) + v1[1]), pk_bf16(bf_lo(xres.w) + v1[2], bf_hi(xres.w) + v1[3])};
            __builtin_nontemporal_store(ov, (GAS u32x4*)(p.out + o));
        }
    }
    STAMP(11);
}

extern "C" void kernel_launch(void* const* d_in, const int* in_sizes, int n_in, void* d_out, int out_size, void* d_ws, size_t ws_size,
                              hipStream_t stream) {
    Params p;
    p.x = (const uint16_t*)d_in[0]; p.wr = (const uint16_t*)d_in[1]; p.wi = (const uint16_t*)d_in[2];
    p.wg = (const uint16_t*)d_in[4]; p.wu = (const uint16_t*)d_in[5]; p.wd = (const uint16_t*)d_in[6];
    p.out = (uint16_t*)d_out; p.ws = (unsigned char*)d_ws;
    (void)hipFuncSetAttribute((const void*)k_fused, hipFuncAttributeMaxDynamicSharedMemorySize, LDS_BYTES);
    k_fused<<<256, NT, LDS_BYTES, stream>>>(p);
}
```

```cpp
#define OUT_SC1
#include <hip/hip_runtime.h>
#include <stdint.h>

#define LAS __attribute__((address_space(3)))
#define GAS __attribute__((address_space(1)))
#define DI __device__ __forceinline__
typedef __bf16 bf8 __attribute__((ext_vector_type(8)));
typedef __bf16 bf2 __attribute__((ext_vector_type(2)));
typedef short s16x4 __attribute__((ext_vector_type(4)));
typedef short s16x8 __attribute__((ext_vector_type(8)));
typedef float f32x4 __attribute__((ext_vector_type(4)));
typedef float f32x2 __attribute__((ext_vector_type(2)));
typedef unsigned u32x4 __attribute__((ext_vector_type(4)));
typedef unsigned u32x2 __attribute__((ext_vector_type(2)));
typedef GAS unsigned gu32;
typedef GAS unsigned long long gu64;
#define RLX_AGENT __ATOMIC_RELAXED, __HIP_MEMORY_SCOPE_AGENT

namespace tabs {
constexpr double PI = 3.14159265358979323846264338327950288;
constexpr double cos_q(double x) {
    double x2 = x * x, t = 1.0, s = 1.0;
    for (int i = 1; i <= 14; ++i) { t *= -x2 / ((2 * i - 1) * (2 * i)); s += t; }
    return s;
}
constexpr uint16_t f2bf(float f) {
    uint32_t u = __builtin_bit_cast(uint32_t, f);
    u += 0x7FFFu + ((u >> 16) & 1u);
    return (uint16_t)(u >> 16);
}
struct Base { uint16_t v[512];
    constexpr Base() : v{} {
        for (int m = 0; m <= 128; ++m) {
            double c = (m == 128) ? 0.0 : cos_q(2.0 * PI * m / 512.0);
            uint16_t b = f2bf((float)c);
            uint16_t nb = (m == 128) ? b : (uint16_t)(b ^ 0x8000u);
            v[m] = b; v[(512 - m) & 511] = b;
            v[256 - m] = nb; v[(256 + m) & 511] = nb;
        }
    }
};
constexpr Base base{};
constexpr uint16_t cosb(int m) { return base.v[m & 511]; }
constexpr uint16_t nsinb(int m) { return base.v[(m + 128) & 511]; }
constexpr uint16_t sinb(int m) { return base.v[(m + 384) & 511]; }
struct Chunk { uint16_t v[16384]; };
constexpr Chunk mk_fwd(int c) {
    Chunk r{};
    for (int d = 0; d < 512; ++d)
        for (int n = 0; n < 32; ++n) { const int k = 16 * c + (n & 15);
            r.v[d * 32 + n] = base.v[((n >> 4) ? (k == 0 ? d * 256 : d * k + 128) : d * k) & 511]; }
    return r;
}
constexpr Chunk mk_inv(int blk) {
    Chunk r{};
    const int cc = blk >> 1;
    for (int e = 0; e < 16384; e += 8) {
        const int l = (e >> 3) & 63, dt = (e >> 9) & 3, kg = (e >> 11) & 3, s = ((blk & 1) << 1) + (e >> 13);
        const int kt = 4 * kg + s, d = 64 * cc + 16 * dt + (l & 15), k0 = 16 * kt + 4 * (l >> 4);
        for (int j = 0; j < 4; ++j) { const int k = k0 + j;
            r.v[e + j] = base.v[(k * d) & 511];
            r.v[e + 4 + j] = base.v[(k == 0 ? d * 256 : k * d + 128) & 511]; }
    }
    return r;
}
template <int I> constexpr Chunk fwd_c = mk_fwd(I);
template <int I> constexpr Chunk inv_c = mk_inv(I);
struct Big { Chunk c[16]; };
}
#define C16(N) {{tabs::N<0>, tabs::N<1>, tabs::N<2>, tabs::N<3>, tabs::N<4>, tabs::N<5>, tabs::N<6>, tabs::N<7>, \
                 tabs::N<8>, tabs::N<9>, tabs::N<10>, tabs::N<11>, tabs::N<12>, tabs::N<13>, tabs::N<14>, tabs::N<15>}}
alignas(256) static __device__ const tabs::Big g_fwd = C16(fwd_c);
alignas(256) static __device__ const tabs::Big g_inv = C16(inv_c);

constexpr int S = 2048, D = 512, F = 1792, DF = 257, NT = 512;
constexpr size_t WS_CTRL = 0;
constexpr size_t WS_STAT = 98304;
constexpr size_t WS_AP = 131072;
constexpr size_t WS_AX = WS_AP + (2u << 20);
constexpr size_t WS_TX = WS_AX + 8192;
constexpr size_t WS_T = WS_AX + 65536;
constexpr size_t WS_H = WS_T + (2u << 20);
constexpr size_t WS_H2 = WS_H + (size_t)16 * 8 * 48 * 1024;
constexpr size_t WS_END = WS_H2 + (size_t)16 * 8 * 16 * 512;
#define FSTR 32
#define PSLEEP 8
constexpr int W_CT = 1024, W_TMO = 2048, W_FA = 4096, W_FH = W_FA + 256 * 32;
constexpr unsigned SPIN_CAP = 1u << 16;
constexpr int CODE_PF_BYTES = 37 * 1024;

struct Params {
    const uint16_t* x; const uint16_t* wr; const uint16_t* wi;
    const uint16_t* wg; const uint16_t* wu; const uint16_t* wd;
    uint16_t* out; unsigned char* ws;
};

template <int I> struct IC { static constexpr int v = I; };
template <int B, int E, class Fn> DI void static_for(Fn&& f) { if constexpr (B < E) { f(IC<B>{}); static_for<B + 1, E>(f); } }
DI f32x4 mfma(u32x4 a, u32x4 b, f32x4 c) {
    return __builtin_amdgcn_mfma_f32_16x16x32_bf16(__builtin_bit_cast(bf8, a), __builtin_bit_cast(bf8, b), c, 0, 0, 0);
}
DI unsigned pk_bf16(float lo, float hi) { bf2 p; p[0] = (__bf16)lo; p[1] = (__bf16)hi; return __builtin_bit_cast(unsigned, p); }
DI float bf_lo(unsigned u) { return __builtin_bit_cast(float, u << 16); }
DI float bf_hi(unsigned u) { return __builtin_bit_cast(float, u & 0xffff0000u); }
DI float bfr(float x) { return bf_lo(pk_bf16(x, x)); }
DI u32x4 lds128(LAS unsigned char* L, int off) { return *(LAS u32x4*)(L + off); }
DI u32x2 lds_tr(LAS unsigned char* L, int off) {
    return __builtin_bit_cast(u32x2, __builtin_amdgcn_ds_read_tr16_b64_v4i16((LAS s16x4*)(L + off)));
}
DI u32x4 lds_tr2(LAS unsigned char* L, int off0, int off1) {
    const u32x2 a = lds_tr(L, off0), b = lds_tr(L, off1);
    return u32x4{a.x, a.y, b.x, b.y};
}
DI u32x4 swap_neg(u32x4 v) { return u32x4{v.z, v.w, v.x ^ 0x80008000u, v.y ^ 0x80008000u}; }
DI u32x4 pack8(const float (&a)[4], const float (&b)[4]) { return u32x4{pk_bf16(a[0], a[1]), pk_bf16(a[2], a[3]), pk_bf16(b[0], b[1]), pk_bf16(b[2], b[3])}; }
DI u32x4 pack8(f32x4 a, f32x4 b) { return u32x4{pk_bf16(a[0], a[1]), pk_bf16(a[2], a[3]), pk_bf16(b[0], b[1]), pk_bf16(b[2], b[3])}; }
DI void glds16(const void* gsrc, unsigned lds_addr) {
    asm volatile("s_mov_b32 m0, %1\n\ts_nop 0\n\tglobal_load_lds_dwordx4 %0, off" :: "v"(gsrc), "s"(lds_addr) : "memory");
}
DI void glds16s(const void* sbase, unsigned voff, unsigned lds_addr) {
    asm volatile("s_mov_b32 m0, %2\n\ts_nop 0\n\tglobal_load_lds_dwordx4 %0, %1" :: "v"(voff), "s"(sbase), "s"(lds_addr) : "memory");
}
DI void glds4(const void* gsrc, unsigned lds_addr) {
    asm volatile("s_mov_b32 m0, %1\n\ts_nop 0\n\tglobal_load_lds_dword %0, off" :: "v"(gsrc), "s"(lds_addr) : "memory");
}
DI void gl128(u32x4& d, const void* p) { asm volatile("global_load_dwordx4 %0, %1, off" : "=v"(d) : "v"(p) : "memory"); }
DI void gl128_sc1(u32x4& d, const void* base, unsigned off) { asm volatile("global_load_dwordx4 %0, %1, %2 sc1" : "=v"(d) : "v"(off), "s"(base) : "memory"); }
DI void gl64_sc1(u32x2& d, const void* base, unsigned off) { asm volatile("global_load_dwordx2 %0, %1, %2 sc1" : "=v"(d) : "v"(off), "s"(base) : "memory"); }
DI void gl32_sc1(unsigned& d, const void* base, unsigned off) { asm volatile("global_load_dword %0, %1, %2 sc1" : "=v"(d) : "v"(off), "s"(base) : "memory"); }
DI void gl16(unsigned& d, const void* base, unsigned off) { asm volatile("global_load_ushort %0, %1, %2" : "=v"(d) : "v"(off), "s"(base) : "memory"); }
template <int N> DI void wait_vm() { static_assert(N >= 0 && N < 64, "vmcnt"); asm volatile("s_waitcnt vmcnt(%0)" :: "n"(N) : "memory"); }
template <class T> DI void pin(T& v) { asm volatile("" : "+v"(v)); }
#define WAIT_LGKM0() asm volatile("s_waitcnt lgkmcnt(0)" ::: "memory")
#define BAR() do { asm volatile("" ::: "memory"); __builtin_amdgcn_s_barrier(); asm volatile("" ::: "memory"); } while (0)
#define SB() __builtin_amdgcn_sched_barrier(0)
DI void st_sc1(__amdgpu_buffer_rsrc_t rs, int off, u32x4 v) { __builtin_amdgcn_raw_buffer_store_b128(v, rs, off, 0, 16); }
DI void st_sc1_64(__amdgpu_buffer_rsrc_t rs, int off, u32x2 v) { __builtin_amdgcn_raw_buffer_store_b64(v, rs, off, 0, 16); }
template <int CTRL> DI float dpp_add(float v) {
    return v + __builtin_bit_cast(float, __builtin_amdgcn_update_dpp(0, __builtin_bit_cast(int, v), CTRL, 0xf, 0xf, true));
}
DI float wave_sum(float v) {
    v = dpp_add<0xB1>(v);
    v = dpp_add<0x4E>(v);
    v = dpp_add<0x141>(v);
    v = dpp_add<0x140>(v);
    const int vi = __builtin_bit_cast(int, v);
    return (__builtin_bit_cast(float, __builtin_amdgcn_readlane(vi, 0)) + __builtin_bit_cast(float, __builtin_amdgcn_readlane(vi, 16))) +
           (__builtin_bit_cast(float, __builtin_amdgcn_readlane(vi, 32)) + __builtin_bit_cast(float, __builtin_amdgcn_readlane(vi, 48)));
}
DI void give_up(const unsigned char* ws, int lane) { if (lane == 0) __hip_atomic_store((gu32*)ws + W_TMO, 1u, RLX_AGENT); }
DI void poll16(const unsigned char* ws, int word, unsigned tag, int lane) {
#pragma unroll 1
    for (unsigned spins = 0;; ++spins) {
        unsigned f; gl32_sc1(f, ws, (unsigned)(word + (lane & 15) * FSTR) * 4u); wait_vm<0>(); pin(f);
        if (__all(f == tag)) break;
        if (spins > SPIN_CAP) { give_up(ws, lane); break; }
        __builtin_amdgcn_s_sleep(PSLEEP);
    }
}
DI void fx_load(unsigned (&w)[8], unsigned o0, unsigned o1, unsigned o2, unsigned o3, const void* wr, const void* wi, int k4) {
    asm volatile("s_cmp_lg_u32 %14, 0\n\ts_cbranch_scc1 .Lnofx%=\n\t"
                 "global_load_ushort %0, %8, %12\n\tglobal_load_ushort %1, %9, %12\n\tglobal_load_ushort %2, %10, %12\n\tglobal_load_ushort %3, %11, %12\n\t"
                 "global_load_ushort %4, %8, %13\n\tglobal_load_ushort %5, %9, %13\n\tglobal_load_ushort %6, %10, %13\n\tglobal_load_ushort %7, %11, %13\n"
                 ".Lnofx%=:" : "=&v"(w[0]), "=&v"(w[1]), "=&v"(w[2]), "=&v"(w[3]), "=&v"(w[4]), "=&v"(w[5]), "=&v"(w[6]), "=&v"(w[7])
                 : "v"(o0), "v"(o1), "v"(o2), "v"(o3), "s"(wr), "s"(wi), "s"(k4) : "memory", "scc");
}
DI float cosr(float x) { return __builtin_amdgcn_cosf(x); }
DI float sinr(float x) { return __builtin_amdgcn_sinf(x); }
DI u32x4 frag_cs(int a, int b0, int n) {
    float c[4], sn[4];
#pragma unroll
    for (int j = 0; j < 4; ++j) { const float th = (float)((a * (b0 + j)) & (n - 1)) * (1.0f / n); c[j] = cosr(th); sn[j] = -sinr(th); }
    return pack8(c, sn);
}

#define STAMP(i) do {} while (0)
#define TS(i) do {} while (0)

constexpr int P1_STAGE = 49152, P1_BOFF = 16384;
constexpr int L_MISC = 147456;
constexpr int L_XI = L_MISC;
constexpr int L_ST = L_MISC + 8704;
constexpr int L_YX = L_ST + 1024;
constexpr int L_SCR = L_YX + 4096;
constexpr int L_FLG = L_SCR + 256;
constexpr int L_END = L_FLG + 16;
static_assert(L_END <= 163840, "lds");
constexpr int LDS_BYTES = 163840;
constexpr int L_RAW = 98304;
constexpr int P3_SLOT = 16384;
DI constexpr int p3_slot(int s) { return (s % 9) * P3_SLOT; }

DI int panel_row(int r, int m) { return 64 * (m & 31) + 4 * r + (m >> 5); }
DI int p3_ks(int s, int kg) { const int n = 4 * s + kg; if (s >= 12) return 7 * (n - 48) + 3; const int p = n / 6, o = n - 6 * p; return 7 * p + o + (o >= 3); }

struct LdSrc { unsigned a[4]; const unsigned char* b[8]; long sB64; const unsigned char* t; long tadv; };
DI void p1_issue(LdSrc& s, const unsigned char* x, unsigned Lb, int lw, int j) {
    const unsigned sb = Lb + (j % 3) * P1_STAGE;
#pragma unroll
    for (int i = 0; i < 4; ++i) glds16s(x + j * 128, s.a[i], sb + (4 * lw + i) * 1024);
#pragma unroll
    for (int u = 0; u < 8; ++u) { const int i = (lw & 1) + 2 * (u & 1) + 4 * (lw >> 1) + 8 * ((u >> 1) & 1) + 16 * (u >> 2);
        glds16(s.b[u], sb + P1_BOFF + i * 1024); s.b[u] += s.sB64; }
}
DI void p1_touch(LdSrc& s, unsigned Lb) { glds4(s.t, Lb + L_SCR); s.t += s.tadv; }
DI void p3_issue(const Params& p, unsigned Lb, int lw, int cc, int s, unsigned wv, unsigned fv) {
    const unsigned sb = Lb + p3_slot(s) + lw * 4096;
    if (s < 14) { const unsigned char* base = (const unsigned char*)p.wd + (size_t)p3_ks(s, lw) * 32768;
#pragma unroll
        for (int i = 0; i < 4; ++i) glds16s(base + i * 8192, wv, sb + i * 1024); }
    else { const unsigned char* base = (const unsigned char*)&g_inv + cc * 65536 + (s - 14) * 16384 + lw * 4096;
#pragma unroll
        for (int i = 0; i < 4; ++i) glds16s(base + i * 1024, fv, sb + i * 1024); }
}

DI void p3_issue2(const Params& p, unsigned Lb, int w, int cc, int s, unsigned wv, unsigned fv) {
    const unsigned sb = Lb + p3_slot(s) + w * 2048;
    if (s < 14) { const unsigned char* base = (const unsigned char*)p.wd + (size_t)p3_ks(s, w >> 1) * 32768 + (w & 1) * 16384;
        glds16s(base, wv, sb); glds16s(base + 8192, wv, sb + 1024); }
    else { const unsigned char* base = (const unsigned char*)&g_inv + cc * 65536 + (s - 14) * 16384 + w * 2048;
        glds16s(base, fv, sb); glds16s(base + 1024, fv, sb + 1024); }
}

template <bool DFTW>
DI void p1_compute(LAS unsigned char* L, unsigned Lb, int wc, int lane, f32x4 (&acc)[4][8], f32x4 (&csm)[4], unsigned (&tsv)[32]) {
    asm volatile("" : "+v"(lane));
    const int fr = lane & 15, g = lane >> 4;
    const int qq = fr >> 2, p = lane & 3;
    int aoff0 = fr * 128 + (((0 + g) ^ (fr & 7)) << 4);
    int aoff1 = fr * 128 + (((4 + g) ^ (fr & 7)) << 4);
    const int sw = 2 * qq + 8 * (g & 1);
    int boff[4];
#pragma unroll
    for (int q = 0; q < 4; ++q) boff[q] = P1_BOFF + (8 * g + qq) * 512 + (((8 * wc + 2 * q + (p >> 1)) ^ sw) << 4) + 8 * (p & 1);
    u32x4 xa[2][4], wb[2][4];
    auto rdx = [&](int ks, int mh, int i) { xa[mh][i] = lds128(L, (ks ? aoff1 : aoff0) + (4 * mh + i) * 2048); };
    auto rdw = [&](int ks, int q) { const int o = boff[q] + ks * 16384; wb[ks][q] = lds_tr2(L, o, o + 2048); };
    auto rebase = [&](int d) { aoff0 += d; aoff1 += d; boff[0] += d; boff[1] += d; boff[2] += d; boff[3] += d;
        asm volatile("" : "+v"(aoff0), "+v"(aoff1), "+v"(boff[0]), "+v"(boff[1]), "+v"(boff[2]), "+v"(boff[3])); };
    auto step = [&](int ks, int mh, bool more) {
#pragma unroll
        for (int i = 0; i < 16; ++i) {
            const int q = i >> 2, mt = i & 3;
            if (DFTW && q >= 2) acc[q][4 * mh + mt] = mfma(xa[mh][mt], wb[ks][q], acc[q][4 * mh + mt]);
            else acc[q][4 * mh + mt] = mfma(wb[ks][q], xa[mh][mt], acc[q][4 * mh + mt]);
            if (more) {
                if (mh == 0) { if (i >= 1 && i <= 4) rdx(ks, 1, i - 1); }
                else { if (i < 4) rdx(ks ^ 1, 0, i); else if (i < 8) rdw(ks ^ 1, i - 4); }
            }
            if (mh == 0 && (i & 3) == 3) { const int d = i >> 2; if (!DFTW || d < 2) csm[d] = mfma(wb[ks][d], u32x4{0x3F803F80u, 0x3F803F80u, 0x3F803F80u, 0x3F803F80u}, csm[d]); }
            SB();
        }
    };
    BAR();
    TS(0);
#pragma unroll
    for (int i = 0; i < 4; ++i) { rdx(0, 0, i); rdw(0, i); }
    SB();
    static_for<0, 8>([&](auto J) {
        constexpr int j = J.v;
        step(0, 0, true);
        step(0, 1, true);
        step(1, 0, true);
        if constexpr (j < 7) { WAIT_LGKM0(); BAR(); TS(j + 1); rebase((j + 1) % 3 == 0 ? -2 * P1_STAGE : P1_STAGE);
            SB(); }
        step(1, 1, j < 7);
    });
    TS(8);
}

constexpr int HD = 5;
constexpr int FTS = 10, FTC = 12;
constexpr int p3_issue_at(int o) { return o < 14 ? o - HD : FTC; }
constexpr int p3_opcnt(int o) { return (o >= 12 && o <= 14) ? 4 : 2; }
enum { K_OP, K_TST, K_W, K_FT };
constexpr int p3_after(int kind, int idx, int s_now) {
    int n = -1;
    auto hit = [&](bool is, int cnt) { if (is) n = 0; else if (n >= 0) n += cnt; };
    for (int o = 0; o < HD; ++o) hit(kind == K_OP && idx == o, 2);
    hit(kind == K_TST, 1); hit(false, 1);
    for (int s = 0; s < s_now; ++s) {
        for (int o = HD; o < 18; ++o) if (p3_issue_at(o) == s) hit(kind == K_OP && idx == o, p3_opcnt(o));
        if (s >= 3 && s + 6 < 18) hit(kind == K_W && idx == s + 6, 2);
        if (s == FTS) hit(kind == K_FT, 1);
    }
    return n;
}
constexpr int cmin(int a, int b) { return a < b ? a : b; }
constexpr int p3_wait(int s) {
    int n = p3_after(K_OP, s, s);
    if (s == 3) n = cmin(n, p3_after(K_TST, 0, s));
    if (s % 3 == 0 && s >= 9) n = cmin(n, p3_after(K_W, s + 2, s));
    if (s == FTC) n = cmin(n, p3_after(K_FT, 0, s));
    return n;
}
static_assert(p3_wait(0) == 2 * (HD - 1) + 2 && p3_wait(3) == 1 + 6, "queue model");

struct P3Ctx { unsigned hbase, h2base, tbase; int woff[4]; };
DI void p3_rdw(LAS unsigned char* L, const P3Ctx& c, int kg, int lane, int s, u32x4 (&wf)[4]) {
#pragma unroll
    for (int dt = 0; dt < 4; ++dt) {
        if (s < 14) wf[dt] = lds_tr2(L, p3_slot(s) + c.woff[dt], p3_slot(s) + c.woff[dt] + 2048);
        else wf[dt] = lds128(L, p3_slot(s) + (kg * 4 + dt) * 1024 + lane * 16);
    }
}

__global__ void __launch_bounds__(NT, 2) k_fused(Params p) {
    extern __shared__ __attribute__((aligned(16))) unsigned char smem[];
    LAS unsigned char* L = (LAS unsigned char*)smem;
    const unsigned Lb = (unsigned)(size_t)L;
    const int wid = __builtin_amdgcn_readfirstlane(threadIdx.x >> 6);
    const int wc = wid & 3;
    const int b = blockIdx.x, xcd = b & 7, bi = b >> 3;
    const int r = (xcd & 1) * 8 + (bi & 7), c = (xcd >> 1) * 4 + (bi >> 3);
    const int jq = b & 7, kq = b >> 3;
    const int cc = (xcd & 1) * 4 + (bi & 3), hp = (xcd >> 1) * 8 + (bi >> 2), r3 = hp >> 1, h3 = hp & 1;
    const unsigned char* ws = p.ws;
    unsigned tsv[32];
#pragma unroll
    for (int i = 0; i < 32; ++i) tsv[i] = 0;
    STAMP(0);
    TS(31);
    const int odd = c & 1;
    const int u2 = wid >> 2, w2 = wid & 3, k4 = 2 * kq + u2;
    LAS f32x2* ST = (LAS f32x2*)(L + L_ST);
    f32x4 acc[4][8], csm[4];
    u32x4 eaC[2], eaS[2]; float tcA[2][4], tsA[2][4];
#pragma unroll
    for (int i = 0; i < 4; ++i) csm[i] = f32x4{0.f, 0.f, 0.f, 0.f};
    unsigned tag;
    {
    const int tid = threadIdx.x, lane = tid & 63;
    if (wid >= 4) {
        const int lw = wid - 4;
        unsigned epoch; gl32_sc1(epoch, ws, 0u);
        LdSrc ls;
        {
            const int idx = lw * 22 + (lane < 22 ? lane : 0);
            if (idx < 32) { ls.t = (const unsigned char*)p.x + (size_t)panel_row(r, 32 * (c & 3) + idx) * 1024; ls.tadv = 128; }
            else { const int i = idx - 32, row = 8 * (r & 7) + i / 7, it = i % 7;
                if (it == 6) { ls.t = (const unsigned char*)&g_fwd + c * 32768 + row * 64; ls.tadv = 64 * 64; }
                else { const int it3 = it % 3; ls.t = (const unsigned char*)(it < 3 ? p.wg : p.wu) + (size_t)row * (F * 2) + c * 224 + (it3 == 2 ? 220 : it3 * 128); ls.tadv = 64L * F * 2; } }
        }
        p1_touch(ls, Lb);
        SB();
#pragma unroll
        for (int i = 0; i < 4; ++i) { const int m = 8 * (4 * lw + i) + (lane >> 3); ls.a[i] = (unsigned)panel_row(r, m) * 1024u + (((lane & 7) ^ (lane >> 3)) << 4); }
        {
            const int b0 = lw & 1, b2 = lw >> 1;
            const int swz = 2 * (2 * b0 + (lane >> 5)) + 8 * b2;
            const int ch = (lane & 31) ^ swz, nt = ch >> 1, half = ch & 1, cw = nt >> 2, q = nt & 3;
            const unsigned char* base; int col, strideB;
            if (cw < 3) { const int ft = odd + 2 * cw + (q & 1); base = (const unsigned char*)(q < 2 ? p.wg : p.wu); col = 112 * c + 16 * ft + 8 * half; strideB = F * 2; }
            else if (q < 2) { const int ft = odd ? 0 : 6; base = (const unsigned char*)(q == 0 ? p.wg : p.wu); col = 112 * c + 16 * ft + 8 * half; strideB = F * 2; }
            else { base = (const unsigned char*)&g_fwd + c * 32768; col = 16 * (q - 2) + 8 * half; strideB = 64; }
            const unsigned char* b00 = base + (size_t)(2 * b0 + 8 * b2 + (lane >> 5)) * strideB + col * 2;
#pragma unroll
            for (int u = 0; u < 8; ++u) ls.b[u] = b00 + (size_t)(4 * (u & 1) + 16 * ((u >> 1) & 1) + 32 * (u >> 2)) * strideB;
            ls.sB64 = (long)strideB * 64;
        }
        p1_issue(ls, (const unsigned char*)p.x, Lb, lw, 0);
        p1_touch(ls, Lb); p1_touch(ls, Lb); p1_touch(ls, Lb);
        p1_issue(ls, (const unsigned char*)p.x, Lb, lw, 1);
        STAMP(14);
        if (wid == 4 && lane < 4) *((volatile LAS unsigned*)(L + L_FLG) + lane) = 0u;
        wait_vm<15>(); pin(epoch); tag = epoch + 1u;
        if (r == 0 && tid == 256) __hip_atomic_store((gu32*)p.ws + W_CT + c * 32, tag << 8, RLX_AGENT);
        WAIT_LGKM0();
        STAMP(15);
        BAR();
        p1_touch(ls, Lb);
        p1_issue(ls, (const unsigned char*)p.x, Lb, lw, 2);
        SB();
        const unsigned char *st0, *st1;
        {
            auto wline = [&](int i) { const int st = i >> 2, ln = (bi >> 2) * 16 + lw * 4 + (i & 3);
                return st < 14 ? (const unsigned char*)p.wd + (size_t)(32 * p3_ks(st, ln >> 5) + (ln & 31)) * 1024 + cc * 128
                               : (const unsigned char*)&g_inv + (size_t)cc * 65536 + (st - 14) * 16384 + ln * 128; };
            st0 = wline(lane);
            const int fi = (bi * 4 + lw) * 20 + (lane >= 8 && lane < 28 ? lane - 8 : 0);
            const int arr = fi / 1280, rr = (fi % 1280) / 5, ll = (fi % 1280) % 5;
            const int jrow = 4 * jq + (rr & 3) + 32 * (rr >> 2);
            st1 = lane < 8 ? wline(64 + lane) : (const unsigned char*)(arr ? p.wi : p.wr) + (size_t)jrow * (DF * 2) + min(ll * 128, 510);
        }
        u32x2 gran; const unsigned goff = (unsigned)WS_STAT + (unsigned)(r * 128 + (tid & 127)) * 8u;
        static_for<0, 7>([&](auto J) {
            constexpr int j = J.v;
            if constexpr (j < 4) wait_vm<13>(); else if constexpr (j == 4) wait_vm<12>();
            else if constexpr (j == 5) wait_vm<15>(); else { wait_vm<3>(); WAIT_LGKM0(); }
            BAR();
            if constexpr (j < 3) p1_touch(ls, Lb);
            if constexpr (j + 3 < 8) p1_issue(ls, (const unsigned char*)p.x, Lb, lw, j + 3);
            if constexpr (j == 4) {
                gl64_sc1(gran, ws, goff);
                glds4(st0, Lb + L_SCR); glds4(st1, Lb + L_SCR);
            }
            if constexpr (j == 5) {
                wait_vm<2>();
                pin(gran);
                if (lw < 2) {
                    if (!__all(gran.y == tag)) {
#pragma unroll 1
                        for (unsigned spins = 0;; ++spins) {
                            u32x2 g2; gl64_sc1(g2, ws, goff); wait_vm<0>(); pin(g2); gran = g2;
                            if (__all(g2.y == tag)) break;
                            if (spins > SPIN_CAP) { give_up(ws, lane); break; }
                            __builtin_amdgcn_s_sleep(1);
                        }
                    }
                    ST[tid & 127] = f32x2{bf_hi(gran.x), 1.0f / bf_lo(gran.x)};
                }
            }
            if constexpr (j == 6) {
                const unsigned wv0 = (unsigned)(lane >> 3) * 1024u + cc * 128u + (unsigned)(((lane & 7) ^ (2 * ((lane >> 4) & 3))) << 4), fv0 = lane * 16u;
#pragma unroll
                for (int st = 0; st < 3; ++st) p3_issue(p, Lb, lw, cc, st, wv0, fv0);
            }
        });
        {
            const int fr = lane & 15, g = lane >> 4;
#pragma unroll
            for (int jt = 0; jt < 2; ++jt) {
                float cv[8], sv[8];
#pragma unroll
                for (int j = 0; j < 8; ++j) { const float th = (float)(((16 * jt + fr) * (8 * g + j)) & 31) * (1.0f / 32.0f); cv[j] = cosr(th); sv[j] = sinr(th); }
                eaC[jt] = u32x4{pk_bf16(cv[0], cv[1]), pk_bf16(cv[2], cv[3]), pk_bf16(cv[4], cv[5]), pk_bf16(cv[6], cv[7])};
                eaS[jt] = u32x4{pk_bf16(sv[0], sv[1]), pk_bf16(sv[2], sv[3]), pk_bf16(sv[4], sv[5]), pk_bf16(sv[6], sv[7])};
#pragma unroll
                for (int e = 0; e < 4; ++e) { const float th = (float)((16 * jt + fr) * (4 * r + e)) * (1.0f / 2048.0f); tcA[jt][e] = cosr(th); tsA[jt][e] = sinr(th); }
            }
        }
#pragma unroll
        for (int q = 0; q < 4; ++q)
#pragma unroll
            for (int mt = 0; mt < 8; ++mt) acc[q][mt] = f32x4{0.f, 0.f, 0.f, 0.f};
    } else {
        if (wid == 0) {
            const unsigned char* pc = (const unsigned char*)__builtin_amdgcn_s_getpc();
            const int ln = bi * 10 + (lane < 10 ? lane : 0);
            glds4(pc - ((size_t)pc & 127) + (size_t)min(ln * 128, CODE_PF_BYTES), Lb + L_SCR);
        }
        if (wid == 1 || wid == 2) {
            const unsigned char* a;
            if (wid == 1) a = lane < 32 ? (const unsigned char*)p.x + (size_t)lane * 65536 : (const unsigned char*)p.wg + (size_t)min(lane - 32, 27) * 65536;
            else a = lane < 32 ? (const unsigned char*)p.wu + (size_t)min(lane, 27) * 65536 : (const unsigned char*)&g_fwd + (size_t)(lane & 7) * 65536;
            glds4(a, Lb + L_SCR);
        }
        unsigned epoch; gl32_sc1(epoch, ws, 0u);
        u32x4 xrow[2];
#pragma unroll
        for (int i = 0; i < 2; ++i) gl128(xrow[i], (const unsigned char*)p.x + (size_t)panel_row(r, 8 * c + 2 * wid + i) * 1024 + lane * 16);
        wait_vm<0>(); pin(epoch); pin(xrow[0]); pin(xrow[1]);
        tag = epoch + 1u;
#pragma unroll
        for (int i = 0; i < 2; ++i) {
            float v[8];
#pragma unroll
            for (int e = 0; e < 4; ++e) { v[2 * e] = bf_lo(xrow[i][e]); v[2 * e + 1] = bf_hi(xrow[i][e]); }
            float s = ((v[0] + v[1]) + (v[2] + v[3])) + ((v[4] + v[5]) + (v[6] + v[7]));
            s = wave_sum(s);
            const float mean = s * (1.0f / 512.0f);
            float q2 = 0.f;
#pragma unroll
            for (int e = 0; e < 8; ++e) { const float d = v[e] - mean; q2 += d * d; }
            q2 = wave_sum(q2);
            const float var = q2 * (1.0f / 512.0f);
            const float m_bf = bfr(mean), v_bf = bfr(var), s_bf = bfr(sqrtf(bfr(v_bf + 1e-5f)));
            const unsigned val = (__builtin_bit_cast(unsigned, m_bf) & 0xffff0000u) | (__builtin_bit_cast(unsigned, s_bf) >> 16);
            if (lane == 0) __hip_atomic_store((gu64*)(p.ws + WS_STAT) + (r * 128 + 8 * c + 2 * wid + i), ((unsigned long long)tag << 32) | val, RLX_AGENT);
        }
#pragma unroll
        for (int q = 0; q < 4; ++q)
#pragma unroll
            for (int mt = 0; mt < 8; ++mt) acc[q][mt] = f32x4{0.f, 0.f, 0.f, 0.f};
        STAMP(1);
        if (wid < 3) p1_compute<false>(L, Lb, wc, lane, acc, csm, tsv); else p1_compute<true>(L, Lb, wc, lane, acc, csm, tsv);
        STAMP(2);
        WAIT_LGKM0();
        if (lane == 0) __hip_atomic_fetch_add((LAS unsigned*)(L + L_FLG) + 3, 1u, __ATOMIC_RELAXED, __HIP_MEMORY_SCOPE_WORKGROUP);
    }
    }
    int tz = threadIdx.x; asm volatile("" : "+v"(tz));
    const int tid = tz, lane = tz & 63, fr = lane & 15, g = lane >> 4;
    const __amdgpu_buffer_rsrc_t rs = __builtin_amdgcn_make_buffer_rsrc((void*)p.ws, 0, (int)WS_END, 0x00020000);
    const unsigned wv = (unsigned)(lane >> 3) * 1024u + cc * 128u + (unsigned)(((lane & 7) ^ (2 * ((lane >> 4) & 3))) << 4), fv = lane * 16u;
    volatile LAS unsigned* flg = (volatile LAS unsigned*)(L + L_FLG);
    STAMP(3);
    const int fj1 = 4 * jq + (fr & 3), fk = 4 * k4 + (fr >> 2);
    unsigned fw[4], fwi[4];
    auto signal = [&](int cnt, int word) {
        if (lane == 0) { const unsigned old = __hip_atomic_fetch_add((LAS unsigned*)(L + L_FLG) + cnt, 1u, __ATOMIC_RELAXED, __HIP_MEMORY_SCOPE_WORKGROUP);
            if (old == 3u) __hip_atomic_store((gu32*)p.ws + word, tag, RLX_AGENT); }
    };
    if (wid < 3) {
        const f32x4 cg0 = csm[0], cg1 = csm[1], cu0 = csm[2], cu1 = csm[3];
#pragma unroll
        for (int mt = 0; mt < 8; ++mt) {
            const f32x2 st = ST[16 * mt + fr];
            const float r2 = st.y * st.y;
            const f32x4 h0 = (acc[0][mt] - st.x * cg0) * (acc[2][mt] - st.x * cu0) * r2;
            const f32x4 h1 = (acc[1][mt] - st.x * cg1) * (acc[3][mt] - st.x * cu1) * r2;
            st_sc1(rs, (int)WS_H + ((((r * 8 + mt) * 48 + 3 * c + wc) * 64 + lane) << 4), pack8(h0, h1));
        }
    } else if (wid == 3) {
#pragma unroll
        for (int q = 0; q < 2; ++q)
#pragma unroll
            for (int mt = 0; mt < 8; ++mt) *(LAS f32x4*)(L + L_RAW + ((q * 8 + mt) * 64 + lane) * 16) = acc[2 + q][mt];
        WAIT_LGKM0();
        flg[0] = 1u;
        const f32x4 cg0 = csm[0], cu0 = csm[1];
#pragma unroll
        for (int mt = 0; mt < 8; ++mt) {
            const f32x2 st = ST[16 * mt + fr];
            const float r2 = st.y * st.y;
            const f32x4 h0 = (acc[0][mt] - st.x * cg0) * (acc[1][mt] - st.x * cu0) * r2;
            st_sc1_64(rs, (int)WS_H2 + ((((r * 8 + mt) * 16 + c) * 64 + lane) << 3), u32x2{pk_bf16(h0[0], h0[1]), pk_bf16(h0[2], h0[3])});
        }
    } else {
        const int mt = wid - 4;
        const int kq_ = fr >> 2, s2l = fr & 3;
#pragma unroll 1
        for (unsigned spins = 0; flg[0] == 0u && spins < (1u << 20); ++spins) __builtin_amdgcn_s_sleep(2);
        asm volatile("" ::: "memory");
        __builtin_amdgcn_s_setprio(3);
        u32x4 are, aim;
        {
            const int m0 = 32 * s2l + 8 * g, tl = 2 * s2l + (g >> 1), ln = 4 * mt + kq_ + 32 * (g & 1);
            const f32x4 r0 = *(LAS f32x4*)(L + L_RAW + (tl * 64 + ln) * 16), r1 = *(LAS f32x4*)(L + L_RAW + (tl * 64 + ln + 16) * 16);
            const f32x4 i0 = *(LAS f32x4*)(L + L_RAW + ((8 + tl) * 64 + ln) * 16), i1 = *(LAS f32x4*)(L + L_RAW + ((8 + tl) * 64 + ln + 16) * 16);
            const f32x4 sa = *(LAS f32x4*)(ST + m0), sb = *(LAS f32x4*)(ST + m0 + 2), sc = *(LAS f32x4*)(ST + m0 + 4), sd = *(LAS f32x4*)(ST + m0 + 6);
            const float csf = (c == 0 && mt == 0 && kq_ == 0) ? 512.0f : 0.0f;
            are = u32x4{pk_bf16((r0[0] - sa[0] * csf) * sa[1], (r0[1] - sa[2] * csf) * sa[3]), pk_bf16((r0[2] - sb[0] * csf) * sb[1], (r0[3] - sb[2] * csf) * sb[3]),
                        pk_bf16((r1[0] - sc[0] * csf) * sc[1], (r1[1] - sc[2] * csf) * sc[3]), pk_bf16((r1[2] - sd[0] * csf) * sd[1], (r1[3] - sd[2] * csf) * sd[3])};
            aim = u32x4{pk_bf16(i0[0] * sa[1], i0[1] * sa[3]), pk_bf16(i0[2] * sb[1], i0[3] * sb[3]), pk_bf16(i1[0] * sc[1], i1[1] * sc[3]), pk_bf16(i1[2] * sd[1], i1[3] * sd[3])};
        }
        const f32x4 z = {0.f, 0.f, 0.f, 0.f};
        const bool special = (c == 0 && mt == 0 && g == 0);
#pragma unroll
        for (int jt = 0; jt < 2; ++jt) {
            const f32x4 Prc = mfma(are, eaC[jt], z), Pis = mfma(aim, eaS[jt], z), Prs = mfma(are, eaS[jt], z), Pic = mfma(aim, eaC[jt], z);
            const int j1 = 16 * jt + fr;
            float pr[4], qi[4], pr2[4], qi2[4];
#pragma unroll
            for (int e = 0; e < 4; ++e) {
                const float cw = tcA[jt][e], sw = tsA[jt][e];
                const float ar = special ? Prc[e] : Prc[e] + Pis[e], ai = special ? -Prs[e] : Pic[e] - Prs[e];
                pr[e] = ar * cw + ai * sw; qi[e] = -(ai * cw - ar * sw);
                const float br = Pic[e], bi2 = -Pis[e];
                pr2[e] = br * cw + bi2 * sw; qi2[e] = -(bi2 * cw - br * sw);
            }
            const int U = (j1 >> 2) * 64 + 4 * c + mt, lp = 16 * (r & 3) + 4 * g + (j1 & 3);
            st_sc1(rs, (int)WS_AP + (((U * 4 + (r >> 2)) * 64 + lp) << 4), pack8(pr, qi));
            if (special) st_sc1(rs, (int)WS_AX + (((((j1 >> 2) * 4 + (r >> 2)) * 4 + (r & 3)) * 4 + (j1 & 3)) << 4), pack8(pr2, qi2));
        }
        __builtin_amdgcn_s_setprio(0);
        wait_vm<0>();
        signal(1, W_FA + (c * 16 + r) * FSTR);
    }
#pragma unroll
    for (int e = 0; e < 4; ++e) { const unsigned o = (unsigned)((fj1 + 32 * (16 * w2 + 4 * g + e)) * DF + fk) * 2u; gl16(fw[e], p.wr, o); gl16(fwi[e], p.wi, o); }
    unsigned fx[8];
    {
        const unsigned o0 = (unsigned)((fj1 + 32 * (16 * w2 + 4 * g)) * DF + 256) * 2u;
        fx_load(fx, o0, o0 + 32u * DF * 2u, o0 + 64u * DF * 2u, o0 + 96u * DF * 2u, p.wr, p.wi, k4);
    }
    if (wid < 4) {
        if (k4 == 0) wait_vm<16>(); else wait_vm<8>();
        signal(2, W_FH + (r * 16 + c) * FSTR);
#pragma unroll 1
        for (unsigned spins = 0; flg[3] < 4u && spins < (1u << 22); ++spins) __builtin_amdgcn_s_sleep(1);
        asm volatile("" ::: "memory");
#pragma unroll
        for (int st = 3; st < 6; ++st) p3_issue(p, Lb, wid, cc, st, wv, fv);
#pragma unroll 1
        for (unsigned spins = 0; flg[1] < 4u && spins < (1u << 22); ++spins) __builtin_amdgcn_s_sleep(1);
        asm volatile("" ::: "memory");
#pragma unroll
        for (int st = 6; st < 9; ++st) p3_issue(p, Lb, wid, cc, st, wv, fv);
    }
    STAMP(5);
    {
        const int kg = wid & 3, rh = wid >> 2;
        u32x4 E1[4], E2[4];
#pragma unroll
        for (int t = 0; t < 4; ++t) E1[t] = frag_cs(16 * w2 + fr, 16 * t + 4 * g, 64);
#pragma unroll
        for (int i = 0; i < 4; ++i) E2[i] = frag_cs(16 * (i >> 1) + fr, 16 * (i & 1) + 4 * g, 32);
        float tc[4], tsn[4];
#pragma unroll
        for (int e = 0; e < 4; ++e) { const float th = (float)((4 * jq + e) * (16 * w2 + fr)) * (1.0f / 2048.0f); tc[e] = cosr(th); tsn[e] = sinr(th); }
        P3Ctx cx;
        {
            const int qq = fr >> 2, pp = lane & 3, row = 32 * kg + 4 * g + qq, sw = 2 * ((row >> 1) & 3);
#pragma unroll
            for (int dt = 0; dt < 4; ++dt) cx.woff[dt] = row * 128 + (((2 * dt + (pp >> 1)) ^ sw) << 4) + 8 * (pp & 1);
            cx.hbase = (unsigned)WS_H + (unsigned)(((r3 * 8 + 4 * h3 + 2 * rh) * 48 + kg) * 64 + lane) * 16u;
            cx.h2base = (unsigned)WS_H2 + (unsigned)(((r3 * 8 + 4 * h3 + 2 * rh) * 16 + 2 * kg) * 64 + lane) * 8u;
            cx.tbase = (unsigned)WS_T + (unsigned)((((4 * r3 + 2 * h3 + rh) * 16 + 4 * kg) * 2) * 64 + lane) * 16u;
        }
        SB();
        STAMP(6);
        if (wid == 0) poll16(ws, W_FA + (kq >> 1) * 16 * FSTR, tag, lane);
        if (wid == 1) poll16(ws, W_FH + r3 * 16 * FSTR, tag, lane);
        BAR();
        STAMP(7);
        const int U = jq * 64 + k4;
        u32x4 Bf[4], Bx[4];
        u32x4 hb[HD][2]; u32x2 hh[2][4];
#pragma unroll
        for (int t = 0; t < 4; ++t) gl128_sc1(Bf[t], ws, (unsigned)WS_AP + (unsigned)((U * 4 + t) * 64 + lane) * 16u);
        {
            const unsigned xo = (unsigned)WS_AX + (unsigned)((jq * 16 + g) * 4 + (fr & 3)) * 16u;
            asm volatile("s_cmp_lg_u32 %6, 0\n\ts_cbranch_scc1 .Lnox%=\n\t"
                         "global_load_dwordx4 %0, %4, %5 sc1\n\tglobal_load_dwordx4 %1, %4, %5 offset:256 sc1\n\t"
                         "global_load_dwordx4 %2, %4, %5 offset:512 sc1\n\tglobal_load_dwordx4 %3, %4, %5 offset:768 sc1\n"
                         ".Lnox%=:" : "=&v"(Bx[0]), "=&v"(Bx[1]), "=&v"(Bx[2]), "=&v"(Bx[3]) : "v"(xo), "s"(ws), "s"(k4) : "memory", "scc");
        }
        static_for<0, HD>([&](auto Sx) { constexpr int o = Sx.v;
            gl128_sc1(hb[o][0], ws, cx.hbase + o * 4096u); gl128_sc1(hb[o][1], ws, cx.hbase + 48u * 1024u + o * 4096u); });
        wait_vm<2 * HD>();
        pin(Bf[0]); pin(Bf[1]); pin(Bf[2]); pin(Bf[3]); pin(Bx[0]); pin(Bx[1]); pin(Bx[2]); pin(Bx[3]);
        pin(fw[0]); pin(fw[1]); pin(fw[2]); pin(fw[3]); pin(fwi[0]); pin(fwi[1]); pin(fwi[2]); pin(fwi[3]);
        pin(fx[0]); pin(fx[1]); pin(fx[2]); pin(fx[3]); pin(fx[4]); pin(fx[5]); pin(fx[6]); pin(fx[7]);
        STAMP(8);
        {
            f32x4 a1 = {0.f, 0.f, 0.f, 0.f}, a2 = a1;
#pragma unroll
            for (int t = 0; t < 4; ++t) { a1 = mfma(E1[t], Bf[t], a1); a2 = mfma(E1[t], swap_neg(Bf[t]), a2); }
            constexpr float SC = 2.0f / (2048.0f * 512.0f);
            const float sc = (k4 == 0 && (fr >> 2) == 0) ? 0.5f * SC : SC;
            float yr[4], yi[4];
#pragma unroll
            for (int e = 0; e < 4; ++e) { const float xr = a1[e], xi = -a2[e], wre = bf_lo(fw[e]), wim = bf_lo(fwi[e]);
                yr[e] = (xr * wre - xi * wim) * sc; yi[e] = (xr * wim + xi * wre) * sc; }
            if (k4 == 0) {
                f32x4 n1 = {0.f, 0.f, 0.f, 0.f}, n2 = n1;
#pragma unroll
                for (int t = 0; t < 4; ++t) { n1 = mfma(E1[t], Bx[t], n1); n2 = mfma(E1[t], swap_neg(Bx[t]), n2); }
                float xr2[4], xi2[4];
#pragma unroll
                for (int e = 0; e < 4; ++e) { const float xr = n1[e], xi = -n2[e], wre = bf_lo(fx[e]), wim = bf_lo(fx[4 + e]);
                    xr2[e] = (xr * wre - xi * wim) * (0.5f * SC); xi2[e] = (xr * wim + xi * wre) * (0.5f * SC); }
                *(LAS u32x4*)(L + L_YX + (w2 * 64 + lane) * 16) = pack8(xr2, xi2);
            }
            *(LAS u32x4*)(L + L_XI + ((u2 * 4 + w2) * 64 + lane) * 16) = pack8(yr, yi);
        }
        WAIT_LGKM0(); BAR();
        u32x4 wf[2][4];
        p3_rdw(L, cx, kg, lane, 0, wf[0]);
        {
            f32x4 t1 = {0.f, 0.f, 0.f, 0.f}, t2 = t1;
#pragma unroll
            for (int t = 0; t < 4; ++t) { const u32x4 y = lds128(L, L_XI + ((u2 * 4 + t) * 64 + lane) * 16);
                t1 = mfma(y, E1[t], t1); t2 = mfma(swap_neg(y), E1[t], t2); }
            const int s2 = 16 * w2 + fr, ko = 4 * k4 + g;
            float tr[4], ti[4];
#pragma unroll
            for (int e = 0; e < 4; ++e) { tr[e] = t1[e] * tc[e] - t2[e] * tsn[e]; ti[e] = t2[e] * tc[e] + t1[e] * tsn[e]; }
            st_sc1(rs, (int)WS_T + ((((s2 * 16 + (ko >> 4)) * 2 + (jq >> 2)) * 64 + (jq & 3) * 16 + (ko & 15)) << 4), pack8(tr, ti));
            if (k4 == 0) {
                f32x4 x1 = {0.f, 0.f, 0.f, 0.f}, x2 = x1;
#pragma unroll
                for (int t = 0; t < 4; ++t) { const u32x4 y = lds128(L, L_YX + (t * 64 + lane) * 16);
                    x1 = mfma(y, E1[t], x1); x2 = mfma(swap_neg(y), E1[t], x2); }
#pragma unroll
                for (int e = 0; e < 4; ++e) { tr[e] = x1[e] * tc[e] - x2[e] * tsn[e]; ti[e] = x2[e] * tc[e] + x1[e] * tsn[e]; }
                if (g == 0) st_sc1(rs, (int)WS_TX + ((s2 * 8 + jq) << 4), pack8(tr, ti));
            }
        }
        u32x4 xres; gl128(xres, (const unsigned char*)p.x + ((size_t)panel_row(r3, 64 * h3 + (tid >> 3)) * 512 + 64 * cc + 8 * (tid & 7)) * 2);
        STAMP(9);
        f32x4 acc3[4][2];
#pragma unroll
        for (int dt = 0; dt < 4; ++dt) { acc3[dt][0] = f32x4{0.f, 0.f, 0.f, 0.f}; acc3[dt][1] = acc3[dt][0]; }
        unsigned ftf; u32x4 tx[2];
        static_for<0, 14>([&](auto Sx) {
            constexpr int s = Sx.v;
            wait_vm<p3_wait(s)>();
            if constexpr (s == 12 || s == 13) { pin(hh[s - 12][0]); pin(hh[s - 12][1]); pin(hh[s - 12][2]); pin(hh[s - 12][3]); }
            else { pin(hb[s % HD][0]); pin(hb[s % HD][1]); }
            if constexpr (s == FTC) {
                STAMP(12);
                pin(ftf);
                if (wid == 0) {
                    if (!__all(ftf == (tag << 8) + 16u)) {
#pragma unroll 1
                        for (unsigned spins = 0;; ++spins) {
                            __builtin_amdgcn_s_sleep(PSLEEP);
                            unsigned f2; gl32_sc1(f2, ws, (unsigned)(W_CT + (lane & 15) * 32) * 4u); wait_vm<0>(); pin(f2);
                            if (__all(f2 == (tag << 8) + 16u)) break;
                            if (spins > SPIN_CAP) { give_up(ws, lane); break; }
                        }
                    }
                    if (b == 0 && lane == 0) __hip_atomic_store((gu32*)p.ws, tag, RLX_AGENT);
                }
                STAMP(13);
            }
            if constexpr (s % 3 == 0 && s > 0) {
                WAIT_LGKM0(); BAR();
                if constexpr (s == 3) { STAMP(4); if (tid == 0) __hip_atomic_fetch_add((gu32*)p.ws + W_CT + (kq >> 1) * 32, 1u, RLX_AGENT); }
                p3_rdw(L, cx, kg, lane, s, wf[s & 1]);
            }
            TS(10 + s);
            if constexpr (s + 1 < 14 && (s + 1) % 3 != 0) p3_rdw(L, cx, kg, lane, s + 1, wf[(s + 1) & 1]);
            u32x4 b0, b1;
            if constexpr (s == 12 || s == 13) { b0 = u32x4{hh[s - 12][0].x, hh[s - 12][0].y, hh[s - 12][1].x, hh[s - 12][1].y}; b1 = u32x4{hh[s - 12][2].x, hh[s - 12][2].y, hh[s - 12][3].x, hh[s - 12][3].y}; }
            else { b0 = hb[s % HD][0]; b1 = hb[s % HD][1]; }
#pragma unroll
            for (int dt = 0; dt < 4; ++dt) { acc3[dt][0] = mfma(wf[s & 1][dt], b0, acc3[dt][0]); acc3[dt][1] = mfma(wf[s & 1][dt], b1, acc3[dt][1]); }
            SB();
            static_for<HD, 18>([&](auto Ox) {
                constexpr int o = Ox.v;
                if constexpr (p3_issue_at(o) == s) {
                    if constexpr (o < 12) { gl128_sc1(hb[o % HD][0], ws, cx.hbase + o * 4096u); gl128_sc1(hb[o % HD][1], ws, cx.hbase + 48u * 1024u + o * 4096u); }
                    else if constexpr (o < 14) {
                        gl64_sc1(hh[o - 12][0], ws, cx.h2base + (o - 12) * 4096u); gl64_sc1(hh[o - 12][1], ws, cx.h2base + (o - 12) * 4096u + 512u);
                        gl64_sc1(hh[o - 12][2], ws, cx.h2base + 8192u + (o - 12) * 4096u); gl64_sc1(hh[o - 12][3], ws, cx.h2base + 8192u + (o - 12) * 4096u + 512u); }
                    else { gl128_sc1(hb[o % HD][0], ws, cx.tbase + (o - 14) * 2048u); gl128_sc1(hb[o % HD][1], ws, cx.tbase + (o - 14) * 2048u + 1024u);
                        if constexpr (o == 14) { const unsigned txo = (unsigned)WS_TX + (unsigned)((4 * r3 + 2 * h3 + rh) * 8 + g) * 16u; gl128_sc1(tx[0], ws, txo); gl128_sc1(tx[1], ws, txo + 64u); } }
                }
            });
            if constexpr (s >= 3 && s + 6 < 18) p3_issue2(p, Lb, wid, cc, s + 6, wv, fv);
            if constexpr (s == FTS) gl32_sc1(ftf, ws, wid == 0 ? (unsigned)(W_CT + (lane & 15) * 32) * 4u : cx.hbase - lane * 16u);
            SB();
        });
        wait_vm<0>();
        static_for<14, 18>([&](auto Ox) { pin(hb[Ox.v % HD][0]); pin(hb[Ox.v % HD][1]); });
        pin(tx[0]); pin(tx[1]); pin(xres);
        WAIT_LGKM0(); BAR();
        TS(24);
        {
            u32x4 wz[2][4];
            p3_rdw(L, cx, kg, lane, 14, wz[0]); p3_rdw(L, cx, kg, lane, 15, wz[1]);
            u32x4 zf[4][2];
            static_for<0, 4>([&](auto Zx) {
                constexpr int z = Zx.v;
                const u32x4 T0 = hb[(14 + z) % HD][0], T1 = hb[(14 + z) % HD][1];
                u32x4 T0s = swap_neg(T0), T1s = swap_neg(T1);
                if constexpr (z == 0) {
                    if (kg == 0 && fr == 0) { T0s = tx[0]; T1s = tx[1]; }
                }
                f32x4 zr[2], zi[2];
#pragma unroll
                for (int st = 0; st < 2; ++st) { const f32x4 z0 = {0.f, 0.f, 0.f, 0.f}; zr[st] = mfma(T0, E2[st * 2 + 0], z0); zi[st] = mfma(T0s, E2[st * 2 + 0], z0); }
#pragma unroll
                for (int st = 0; st < 2; ++st) { zr[st] = mfma(T1, E2[st * 2 + 1], zr[st]); zi[st] = mfma(T1s, E2[st * 2 + 1], zi[st]); }
#pragma unroll
                for (int st = 0; st < 2; ++st) zf[z][st] = pack8(zr[st], zi[st]);
            });
#pragma unroll
            for (int z = 0; z < 2; ++z)
#pragma unroll
                for (int dt = 0; dt < 4; ++dt) { acc3[dt][0] = mfma(wz[z][dt], zf[z][0], acc3[dt][0]); acc3[dt][1] = mfma(wz[z][dt], zf[z][1], acc3[dt][1]); }
            p3_rdw(L, cx, kg, lane, 16, wz[0]); p3_rdw(L, cx, kg, lane, 17, wz[1]);
#pragma unroll
            for (int z = 0; z < 2; ++z)
#pragma unroll
                for (int dt = 0; dt < 4; ++dt) { acc3[dt][0] = mfma(wz[z][dt], zf[2 + z][0], acc3[dt][0]); acc3[dt][1] = mfma(wz[z][dt], zf[2 + z][1], acc3[dt][1]); }
        }
        STAMP(10);
        TS(25);
        WAIT_LGKM0(); BAR();
        constexpr int RP = 272;
#pragma unroll
        for (int dt = 0; dt < 4; ++dt)
#pragma unroll
            for (int st = 0; st < 2; ++st)
                *(LAS f32x4*)(L + (kg * 64 + 32 * rh + 16 * st + fr) * RP + (16 * dt + 4 * g) * 4) = acc3[dt][st];
        WAIT_LGKM0(); BAR();
        const int mm = tid >> 3, cg = tid & 7;
        f32x4 v0 = {0.f, 0.f, 0.f, 0.f}, v1 = v0;
#pragma unroll
        for (int q = 0; q < 4; ++q) { v0 += *(LAS f32x4*)(L + (q * 64 + mm) * RP + cg * 32); v1 += *(LAS f32x4*)(L + (q * 64 + mm) * RP + cg * 32 + 16); }
        const size_t o = (size_t)panel_row(r3, 64 * h3 + mm) * 512 + 64 * cc + 8 * cg;
        {
            const u32x4 ov = u32x4{pk_bf16(bf_lo(xres.x) + v0[0], bf_hi(xres.x) + v0[1]), pk_bf16(bf_lo(xres.y) + v0[2], bf_hi(xres.y) + v0[3]),
                                   pk_bf16(bf_lo(xres.z) + v1[0], bf_hi(xres.z) + v1[1]), pk_bf16(bf_lo(xres.w) + v1[2], bf_hi(xres.w) + v1[3])};
            const __amdgpu_buffer_rsrc_t ro = __builtin_amdgcn_make_buffer_rsrc((void*)p.out, 0, S * D * 2, 0x00020000);
            __builtin_amdgcn_raw_buffer_store_b128(ov, ro, (int)(o * 2), 0, 16);
        }
    }
    STAMP(11);
}

extern "C" void kernel_launch(void* const* d_in, const int* in_sizes, int n_in, void* d_out, int out_size, void* d_ws, size_t ws_size,
                              hipStream_t stream) {
    Params p;
    p.x = (const uint16_t*)d_in[0]; p.wr = (const uint16_t*)d_in[1]; p.wi = (const uint16_t*)d_in[2];
    p.wg = (const uint16_t*)d_in[4]; p.wu = (const uint16_t*)d_in[5]; p.wd = (const uint16_t*)d_in[6];
    p.out = (uint16_t*)d_out; p.ws = (unsigned char*)d_ws;
    (void)hipFuncSetAttribute((const void*)k_fused, hipFuncAttributeMaxDynamicSharedMemorySize, LDS_BYTES);
    k_fused<<<256, NT, LDS_BYTES, stream>>>(p);
}
```

```cpp
#include <hip/hip_runtime.h>
#include <stdint.h>

#define LAS __attribute__((address_space(3)))
#define GAS __attribute__((address_space(1)))
#define DI __device__ __forceinline__
typedef __bf16 bf8 __attribute__((ext_vector_type(8)));
typedef __bf16 bf2 __attribute__((ext_vector_type(2)));
typedef short s16x4 __attribute__((ext_vector_type(4)));
typedef float f32x4 __attribute__((ext_vector_type(4)));
typedef float f32x2 __attribute__((ext_vector_type(2)));
typedef unsigned u32x4 __attribute__((ext_vector_type(4)));
typedef unsigned u32x2 __attribute__((ext_vector_type(2)));
typedef GAS unsigned gu32;
typedef GAS unsigned long long gu64;
#define RLX_AGENT __ATOMIC_RELAXED, __HIP_MEMORY_SCOPE_AGENT

namespace tabs {
constexpr double PI = 3.14159265358979323846264338327950288;
constexpr double cos_q(double x) {
    double x2 = x * x, t = 1.0, s = 1.0;
    for (int i = 1; i <= 14; ++i) { t *= -x2 / ((2 * i - 1) * (2 * i)); s += t; }
    return s;
}
constexpr uint16_t f2bf(float f) {
    uint32_t u = __builtin_bit_cast(uint32_t, f);
    u += 0x7FFFu + ((u >> 16) & 1u);
    return (uint16_t)(u >> 16);
}
struct Base { uint16_t v[512];
    constexpr Base() : v{} {
        for (int m = 0; m <= 128; ++m) {
            double c = (m == 128) ? 0.0 : cos_q(2.0 * PI * m / 512.0);
            uint16_t b = f2bf((float)c);
            uint16_t nb = (m == 128) ? b : (uint16_t)(b ^ 0x8000u);
            v[m] = b; v[(512 - m) & 511] = b;
            v[256 - m] = nb; v[(256 + m) & 511] = nb;
        }
    }
};
constexpr Base base{};
struct Chunk { uint16_t v[16384]; };
constexpr Chunk mk_fwd(int c) {
    Chunk r{};
    for (int d = 0; d < 512; ++d)
        for (int n = 0; n < 32; ++n) { const int k = 16 * c + (n & 15);
            r.v[d * 32 + n] = base.v[((n >> 4) ? (k == 0 ? d * 256 : d * k + 128) : d * k) & 511]; }
    return r;
}
constexpr Chunk mk_inv(int blk) {
    Chunk r{};
    const int cc = blk >> 1;
    for (int e = 0; e < 16384; e += 8) {
        const int l = (e >> 3) & 63, dt = (e >> 9) & 3, kg = (e >> 11) & 3, s = ((blk & 1) << 1) + (e >> 13);
        const int kt = 4 * kg + s, d = 64 * cc + 16 * dt + (l & 15), k0 = 16 * kt + 4 * (l >> 4);
        for (int j = 0; j < 4; ++j) { const int k = k0 + j;
            r.v[e + j] = base.v[(k * d) & 511];
            r.v[e + 4 + j] = base.v[(k == 0 ? d * 256 : k * d + 128) & 511]; }
    }
    return r;
}
template <int I> constexpr Chunk fwd_c = mk_fwd(I);
template <int I> constexpr Chunk inv_c = mk_inv(I);
struct Big { Chunk c[16]; };
}
#define C16(N) {{tabs::N<0>, tabs::N<1>, tabs::N<2>, tabs::N<3>, tabs::N<4>, tabs::N<5>, tabs::N<6>, tabs::N<7>, \
                 tabs::N<8>, tabs::N<9>, tabs::N<10>, tabs::N<11>, tabs::N<12>, tabs::N<13>, tabs::N<14>, tabs::N<15>}}
alignas(256) static __device__ const tabs::Big g_fwd = C16(fwd_c);
alignas(256) static __device__ const tabs::Big g_inv = C16(inv_c);

constexpr int S = 2048, D = 512, F = 1792, DF = 257, NT = 512;
constexpr size_t WS_STAT = 98304;
constexpr size_t WS_AP = 131072;
constexpr size_t WS_AX = WS_AP + (2u << 20);
constexpr size_t WS_TX = WS_AX + 8192;
constexpr size_t WS_T = WS_AX + 65536;
constexpr size_t WS_H = WS_T + (2u << 20);
constexpr size_t WS_H2 = WS_H + (size_t)16 * 8 * 48 * 1024;
constexpr size_t WS_END = WS_H2 + (size_t)16 * 8 * 16 * 512;
constexpr int FSTR = 32;
constexpr int PSLEEP = 8;
constexpr int W_CT = 1024, W_TMO = 2048, W_FA = 4096, W_FH = W_FA + 256 * 32;
constexpr unsigned SPIN_CAP = 1u << 16;
constexpr int CODE_PF_BYTES = 37 * 1024;

struct Params {
    const uint16_t* x; const uint16_t* wr; const uint16_t* wi;
    const uint16_t* wg; const uint16_t* wu; const uint16_t* wd;
    uint16_t* out; unsigned char* ws;
};

template <int I> struct IC { static constexpr int v = I; };
template <int B, int E, class Fn> DI void static_for(Fn&& f) { if constexpr (B < E) { f(IC<B>{}); static_for<B + 1, E>(f); } }
DI f32x4 mfma(u32x4 a, u32x4 b, f32x4 c) {
    return __builtin_amdgcn_mfma_f32_16x16x32_bf16(__builtin_bit_cast(bf8, a), __builtin_bit_cast(bf8, b), c, 0, 0, 0);
}
DI unsigned pk_bf16(float lo, float hi) { bf2 p; p[0] = (__bf16)lo; p[1] = (__bf16)hi; return __builtin_bit_cast(unsigned, p); }
DI float bf_lo(unsigned u) { return __builtin_bit_cast(float, u << 16); }
DI float bf_hi(unsigned u) { return __builtin_bit_cast(float, u & 0xffff0000u); }
DI float bfr(float x) { return bf_lo(pk_bf16(x, x)); }
DI u32x4 lds128(LAS unsigned char* L, int off) { return *(LAS u32x4*)(L + off); }
DI u32x2 lds_tr(LAS unsigned char* L, int off) {
    return __builtin_bit_cast(u32x2, __builtin_amdgcn_ds_read_tr16_b64_v4i16((LAS s16x4*)(L + off)));
}
DI u32x4 lds_tr2(LAS unsigned char* L, int off0, int off1) {
    const u32x2 a = lds_tr(L, off0), b = lds_tr(L, off1);
    return u32x4{a.x, a.y, b.x, b.y};
}
DI u32x4 swap_neg(u32x4 v) { return u32x4{v.z, v.w, v.x ^ 0x80008000u, v.y ^ 0x80008000u}; }
DI u32x4 pack8(const float (&a)[4], const float (&b)[4]) { return u32x4{pk_bf16(a[0], a[1]), pk_bf16(a[2], a[3]), pk_bf16(b[0], b[1]), pk_bf16(b[2], b[3])}; }
DI u32x4 pack8(f32x4 a, f32x4 b) { return u32x4{pk_bf16(a[0], a[1]), pk_bf16(a[2], a[3]), pk_bf16(b[0], b[1]), pk_bf16(b[2], b[3])}; }
DI void glds16(const void* gsrc, unsigned lds_addr) {
    asm volatile("s_mov_b32 m0, %1\n\ts_nop 0\n\tglobal_load_lds_dwordx4 %0, off" :: "v"(gsrc), "s"(lds_addr) : "memory");
}
DI void glds16s(const void* sbase, unsigned voff, unsigned lds_addr) {
    asm volatile("s_mov_b32 m0, %2\n\ts_nop 0\n\tglobal_load_lds_dwordx4 %0, %1" :: "v"(voff), "s"(sbase), "s"(lds_addr) : "memory");
}
DI void glds4(const void* gsrc, unsigned lds_addr) {
    asm volatile("s_mov_b32 m0, %1\n\ts_nop 0\n\tglobal_load_lds_dword %0, off" :: "v"(gsrc), "s"(lds_addr) : "memory");
}
DI void gl128(u32x4& d, const void* p) { asm volatile("global_load_dwordx4 %0, %1, off" : "=v"(d) : "v"(p) : "memory"); }
DI void gl128_sc1(u32x4& d, const void* base, unsigned off) { asm volatile("global_load_dwordx4 %0, %1, %2 sc1" : "=v"(d) : "v"(off), "s"(base) : "memory"); }
DI void gl64_sc1(u32x2& d, const void* base, unsigned off) { asm volatile("global_load_dwordx2 %0, %1, %2 sc1" : "=v"(d) : "v"(off), "s"(base) : "memory"); }
DI void gl32_sc1(unsigned& d, const void* base, unsigned off) { asm volatile("global_load_dword %0, %1, %2 sc1" : "=v"(d) : "v"(off), "s"(base) : "memory"); }
DI void gl16(unsigned& d, const void* base, unsigned off) { asm volatile("global_load_ushort %0, %1, %2" : "=v"(d) : "v"(off), "s"(base) : "memory"); }
template <int N> DI void wait_vm() { static_assert(N >= 0 && N < 64, "vmcnt"); asm volatile("s_waitcnt vmcnt(%0)" :: "n"(N) : "memory"); }
template <class T> DI void pin(T& v) { asm volatile("" : "+v"(v)); }
#define WAIT_LGKM0() asm volatile("s_waitcnt lgkmcnt(0)" ::: "memory")
#define BAR() do { asm volatile("" ::: "memory"); __builtin_amdgcn_s_barrier(); asm volatile("" ::: "memory"); } while (0)
#define SB() __builtin_amdgcn_sched_barrier(0)
DI void st_sc1(__amdgpu_buffer_rsrc_t rs, int off, u32x4 v) { __builtin_amdgcn_raw_buffer_store_b128(v, rs, off, 0, 16); }
DI void st_sc1_64(__amdgpu_buffer_rsrc_t rs, int off, u32x2 v) { __builtin_amdgcn_raw_buffer_store_b64(v, rs, off, 0, 16); }
template <int CTRL> DI float dpp_add(float v) {
    return v + __builtin_bit_cast(float, __builtin_amdgcn_update_dpp(0, __builtin_bit_cast(int, v), CTRL, 0xf, 0xf, true));
}
DI float wave_sum(float v) {
    v = dpp_add<0xB1>(v);
    v = dpp_add<0x4E>(v);
    v = dpp_add<0x141>(v);
    v = dpp_add<0x140>(v);
    const int vi = __builtin_bit_cast(int, v);
    return (__builtin_bit_cast(float, __builtin_amdgcn_readlane(vi, 0)) + __builtin_bit_cast(float, __builtin_amdgcn_readlane(vi, 16))) +
           (__builtin_bit_cast(float, __builtin_amdgcn_readlane(vi, 32)) + __builtin_bit_cast(float, __builtin_amdgcn_readlane(vi, 48)));
}
DI void give_up(const unsigned char* ws, int lane) { if (lane == 0) __hip_atomic_store((gu32*)ws + W_TMO, 1u, RLX_AGENT); }
DI void poll16(const unsigned char* ws, int word, unsigned tag, int lane) {
#pragma unroll 1
    for (unsigned spins = 0;; ++spins) {
        unsigned f; gl32_sc1(f, ws, (unsigned)(word + (lane & 15) * FSTR) * 4u); wait_vm<0>(); pin(f);
        if (__all(f == tag)) break;
        if (spins > SPIN_CAP) { give_up(ws, lane); break; }
        __builtin_amdgcn_s_sleep(PSLEEP);
    }
}
DI void fx_load(unsigned (&w)[8], unsigned o0, unsigned o1, unsigned o2, unsigned o3, const void* wr, const void* wi, int k4) {
    asm volatile("s_cmp_lg_u32 %14, 0\n\ts_cbranch_scc1 .Lnofx%=\n\t"
                 "global_load_ushort %0, %8, %12\n\tglobal_load_ushort %1, %9, %12\n\tglobal_load_ushort %2, %10, %12\n\tglobal_load_ushort %3, %11, %12\n\t"
                 "global_load_ushort %4, %8, %13\n\tglobal_load_ushort %5, %9, %13\n\tglobal_load_ushort %6, %10, %13\n\tglobal_load_ushort %7, %11, %13\n"
                 ".Lnofx%=:" : "=&v"(w[0]), "=&v"(w[1]), "=&v"(w[2]), "=&v"(w[3]), "=&v"(w[4]), "=&v"(w[5]), "=&v"(w[6]), "=&v"(w[7])
                 : "v"(o0), "v"(o1), "v"(o2), "v"(o3), "s"(wr), "s"(wi), "s"(k4) : "memory", "scc");
}
DI float cosr(float x) { return __builtin_amdgcn_cosf(x); }
DI float sinr(float x) { return __builtin_amdgcn_sinf(x); }
DI u32x4 frag_cs(int a, int b0, int n) {
    float c[4], sn[4];
#pragma unroll
    for (int j = 0; j < 4; ++j) { const float th = (float)((a * (b0 + j)) & (n - 1)) * (1.0f / n); c[j] = cosr(th); sn[j] = -sinr(th); }
    return pack8(c, sn);
}

constexpr int P1_STAGE = 49152, P1_BOFF = 16384;
constexpr int L_MISC = 147456;
constexpr int L_XI = L_MISC;
constexpr int L_ST = L_MISC + 8704;
constexpr int L_YX = L_ST + 1024;
constexpr int L_SCR = L_YX + 4096;
constexpr int L_FLG = L_SCR + 256;
constexpr int L_END = L_FLG + 16;
static_assert(L_END <= 163840, "lds");
constexpr int LDS_BYTES = 163840;
constexpr int L_RAW = 98304;
constexpr int P3_SLOT = 16384;
DI constexpr int p3_slot(int s) { return (s % 9) * P3_SLOT; }

DI int panel_row(int r, int m) { return 64 * (m & 31) + 4 * r + (m >> 5); }
DI int p3_ks(int s, int kg) { const int n = 4 * s + kg; if (s >= 12) return 7 * (n - 48) + 3; const int p = n / 6, o = n - 6 * p; return 7 * p + o + (o >= 3); }

struct LdSrc { unsigned a[4]; const unsigned char* b[8]; long sB64; const unsigned char* t; long tadv; };
DI void p1_issue(LdSrc& s, const unsigned char* x, unsigned Lb, int lw, int j) {
    const unsigned sb = Lb + (j % 3) * P1_STAGE;
#pragma unroll
    for (int i = 0; i < 4; ++i) glds16s(x + j * 128, s.a[i], sb + (4 * lw + i) * 1024);
#pragma unroll
    for (int u = 0; u < 8; ++u) { const int i = (lw & 1) + 2 * (u & 1) + 4 * (lw >> 1) + 8 * ((u >> 1) & 1) + 16 * (u >> 2);
        glds16(s.b[u], sb + P1_BOFF + i * 1024); s.b[u] += s.sB64; }
}
DI void p1_touch(LdSrc& s, unsigned Lb) { glds4(s.t, Lb + L_SCR); s.t += s.tadv; }
DI void p3_issue(const Params& p, unsigned Lb, int lw, int cc, int s, unsigned wv, unsigned fv) {
    const unsigned sb = Lb + p3_slot(s) + lw * 4096;
    if (s < 14) { const unsigned char* base = (const unsigned char*)p.wd + (size_t)p3_ks(s, lw) * 32768;
#pragma unroll
        for (int i = 0; i < 4; ++i) glds16s(base + i * 8192, wv, sb + i * 1024); }
    else { const unsigned char* base = (const unsigned char*)&g_inv + cc * 65536 + (s - 14) * 16384 + lw * 4096;
#pragma unroll
        for (int i = 0; i < 4; ++i) glds16s(base + i * 1024, fv, sb + i * 1024); }
}

DI void p3_issue2(const Params& p, unsigned Lb, int w, int cc, int s, unsigned wv, unsigned fv) {
    const unsigned sb = Lb + p3_slot(s) + w * 2048;
    if (s < 14) { const unsigned char* base = (const unsigned char*)p.wd + (size_t)p3_ks(s, w >> 1) * 32768 + (w & 1) * 16384;
        glds16s(base, wv, sb); glds16s(base + 8192, wv, sb + 1024); }
    else { const unsigned char* base = (const unsigned char*)&g_inv + cc * 65536 + (s - 14) * 16384 + w * 2048;
        glds16s(base, fv, sb); glds16s(base + 1024, fv, sb + 1024); }
}

template <bool DFTW>
DI void p1_compute(LAS unsigned char* L, unsigned Lb, int wc, int lane, f32x4 (&acc)[4][8], f32x4 (&csm)[4]) {
    asm volatile("" : "+v"(lane));
    const int fr = lane & 15, g = lane >> 4;
    const int qq = fr >> 2, p = lane & 3;
    int aoff0 = fr * 128 + (((0 + g) ^ (fr & 7)) << 4);
    int aoff1 = fr * 128 + (((4 + g) ^ (fr & 7)) << 4);
    const int sw = 2 * qq + 8 * (g & 1);
    int boff[4];
#pragma unroll
    for (int q = 0; q < 4; ++q) boff[q] = P1_BOFF + (8 * g + qq) * 512 + (((8 * wc + 2 * q + (p >> 1)) ^ sw) << 4) + 8 * (p & 1);
    u32x4 xa[2][4], wb[2][4];
    auto rdx = [&](int ks, int mh, int i) { xa[mh][i] = lds128(L, (ks ? aoff1 : aoff0) + (4 * mh + i) * 2048); };
    auto rdw = [&](int ks, int q) { const int o = boff[q] + ks * 16384; wb[ks][q] = lds_tr2(L, o, o + 2048); };
    auto rebase = [&](int d) { aoff0 += d; aoff1 += d; boff[0] += d; boff[1] += d; boff[2] += d; boff[3] += d;
        asm volatile("" : "+v"(aoff0), "+v"(aoff1), "+v"(boff[0]), "+v"(boff[1]), "+v"(boff[2]), "+v"(boff[3])); };
    auto step = [&](int ks, int mh, bool more) {
#pragma unroll
        for (int i = 0; i < 16; ++i) {
            const int q = i >> 2, mt = i & 3;
            if (DFTW && q >= 2) acc[q][4 * mh + mt] = mfma(xa[mh][mt], wb[ks][q], acc[q][4 * mh + mt]);
            else acc[q][4 * mh + mt] = mfma(wb[ks][q], xa[mh][mt], acc[q][4 * mh + mt]);
            if (more) {
                if (mh == 0) { if (i >= 1 && i <= 4) rdx(ks, 1, i - 1); }
                else { if (i < 4) rdx(ks ^ 1, 0, i); else if (i < 8) rdw(ks ^ 1, i - 4); }
            }
            if (mh == 0 && (i & 3) == 3) { const int d = i >> 2; if (!DFTW || d < 2) csm[d] = mfma(wb[ks][d], u32x4{0x3F803F80u, 0x3F803F80u, 0x3F803F80u, 0x3F803F80u}, csm[d]); }
            SB();
        }
    };
    BAR();
#pragma unroll
    for (int i = 0; i < 4; ++i) { rdx(0, 0, i); rdw(0, i); }
    SB();
    static_for<0, 8>([&](auto J) {
        constexpr int j = J.v;
        step(0, 0, true);
        step(0, 1, true);
        step(1, 0, true);
        if constexpr (j < 7) { WAIT_LGKM0(); BAR(); rebase((j + 1) % 3 == 0 ? -2 * P1_STAGE : P1_STAGE);
            SB(); }
        step(1, 1, j < 7);
    });
}

constexpr int HD = 5;
constexpr int FTS = 10, FTC = 12;
constexpr int p3_issue_at(int o) { return o < 14 ? o - HD : FTC; }
constexpr int p3_opcnt(int o) { return (o >= 12 && o <= 14) ? 4 : 2; }
enum { K_OP, K_TST, K_W, K_FT };
constexpr int p3_after(int kind, int idx, int s_now) {
    int n = -1;
    auto hit = [&](bool is, int cnt) { if (is) n = 0; else if (n >= 0) n += cnt; };
    for (int o = 0; o < HD; ++o) hit(kind == K_OP && idx == o, 2);
    hit(kind == K_TST, 1); hit(false, 1);
    for (int s = 0; s < s_now; ++s) {
        for (int o = HD; o < 18; ++o) if (p3_issue_at(o) == s) hit(kind == K_OP && idx == o, p3_opcnt(o));
        if (s >= 3 && s + 6 < 18) hit(kind == K_W && idx == s + 6, 2);
        if (s == FTS) hit(kind == K_FT, 1);
    }
    return n;
}
constexpr int cmin(int a, int b) { return a < b ? a : b; }
constexpr int p3_wait(int s) {
    int n = p3_after(K_OP, s, s);
    if (s == 3) n = cmin(n, p3_after(K_TST, 0, s));
    if (s % 3 == 0 && s >= 9) n = cmin(n, p3_after(K_W, s + 2, s));
    if (s == FTC) n = cmin(n, p3_after(K_FT, 0, s));
    return n;
}
static_assert(p3_wait(0) == 2 * (HD - 1) + 2 && p3_wait(3) == 1 + 6, "queue model");

struct P3Ctx { unsigned hbase, h2base, tbase; int woff[4]; };
DI void p3_rdw(LAS unsigned char* L, const P3Ctx& c, int kg, int lane, int s, u32x4 (&wf)[4]) {
#pragma unroll
    for (int dt = 0; dt < 4; ++dt) {
        if (s < 14) wf[dt] = lds_tr2(L, p3_slot(s) + c.woff[dt], p3_slot(s) + c.woff[dt] + 2048);
        else wf[dt] = lds128(L, p3_slot(s) + (kg * 4 + dt) * 1024 + lane * 16);
    }
}

__global__ void __launch_bounds__(NT, 2) k_fused(Params p) {
    extern __shared__ __attribute__((aligned(16))) unsigned char smem[];
    LAS unsigned char* L = (LAS unsigned char*)smem;
    const unsigned Lb = (unsigned)(size_t)L;
    const int wid = __builtin_amdgcn_readfirstlane(threadIdx.x >> 6);
    const int wc = wid & 3;
    const int b = blockIdx.x, xcd = b & 7, bi = b >> 3;
    const int r = (xcd & 1) * 8 + (bi & 7), c = (xcd >> 1) * 4 + (bi >> 3);
    const int jq = b & 7, kq = b >> 3;
    const int cc = (xcd & 1) * 4 + (bi & 3), hp = (xcd >> 1) * 8 + (bi >> 2), r3 = hp >> 1, h3 = hp & 1;
    const unsigned char* ws = p.ws;
    const int odd = c & 1;
    const int u2 = wid >> 2, w2 = wid & 3, k4 = 2 * kq + u2;
    LAS f32x2* ST = (LAS f32x2*)(L + L_ST);
    f32x4 acc[4][8], csm[4];
    u32x4 eaC[2], eaS[2]; float tcA[2][4], tsA[2][4];
#pragma unroll
    for (int i = 0; i < 4; ++i) csm[i] = f32x4{0.f, 0.f, 0.f, 0.f};
    unsigned tag;
    {
    const int tid = threadIdx.x, lane = tid & 63;
    if (wid >= 4) {
        const int lw = wid - 4;
        unsigned epoch; gl32_sc1(epoch, ws, 0u);
        LdSrc ls;
        {
            const int idx = lw * 22 + (lane < 22 ? lane : 0);
            if (idx < 32) { ls.t = (const unsigned char*)p.x + (size_t)panel_row(r, 32 * (c & 3) + idx) * 1024; ls.tadv = 128; }
            else { const int i = idx - 32, row = 8 * (r & 7) + i / 7, it = i % 7;
                if (it == 6) { ls.t = (const unsigned char*)&g_fwd + c * 32768 + row * 64; ls.tadv = 64 * 64; }
                else { const int it3 = it % 3; ls.t = (const unsigned char*)(it < 3 ? p.wg : p.wu) + (size_t)row * (F * 2) + c * 224 + (it3 == 2 ? 220 : it3 * 128); ls.tadv = 64L * F * 2; } }
        }
        p1_touch(ls, Lb);
        SB();
#pragma unroll
        for (int i = 0; i < 4; ++i) { const int m = 8 * (4 * lw + i) + (lane >> 3); ls.a[i] = (unsigned)panel_row(r, m) * 1024u + (((lane & 7) ^ (lane >> 3)) << 4); }
        {
            const int b0 = lw & 1, b2 = lw >> 1;
            const int swz = 2 * (2 * b0 + (lane >> 5)) + 8 * b2;
            const int ch = (lane & 31) ^ swz, nt = ch >> 1, half = ch & 1, cw = nt >> 2, q = nt & 3;
            const unsigned char* base; int col, strideB;
            if (cw < 3) { const int ft = odd + 2 * cw + (q & 1); base = (const unsigned char*)(q < 2 ? p.wg : p.wu); col = 112 * c + 16 * ft + 8 * half; strideB = F * 2; }
            else if (q < 2) { const int ft = odd ? 0 : 6; base = (const unsigned char*)(q == 0 ? p.wg : p.wu); col = 112 * c + 16 * ft + 8 * half; strideB = F * 2; }
            else { base = (const unsigned char*)&g_fwd + c * 32768; col = 16 * (q - 2) + 8 * half; strideB = 64; }
            const unsigned char* b00 = base + (size_t)(2 * b0 + 8 * b2 + (lane >> 5)) * strideB + col * 2;
#pragma unroll
            for (int u = 0; u < 8; ++u) ls.b[u] = b00 + (size_t)(4 * (u & 1) + 16 * ((u >> 1) & 1) + 32 * (u >> 2)) * strideB;
            ls.sB64 = (long)strideB * 64;
        }
        p1_issue(ls, (const unsigned char*)p.x, Lb, lw, 0);
        p1_touch(ls, Lb); p1_touch(ls, Lb); p1_touch(ls, Lb);
        p1_issue(ls, (const unsigned char*)p.x, Lb, lw, 1);
        if (wid == 4 && lane < 4) *((volatile LAS unsigned*)(L + L_FLG) + lane) = 0u;
        wait_vm<15>(); pin(epoch); tag = epoch + 1u;
        if (r == 0 && tid == 256) __hip_atomic_store((gu32*)p.ws + W_CT + c * 32, tag << 8, RLX_AGENT);
        WAIT_LGKM0();
        BAR();
        p1_touch(ls, Lb);
        p1_issue(ls, (const unsigned char*)p.x, Lb, lw, 2);
        SB();
        const unsigned char *st0, *st1;
        {
            auto wline = [&](int i) { const int st = i >> 2, ln = (bi >> 2) * 16 + lw * 4 + (i & 3);
                return st < 14 ? (const unsigned char*)p.wd + (size_t)(32 * p3_ks(st, ln >> 5) + (ln & 31)) * 1024 + cc * 128
                               : (const unsigned char*)&g_inv + (size_t)cc * 65536 + (st - 14) * 16384 + ln * 128; };
            st0 = wline(lane);
            const int fi = (bi * 4 + lw) * 20 + (lane >= 8 && lane < 28 ? lane - 8 : 0);
            const int arr = fi / 1280, rr = (fi % 1280) / 5, ll = (fi % 1280) % 5;
            const int jrow = 4 * jq + (rr & 3) + 32 * (rr >> 2);
            st1 = lane < 8 ? wline(64 + lane) : (const unsigned char*)(arr ? p.wi : p.wr) + (size_t)jrow * (DF * 2) + min(ll * 128, 510);
        }
        u32x2 gran; const unsigned goff = (unsigned)WS_STAT + (unsigned)(r * 128 + (tid & 127)) * 8u;
        static_for<0, 7>([&](auto J) {
            constexpr int j = J.v;
            if constexpr (j < 4) wait_vm<13>(); else if constexpr (j == 4) wait_vm<12>();
            else if constexpr (j == 5) wait_vm<15>(); else { wait_vm<3>(); WAIT_LGKM0(); }
            BAR();
            if constexpr (j < 3) p1_touch(ls, Lb);
            if constexpr (j + 3 < 8) p1_issue(ls, (const unsigned char*)p.x, Lb, lw, j + 3);
            if constexpr (j == 4) {
                gl64_sc1(gran, ws, goff);
                glds4(st0, Lb + L_SCR); glds4(st1, Lb + L_SCR);
            }
            if constexpr (j == 5) {
                wait_vm<2>();
                pin(gran);
                if (lw < 2) {
                    if (!__all(gran.y == tag)) {
#pragma unroll 1
                        for (unsigned spins = 0;; ++spins) {
                            u32x2 g2; gl64_sc1(g2, ws, goff); wait_vm<0>(); pin(g2); gran = g2;
                            if (__all(g2.y == tag)) break;
                            if (spins > SPIN_CAP) { give_up(ws, lane); break; }
                            __builtin_amdgcn_s_sleep(1);
                        }
                    }
                    ST[tid & 127] = f32x2{bf_hi(gran.x), 1.0f / bf_lo(gran.x)};
                }
            }
            if constexpr (j == 6) {
                const unsigned wv0 = (unsigned)(lane >> 3) * 1024u + cc * 128u + (unsigned)(((lane & 7) ^ (2 * ((lane >> 4) & 3))) << 4), fv0 = lane * 16u;
#pragma unroll
                for (int st = 0; st < 3; ++st) p3_issue(p, Lb, lw, cc, st, wv0, fv0);
            }
        });
        {
            const int fr = lane & 15, g = lane >> 4;
#pragma unroll
            for (int jt = 0; jt < 2; ++jt) {
                float cv[8], sv[8];
#pragma unroll
                for (int j = 0; j < 8; ++j) { const float th = (float)(((16 * jt + fr) * (8 * g + j)) & 31) * (1.0f / 32.0f); cv[j] = cosr(th); sv[j] = sinr(th); }
                eaC[jt] = u32x4{pk_bf16(cv[0], cv[1]), pk_bf16(cv[2], cv[3]), pk_bf16(cv[4], cv[5]), pk_bf16(cv[6], cv[7])};
                eaS[jt] = u32x4{pk_bf16(sv[0], sv[1]), pk_bf16(sv[2], sv[3]), pk_bf16(sv[4], sv[5]), pk_bf16(sv[6], sv[7])};
#pragma unroll
                for (int e = 0; e < 4; ++e) { const float th = (float)((16 * jt + fr) * (4 * r + e)) * (1.0f / 2048.0f); tcA[jt][e] = cosr(th); tsA[jt][e] = sinr(th); }
            }
        }
#pragma unroll
        for (int q = 0; q < 4; ++q)
#pragma unroll
            for (int mt = 0; mt < 8; ++mt) acc[q][mt] = f32x4{0.f, 0.f, 0.f, 0.f};
    } else {
        if (wid == 0) {
            const unsigned char* pc = (const unsigned char*)__builtin_amdgcn_s_getpc();
            const int ln = bi * 10 + (lane < 10 ? lane : 0);
            glds4(pc - ((size_t)pc & 127) + (size_t)min(ln * 128, CODE_PF_BYTES), Lb + L_SCR);
        }
        if (wid == 1 || wid == 2) {
            const unsigned char* a;
            if (wid == 1) a = lane < 32 ? (const unsigned char*)p.x + (size_t)lane * 65536 : (const unsigned char*)p.wg + (size_t)min(lane - 32, 27) * 65536;
            else a = lane < 32 ? (const unsigned char*)p.wu + (size_t)min(lane, 27) * 65536 : (const unsigned char*)&g_fwd + (size_t)(lane & 7) * 65536;
            glds4(a, Lb + L_SCR);
        }
        unsigned epoch; gl32_sc1(epoch, ws, 0u);
        u32x4 xrow[2];
#pragma unroll
        for (int i = 0; i < 2; ++i) gl128(xrow[i], (const unsigned char*)p.x + (size_t)panel_row(r, 8 * c + 2 * wid + i) * 1024 + lane * 16);
        wait_vm<0>(); pin(epoch); pin(xrow[0]); pin(xrow[1]);
        tag = epoch + 1u;
#pragma unroll
        for (int i = 0; i < 2; ++i) {
            float v[8];
#pragma unroll
            for (int e = 0; e < 4; ++e) { v[2 * e] = bf_lo(xrow[i][e]); v[2 * e + 1] = bf_hi(xrow[i][e]); }
            float s = ((v[0] + v[1]) + (v[2] + v[3])) + ((v[4] + v[5]) + (v[6] + v[7]));
            s = wave_sum(s);
            const float mean = s * (1.0f / 512.0f);
            float q2 = 0.f;
#pragma unroll
            for (int e = 0; e < 8; ++e) { const float d = v[e] - mean; q2 += d * d; }
            q2 = wave_sum(q2);
            const float var = q2 * (1.0f / 512.0f);
            const float m_bf = bfr(mean), v_bf = bfr(var), s_bf = bfr(sqrtf(bfr(v_bf + 1e-5f)));
            const unsigned val = (__builtin_bit_cast(unsigned, m_bf) & 0xffff0000u) | (__builtin_bit_cast(unsigned, s_bf) >> 16);
            if (lane == 0) __hip_atomic_store((gu64*)(p.ws + WS_STAT) + (r * 128 + 8 * c + 2 * wid + i), ((unsigned long long)tag << 32) | val, RLX_AGENT);
        }
#pragma unroll
        for (int q = 0; q < 4; ++q)
#pragma unroll
            for (int mt = 0; mt < 8; ++mt) acc[q][mt] = f32x4{0.f, 0.f, 0.f, 0.f};
        if (wid < 3) p1_compute<false>(L, Lb, wc, lane, acc, csm); else p1_compute<true>(L, Lb, wc, lane, acc, csm);
        WAIT_LGKM0();
        if (lane == 0) __hip_atomic_fetch_add((LAS unsigned*)(L + L_FLG) + 3, 1u, __ATOMIC_RELAXED, __HIP_MEMORY_SCOPE_WORKGROUP);
    }
    }
    int tz = threadIdx.x; asm volatile("" : "+v"(tz));
    const int tid = tz, lane = tz & 63, fr = lane & 15, g = lane >> 4;
    const __amdgpu_buffer_rsrc_t rs = __builtin_amdgcn_make_buffer_rsrc((void*)p.ws, 0, (int)WS_END, 0x00020000);
    const unsigned wv = (unsigned)(lane >> 3) * 1024u + cc * 128u + (unsigned)(((lane & 7) ^ (2 * ((lane >> 4) & 3))) << 4), fv = lane * 16u;
    volatile LAS unsigned* flg = (volatile LAS unsigned*)(L + L_FLG);
    const int fj1 = 4 * jq + (fr & 3), fk = 4 * k4 + (fr >> 2);
    unsigned fw[4], fwi[4];
    auto signal = [&](int cnt, int word) {
        if (lane == 0) { const unsigned old = __hip_atomic_fetch_add((LAS unsigned*)(L + L_FLG) + cnt, 1u, __ATOMIC_RELAXED, __HIP_MEMORY_SCOPE_WORKGROUP);
            if (old == 3u) __hip_atomic_store((gu32*)p.ws + word, tag, RLX_AGENT); }
    };
    if (wid < 3) {
        const f32x4 cg0 = csm[0], cg1 = csm[1], cu0 = csm[2], cu1 = csm[3];
#pragma unroll
        for (int mt = 0; mt < 8; ++mt) {
            const f32x2 st = ST[16 * mt + fr];
            const float r2 = st.y * st.y;
            const f32x4 h0 = (acc[0][mt] - st.x * cg0) * (acc[2][mt] - st.x * cu0) * r2;
            const f32x4 h1 = (acc[1][mt] - st.x * cg1) * (acc[3][mt] - st.x * cu1) * r2;
            st_sc1(rs, (int)WS_H + ((((r * 8 + mt) * 48 + 3 * c + wc) * 64 + lane) << 4), pack8(h0, h1));
        }
    } else if (wid == 3) {
#pragma unroll
        for (int q = 0; q < 2; ++q)
#pragma unroll
            for (int mt = 0; mt < 8; ++mt) *(LAS f32x4*)(L + L_RAW + ((q * 8 + mt) * 64 + lane) * 16) = acc[2 + q][mt];
        WAIT_LGKM0();
        flg[0] = 1u;
        const f32x4 cg0 = csm[0], cu0 = csm[1];
#pragma unroll
        for (int mt = 0; mt < 8; ++mt) {
            const f32x2 st = ST[16 * mt + fr];
            const float r2 = st.y * st.y;
            const f32x4 h0 = (acc[0][mt] - st.x * cg0) * (acc[1][mt] - st.x * cu0) * r2;
            st_sc1_64(rs, (int)WS_H2 + ((((r * 8 + mt) * 16 + c) * 64 + lane) << 3), u32x2{pk_bf16(h0[0], h0[1]), pk_bf16(h0[2], h0[3])});
        }
    } else {
        const int mt = wid - 4;
        const int kq_ = fr >> 2, s2l = fr & 3;
#pragma unroll 1
        for (unsigned spins = 0; flg[0] == 0u && spins < (1u << 20); ++spins) __builtin_amdgcn_s_sleep(2);
        asm volatile("" ::: "memory");
        __builtin_amdgcn_s_setprio(3);
        u32x4 are, aim;
        {
            const int m0 = 32 * s2l + 8 * g, tl = 2 * s2l + (g >> 1), ln = 4 * mt + kq_ + 32 * (g & 1);
            const f32x4 r0 = *(LAS f32x4*)(L + L_RAW + (tl * 64 + ln) * 16), r1 = *(LAS f32x4*)(L + L_RAW + (tl * 64 + ln + 16) * 16);
            const f32x4 i0 = *(LAS f32x4*)(L + L_RAW + ((8 + tl) * 64 + ln) * 16), i1 = *(LAS f32x4*)(L + L_RAW + ((8 + tl) * 64 + ln + 16) * 16);
            const f32x4 sa = *(LAS f32x4*)(ST + m0), sb = *(LAS f32x4*)(ST + m0 + 2), sc = *(LAS f32x4*)(ST + m0 + 4), sd = *(LAS f32x4*)(ST + m0 + 6);
            const float csf = (c == 0 && mt == 0 && kq_ == 0) ? 512.0f : 0.0f;
            are = u32x4{pk_bf16((r0[0] - sa[0] * csf) * sa[1], (r0[1] - sa[2] * csf) * sa[3]), pk_bf16((r0[2] - sb[0] * csf) * sb[1], (r0[3] - sb[2] * csf) * sb[3]),
                        pk_bf16((r1[0] - sc[0] * csf) * sc[1], (r1[1] - sc[2] * csf) * sc[3]), pk_bf16((r1[2] - sd[0] * csf) * sd[1], (r1[3] - sd[2] * csf) * sd[3])};
            aim = u32x4{pk_bf16(i0[0] * sa[1], i0[1] * sa[3]), pk_bf16(i0[2] * sb[1], i0[3] * sb[3]), pk_bf16(i1[0] * sc[1], i1[1] * sc[3]), pk_bf16(i1[2] * sd[1], i1[3] * sd[3])};
        }
        const f32x4 z = {0.f, 0.f, 0.f, 0.f};
        const bool special = (c == 0 && mt == 0 && g == 0);
#pragma unroll
        for (int jt = 0; jt < 2; ++jt) {
            const f32x4 Prc = mfma(are, eaC[jt], z), Pis = mfma(aim, eaS[jt], z), Prs = mfma(are, eaS[jt], z), Pic = mfma(aim, eaC[jt], z);
            const int j1 = 16 * jt + fr;
            float pr[4], qi[4], pr2[4], qi2[4];
#pragma unroll
            for (int e = 0; e < 4; ++e) {
                const float cw = tcA[jt][e], sw = tsA[jt][e];
                const float ar = special ? Prc[e] : Prc[e] + Pis[e], ai = special ? -Prs[e] : Pic[e] - Prs[e];
                pr[e] = ar * cw + ai * sw; qi[e] = -(ai * cw - ar * sw);
                const float br = Pic[e], bi2 = -Pis[e];
                pr2[e] = br * cw + bi2 * sw; qi2[e] = -(bi2 * cw - br * sw);
            }
            const int U = (j1 >> 2) * 64 + 4 * c + mt, lp = 16 * (r & 3) + 4 * g + (j1 & 3);
            st_sc1(rs, (int)WS_AP + (((U * 4 + (r >> 2)) * 64 + lp) << 4), pack8(pr, qi));
            if (special) st_sc1(rs, (int)WS_AX + (((((j1 >> 2) * 4 + (r >> 2)) * 4 + (r & 3)) * 4 + (j1 & 3)) << 4), pack8(pr2, qi2));
        }
        __builtin_amdgcn_s_setprio(0);
        wait_vm<0>();
        signal(1, W_FA + (c * 16 + r) * FSTR);
    }
#pragma unroll
    for (int e = 0; e < 4; ++e) { const unsigned o = (unsigned)((fj1 + 32 * (16 * w2 + 4 * g + e)) * DF + fk) * 2u; gl16(fw[e], p.wr, o); gl16(fwi[e], p.wi, o); }
    unsigned fx[8];
    {
        const unsigned o0 = (unsigned)((fj1 + 32 * (16 * w2 + 4 * g)) * DF + 256) * 2u;
        fx_load(fx, o0, o0 + 32u * DF * 2u, o0 + 64u * DF * 2u, o0 + 96u * DF * 2u, p.wr, p.wi, k4);
    }
    if (wid < 4) {
        if (k4 == 0) wait_vm<16>(); else wait_vm<8>();
        signal(2, W_FH + (r * 16 + c) * FSTR);
#pragma unroll 1
        for (unsigned spins = 0; flg[3] < 4u && spins < (1u << 22); ++spins) __builtin_amdgcn_s_sleep(1);
        asm volatile("" ::: "memory");
#pragma unroll
        for (int st = 3; st < 6; ++st) p3_issue(p, Lb, wid, cc, st, wv, fv);
#pragma unroll 1
        for (unsigned spins = 0; flg[1] < 4u && spins < (1u << 22); ++spins) __builtin_amdgcn_s_sleep(1);
        asm volatile("" ::: "memory");
#pragma unroll
        for (int st = 6; st < 9; ++st) p3_issue(p, Lb, wid, cc, st, wv, fv);
    }
    {
        const int kg = wid & 3, rh = wid >> 2;
        u32x4 E1[4], E2[4];
#pragma unroll
        for (int t = 0; t < 4; ++t) E1[t] = frag_cs(16 * w2 + fr, 16 * t + 4 * g, 64);
#pragma unroll
        for (int i = 0; i < 4; ++i) E2[i] = frag_cs(16 * (i >> 1) + fr, 16 * (i & 1) + 4 * g, 32);
        float tc[4], tsn[4];
#pragma unroll
        for (int e = 0; e < 4; ++e) { const float th = (float)((4 * jq + e) * (16 * w2 + fr)) * (1.0f / 2048.0f); tc[e] = cosr(th); tsn[e] = sinr(th); }
        P3Ctx cx;
        {
            const int qq = fr >> 2, pp = lane & 3, row = 32 * kg + 4 * g + qq, sw = 2 * ((row >> 1) & 3);
#pragma unroll
            for (int dt = 0; dt < 4; ++dt) cx.woff[dt] = row * 128 + (((2 * dt + (pp >> 1)) ^ sw) << 4) + 8 * (pp & 1);
            cx.hbase = (unsigned)WS_H + (unsigned)(((r3 * 8 + 4 * h3 + 2 * rh) * 48 + kg) * 64 + lane) * 16u;
            cx.h2base = (unsigned)WS_H2 + (unsigned)(((r3 * 8 + 4 * h3 + 2 * rh) * 16 + 2 * kg) * 64 + lane) * 8u;
            cx.tbase = (unsigned)WS_T + (unsigned)((((4 * r3 + 2 * h3 + rh) * 16 + 4 * kg) * 2) * 64 + lane) * 16u;
        }
        SB();
        if (wid == 0) poll16(ws, W_FA + (kq >> 1) * 16 * FSTR, tag, lane);
        if (wid == 1) poll16(ws, W_FH + r3 * 16 * FSTR, tag, lane);
        BAR();
        const int U = jq * 64 + k4;
        u32x4 Bf[4], Bx[4];
        u32x4 hb[HD][2]; u32x2 hh[2][4];
#pragma unroll
        for (int t = 0; t < 4; ++t) gl128_sc1(Bf[t], ws, (unsigned)WS_AP + (unsigned)((U * 4 + t) * 64 + lane) * 16u);
        {
            const unsigned xo = (unsigned)WS_AX + (unsigned)((jq * 16 + g) * 4 + (fr & 3)) * 16u;
            asm volatile("s_cmp_lg_u32 %6, 0\n\ts_cbranch_scc1 .Lnox%=\n\t"
                         "global_load_dwordx4 %0, %4, %5 sc1\n\tglobal_load_dwordx4 %1, %4, %5 offset:256 sc1\n\t"
                         "global_load_dwordx4 %2, %4, %5 offset:512 sc1\n\tglobal_load_dwordx4 %3, %4, %5 offset:768 sc1\n"
                         ".Lnox%=:" : "=&v"(Bx[0]), "=&v"(Bx[1]), "=&v"(Bx[2]), "=&v"(Bx[3]) : "v"(xo), "s"(ws), "s"(k4) : "memory", "scc");
        }
        static_for<0, HD>([&](auto Sx) { constexpr int o = Sx.v;
            gl128_sc1(hb[o][0], ws, cx.hbase + o * 4096u); gl128_sc1(hb[o][1], ws, cx.hbase + 48u * 1024u + o * 4096u); });
        wait_vm<2 * HD>();
        pin(Bf[0]); pin(Bf[1]); pin(Bf[2]); pin(Bf[3]); pin(Bx[0]); pin(Bx[1]); pin(Bx[2]); pin(Bx[3]);
        pin(fw[0]); pin(fw[1]); pin(fw[2]); pin(fw[3]); pin(fwi[0]); pin(fwi[1]); pin(fwi[2]); pin(fwi[3]);
        pin(fx[0]); pin(fx[1]); pin(fx[2]); pin(fx[3]); pin(fx[4]); pin(fx[5]); pin(fx[6]); pin(fx[7]);
        {
            f32x4 a1 = {0.f, 0.f, 0.f, 0.f}, a2 = a1;
#pragma unroll
            for (int t = 0; t < 4; ++t) { a1 = mfma(E1[t], Bf[t], a1); a2 = mfma(E1[t], swap_neg(Bf[t]), a2); }
            constexpr float SC = 2.0f / (2048.0f * 512.0f);
            const float sc = (k4 == 0 && (fr >> 2) == 0) ? 0.5f * SC : SC;
            float yr[4], yi[4];
#pragma unroll
            for (int e = 0; e < 4; ++e) { const float xr = a1[e], xi = -a2[e], wre = bf_lo(fw[e]), wim = bf_lo(fwi[e]);
                yr[e] = (xr * wre - xi * wim) * sc; yi[e] = (xr * wim + xi * wre) * sc; }
            if (k4 == 0) {
                f32x4 n1 = {0.f, 0.f, 0.f, 0.f}, n2 = n1;
#pragma unroll
                for (int t = 0; t < 4; ++t) { n1 = mfma(E1[t], Bx[t], n1); n2 = mfma(E1[t], swap_neg(Bx[t]), n2); }
                float xr2[4], xi2[4];
#pragma unroll
                for (int e = 0; e < 4; ++e) { const float xr = n1[e], xi = -n2[e], wre = bf_lo(fx[e]), wim = bf_lo(fx[4 + e]);
                    xr2[e] = (xr * wre - xi * wim) * (0.5f * SC); xi2[e] = (xr * wim + xi * wre) * (0.5f * SC); }
                *(LAS u32x4*)(L + L_YX + (w2 * 64 + lane) * 16) = pack8(xr2, xi2);
            }
            *(LAS u32x4*)(L + L_XI + ((u2 * 4 + w2) * 64 + lane) * 16) = pack8(yr, yi);
        }
        WAIT_LGKM0(); BAR();
        u32x4 wf[2][4];
        p3_rdw(L, cx, kg, lane, 0, wf[0]);
        {
            f32x4 t1 = {0.f, 0.f, 0.f, 0.f}, t2 = t1;
#pragma unroll
            for (int t = 0; t < 4; ++t) { const u32x4 y = lds128(L, L_XI + ((u2 * 4 + t) * 64 + lane) * 16);
                t1 = mfma(y, E1[t], t1); t2 = mfma(swap_neg(y), E1[t], t2); }
            const int s2 = 16 * w2 + fr, ko = 4 * k4 + g;
            float tr[4], ti[4];
#pragma unroll
            for (int e = 0; e < 4; ++e) { tr[e] = t1[e] * tc[e] - t2[e] * tsn[e]; ti[e] = t2[e] * tc[e] + t1[e] * tsn[e]; }
            st_sc1(rs, (int)WS_T + ((((s2 * 16 + (ko >> 4)) * 2 + (jq >> 2)) * 64 + (jq & 3) * 16 + (ko & 15)) << 4), pack8(tr, ti));
            if (k4 == 0) {
                f32x4 x1 = {0.f, 0.f, 0.f, 0.f}, x2 = x1;
#pragma unroll
                for (int t = 0; t < 4; ++t) { const u32x4 y = lds128(L, L_YX + (t * 64 + lane) * 16);
                    x1 = mfma(y, E1[t], x1); x2 = mfma(swap_neg(y), E1[t], x2); }
#pragma unroll
                for (int e = 0; e < 4; ++e) { tr[e] = x1[e] * tc[e] - x2[e] * tsn[e]; ti[e] = x2[e] * tc[e] + x1[e] * tsn[e]; }
                if (g == 0) st_sc1(rs, (int)WS_TX + ((s2 * 8 + jq) << 4), pack8(tr, ti));
            }
        }
        u32x4 xres; gl128(xres, (const unsigned char*)p.x + ((size_t)panel_row(r3, 64 * h3 + (tid >> 3)) * 512 + 64 * cc + 8 * (tid & 7)) * 2);
        f32x4 acc3[4][2];
#pragma unroll
        for (int dt = 0; dt < 4; ++dt) { acc3[dt][0] = f32x4{0.f, 0.f, 0.f, 0.f}; acc3[dt][1] = acc3[dt][0]; }
        unsigned ftf; u32x4 tx[2];
        static_for<0, 14>([&](auto Sx) {
            constexpr int s = Sx.v;
            wait_vm<p3_wait(s)>();
            if constexpr (s == 12 || s == 13) { pin(hh[s - 12][0]); pin(hh[s - 12][1]); pin(hh[s - 12][2]); pin(hh[s - 12][3]); }
            else { pin(hb[s % HD][0]); pin(hb[s % HD][1]); }
            if constexpr (s == FTC) {
                pin(ftf);
                if (wid == 0) {
                    if (!__all(ftf == (tag << 8) + 16u)) {
#pragma unroll 1
                        for (unsigned spins = 0;; ++spins) {
                            __builtin_amdgcn_s_sleep(PSLEEP);
                            unsigned f2; gl32_sc1(f2, ws, (unsigned)(W_CT + (lane & 15) * 32) * 4u); wait_vm<0>(); pin(f2);
                            if (__all(f2 == (tag << 8) + 16u)) break;
                            if (spins > SPIN_CAP) { give_up(ws, lane); break; }
                        }
                    }
                    if (b == 0 && lane == 0) __hip_atomic_store((gu32*)p.ws, tag, RLX_AGENT);
                }
            }
            if constexpr (s % 3 == 0 && s > 0) {
                WAIT_LGKM0(); BAR();
                if constexpr (s == 3) { if (tid == 0) __hip_atomic_fetch_add((gu32*)p.ws + W_CT + (kq >> 1) * 32, 1u, RLX_AGENT); }
                p3_rdw(L, cx, kg, lane, s, wf[s & 1]);
            }
            if constexpr (s + 1 < 14 && (s + 1) % 3 != 0) p3_rdw(L, cx, kg, lane, s + 1, wf[(s + 1) & 1]);
            u32x4 b0, b1;
            if constexpr (s == 12 || s == 13) { b0 = u32x4{hh[s - 12][0].x, hh[s - 12][0].y, hh[s - 12][1].x, hh[s - 12][1].y}; b1 = u32x4{hh[s - 12][2].x, hh[s - 12][2].y, hh[s - 12][3].x, hh[s - 12][3].y}; }
            else { b0 = hb[s % HD][0]; b1 = hb[s % HD][1]; }
#pragma unroll
            for (int dt = 0; dt < 4; ++dt) { acc3[dt][0] = mfma(wf[s & 1][dt], b0, acc3[dt][0]); acc3[dt][1] = mfma(wf[s & 1][dt], b1, acc3[dt][1]); }
            SB();
            static_for<HD, 18>([&](auto Ox) {
                constexpr int o = Ox.v;
                if constexpr (p3_issue_at(o) == s) {
                    if constexpr (o < 12) { gl128_sc1(hb[o % HD][0], ws, cx.hbase + o * 4096u); gl128_sc1(hb[o % HD][1], ws, cx.hbase + 48u * 1024u + o * 4096u); }
                    else if constexpr (o < 14) {
                        gl64_sc1(hh[o - 12][0], ws, cx.h2base + (o - 12) * 4096u); gl64_sc1(hh[o - 12][1], ws, cx.h2base + (o - 12) * 4096u + 512u);
                        gl64_sc1(hh[o - 12][2], ws, cx.h2base + 8192u + (o - 12) * 4096u); gl64_sc1(hh[o - 12][3], ws, cx.h2base + 8192u + (o - 12) * 4096u + 512u); }
                    else { gl128_sc1(hb[o % HD][0], ws, cx.tbase + (o - 14) * 2048u); gl128_sc1(hb[o % HD][1], ws, cx.tbase + (o - 14) * 2048u + 1024u);
                        if constexpr (o == 14) { const unsigned txo = (unsigned)WS_TX + (unsigned)((4 * r3 + 2 * h3 + rh) * 8 + g) * 16u; gl128_sc1(tx[0], ws, txo); gl128_sc1(tx[1], ws, txo + 64u); } }
                }
            });
            if constexpr (s >= 3 && s + 6 < 18) p3_issue2(p, Lb, wid, cc, s + 6, wv, fv);
            if constexpr (s == FTS) gl32_sc1(ftf, ws, wid == 0 ? (unsigned)(W_CT + (lane & 15) * 32) * 4u : cx.hbase - lane * 16u);
            SB();
        });
        wait_vm<0>();
        static_for<14, 18>([&](auto Ox) { pin(hb[Ox.v % HD][0]); pin(hb[Ox.v % HD][1]); });
        pin(tx[0]); pin(tx[1]); pin(xres);
        WAIT_LGKM0(); BAR();
        {
            u32x4 wz[2][4];
            p3_rdw(L, cx, kg, lane, 14, wz[0]); p3_rdw(L, cx, kg, lane, 15, wz[1]);
            u32x4 zf[4][2];
            static_for<0, 4>([&](auto Zx) {
                constexpr int z = Zx.v;
                const u32x4 T0 = hb[(14 + z) % HD][0], T1 = hb[(14 + z) % HD][1];
                u32x4 T0s = swap_neg(T0), T1s = swap_neg(T1);
                if constexpr (z == 0) {
                    if (kg == 0 && fr == 0) { T0s = tx[0]; T1s = tx[1]; }
                }
                f32x4 zr[2], zi[2];
#pragma unroll
                for (int st = 0; st < 2; ++st) { const f32x4 z0 = {0.f, 0.f, 0.f, 0.f}; zr[st] = mfma(T0, E2[st * 2 + 0], z0); zi[st] = mfma(T0s, E2[st * 2 + 0], z0); }
#pragma unroll
                for (int st = 0; st < 2; ++st) { zr[st] = mfma(T1, E2[st * 2 + 1], zr[st]); zi[st] = mfma(T1s, E2[st * 2 + 1], zi[st]); }
#pragma unroll
                for (int st = 0; st < 2; ++st) zf[z][st] = pack8(zr[st], zi[st]);
            });
#pragma unroll
            for (int z = 0; z < 2; ++z)
#pragma unroll
                for (int dt = 0; dt < 4; ++dt) { acc3[dt][0] = mfma(wz[z][dt], zf[z][0], acc3[dt][0]); acc3[dt][1] = mfma(wz[z][dt], zf[z][1], acc3[dt][1]); }
            p3_rdw(L, cx, kg, lane, 16, wz[0]); p3_rdw(L, cx, kg, lane, 17, wz[1]);
#pragma unroll
            for (int z = 0; z < 2; ++z)
#pragma unroll
                for (int dt = 0; dt < 4; ++dt) { acc3[dt][0] = mfma(wz[z][dt], zf[2 + z][0], acc3[dt][0]); acc3[dt][1] = mfma(wz[z][dt], zf[2 + z][1], acc3[dt][1]); }
        }
        WAIT_LGKM0(); BAR();
        constexpr int RP = 272;
#pragma unroll
        for (int dt = 0; dt < 4; ++dt)
#pragma unroll
            for (int st = 0; st < 2; ++st)
                *(LAS f32x4*)(L + (kg * 64 + 32 * rh + 16 * st + fr) * RP + (16 * dt + 4 * g) * 4) = acc3[dt][st];
        WAIT_LGKM0(); BAR();
        const int mm = tid >> 3, cg = tid & 7;
        f32x4 v0 = {0.f, 0.f, 0.f, 0.f}, v1 = v0;
#pragma unroll
        for (int q = 0; q < 4; ++q) { v0 += *(LAS f32x4*)(L + (q * 64 + mm) * RP + cg * 32); v1 += *(LAS f32x4*)(L + (q * 64 + mm) * RP + cg * 32 + 16); }
        const size_t o = (size_t)panel_row(r3, 64 * h3 + mm) * 512 + 64 * cc + 8 * cg;
        {
            const u32x4 ov = u32x4{pk_bf16(bf_lo(xres.x) + v0[0], bf_hi(xres.x) + v0[1]), pk_bf16(bf_lo(xres.y) + v0[2], bf_hi(xres.y) + v0[3]),
                                   pk_bf16(bf_lo(xres.z) + v1[0], bf_hi(xres.z) + v1[1]), pk_bf16(bf_lo(xres.w) + v1[2], bf_hi(xres.w) + v1[3])};
            const __amdgpu_buffer_rsrc_t ro = __builtin_amdgcn_make_buffer_rsrc((void*)p.out, 0, S * D * 2, 0x00020000);
            __builtin_amdgcn_raw_buffer_store_b128(ov, ro, (int)(o * 2), 0, 16);
        }
    }
}

extern "C" void kernel_launch(void* const* d_in, const int* in_sizes, int n_in, void* d_out, int out_size, void* d_ws, size_t ws_size,
                              hipStream_t stream) {
    Params p;
    p.x = (const uint16_t*)d_in[0]; p.wr = (const uint16_t*)d_in[1]; p.wi = (const uint16_t*)d_in[2];
    p.wg = (const uint16_t*)d_in[4]; p.wu = (const uint16_t*)d_in[5]; p.wd = (const uint16_t*)d_in[6];
    p.out = (uint16_t*)d_out; p.ws = (unsigned char*)d_ws;
    (void)hipFuncSetAttribute((const void*)k_fused, hipFuncAttributeMaxDynamicSharedMemorySize, LDS_BYTES);
    k_fused<<<256, NT, LDS_BYTES, stream>>>(p);
}
```

```cpp
#include <hip/hip_runtime.h>
#include <stdint.h>

#define LAS __attribute__((address_space(3)))
#define GAS __attribute__((address_space(1)))
#define DI __device__ __forceinline__
typedef __bf16 bf8 __attribute__((ext_vector_type(8)));
typedef __bf16 bf2 __attribute__((ext_vector_type(2)));
typedef short s16x4 __attribute__((ext_vector_type(4)));
typedef float f32x4 __attribute__((ext_vector_type(4)));
typedef float f32x2 __attribute__((ext_vector_type(2)));
typedef unsigned u32x4 __attribute__((ext_vector_type(4)));
typedef unsigned u32x2 __attribute__((ext_vector_type(2)));
typedef GAS unsigned gu32;
typedef GAS unsigned long long gu64;
#define RLX_AGENT __ATOMIC_RELAXED, __HIP_MEMORY_SCOPE_AGENT

namespace tabs {
constexpr double PI = 3.14159265358979323846264338327950288;
constexpr double cos_q(double x) {
    double x2 = x * x, t = 1.0, s = 1.0;
    for (int i = 1; i <= 14; ++i) { t *= -x2 / ((2 * i - 1) * (2 * i)); s += t; }
    return s;
}
constexpr uint16_t f2bf(float f) {
    uint32_t u = __builtin_bit_cast(uint32_t, f);
    u += 0x7FFFu + ((u >> 16) & 1u);
    return (uint16_t)(u >> 16);
}
struct Base { uint16_t v[512];
    constexpr Base() : v{} {
        for (int m = 0; m <= 128; ++m) {
            double c = (m == 128) ? 0.0 : cos_q(2.0 * PI * m / 512.0);
            uint16_t b = f2bf((float)c);
            uint16_t nb = (m == 128) ? b : (uint16_t)(b ^ 0x8000u);
            v[m] = b; v[(512 - m) & 511] = b;
            v[256 - m] = nb; v[(256 + m) & 511] = nb;
        }
    }
};
constexpr Base base{};
struct Chunk { uint16_t v[16384]; };
constexpr Chunk mk_fwd(int c) {
    Chunk r{};
    for (int d = 0; d < 512; ++d)
        for (int n = 0; n < 32; ++n) { const int k = 16 * c + (n & 15);
            r.v[d * 32 + n] = base.v[((n >> 4) ? (k == 0 ? d * 256 : d * k + 128) : d * k) & 511]; }
    return r;
}
constexpr Chunk mk_inv(int blk) {
    Chunk r{};
    const int cc = blk >> 1;
    for (int e = 0; e < 16384; e += 8) {
        const int l = (e >> 3) & 63, dt = (e >> 9) & 3, kg = (e >> 11) & 3, s = ((blk & 1) << 1) + (e >> 13);
        const int kt = 4 * kg + s, d = 64 * cc + 16 * dt + (l & 15), k0 = 16 * kt + 4 * (l >> 4);
        for (int j = 0; j < 4; ++j) { const int k = k0 + j;
            r.v[e + j] = base.v[(k * d) & 511];
            r.v[e + 4 + j] = base.v[(k == 0 ? d * 256 : k * d + 128) & 511]; }
    }
    return r;
}
template <int I> constexpr Chunk fwd_c = mk_fwd(I);
template <int I> constexpr Chunk inv_c = mk_inv(I);
struct Big { Chunk c[16]; };
}
#define C16(N) {{tabs::N<0>, tabs::N<1>, tabs::N<2>, tabs::N<3>, tabs::N<4>, tabs::N<5>, tabs::N<6>, tabs::N<7>, \
                 tabs::N<8>, tabs::N<9>, tabs::N<10>, tabs::N<11>, tabs::N<12>, tabs::N<13>, tabs::N<14>, tabs::N<15>}}
alignas(256) static __device__ const tabs::Big g_fwd = C16(fwd_c);
alignas(256) static __device__ const tabs::Big g_inv = C16(inv_c);

constexpr int S = 2048, D = 512, F = 1792, DF = 257, NT = 512;
constexpr size_t WS_STAT = 98304;
constexpr size_t WS_AP = 131072;
constexpr size_t WS_AX = WS_AP + (2u << 20);
constexpr size_t WS_TX = WS_AX + 8192;
constexpr size_t WS_T = WS_AX + 65536;
constexpr size_t WS_H = WS_T + (2u << 20);
constexpr size_t WS_H2 = WS_H + (size_t)16 * 8 * 48 * 1024;
constexpr size_t WS_END = WS_H2 + (size_t)16 * 8 * 16 * 512;
constexpr int FSTR = 32;
constexpr int PSLEEP = 8;
constexpr int W_CT = 1024, W_TMO = 2048, W_FA = 4096, W_FH = W_FA + 256 * 32;
constexpr unsigned SPIN_CAP = 1u << 16;
constexpr int CODE_PF_BYTES = 37 * 1024;

struct Params {
    const uint16_t* x; const uint16_t* wr; const uint16_t* wi;
    const uint16_t* wg; const uint16_t* wu; const uint16_t* wd;
    uint16_t* out; unsigned char* ws;
};

template <int I> struct IC { static constexpr int v = I; };
template <int B, int E, class Fn> DI void static_for(Fn&& f) { if constexpr (B < E) { f(IC<B>{}); static_for<B + 1, E>(f); } }
DI f32x4 mfma(u32x4 a, u32x4 b, f32x4 c) {
    return __builtin_amdgcn_mfma_f32_16x16x32_bf16(__builtin_bit_cast(bf8, a), __builtin_bit_cast(bf8, b), c, 0, 0, 0);
}
DI unsigned pk_bf16(float lo, float hi) { bf2 p; p[0] = (__bf16)lo; p[1] = (__bf16)hi; return __builtin_bit_cast(unsigned, p); }
DI float bf_lo(unsigned u) { return __builtin_bit_cast(float, u << 16); }
DI float bf_hi(unsigned u) { return __builtin_bit_cast(float, u & 0xffff0000u); }
DI float bfr(float x) { return bf_lo(pk_bf16(x, x)); }
DI u32x4 lds128(LAS unsigned char* L, int off) { return *(LAS u32x4*)(L + off); }
DI u32x2 lds_tr(LAS unsigned char* L, int off) {
    return __builtin_bit_cast(u32x2, __builtin_amdgcn_ds_read_tr16_b64_v4i16((LAS s16x4*)(L + off)));
}
DI u32x4 lds_tr2(LAS unsigned char* L, int off0, int off1) {
    const u32x2 a = lds_tr(L, off0), b = lds_tr(L, off1);
    return u32x4{a.x, a.y, b.x, b.y};
}
DI u32x4 swap_neg(u32x4 v) { return u32x4{v.z, v.w, v.x ^ 0x80008000u, v.y ^ 0x80008000u}; }
DI u32x4 pack8(const float (&a)[4], const float (&b)[4]) { return u32x4{pk_bf16(a[0], a[1]), pk_bf16(a[2], a[3]), pk_bf16(b[0], b[1]), pk_bf16(b[2], b[3])}; }
DI u32x4 pack8(f32x4 a, f32x4 b) { return u32x4{pk_bf16(a[0], a[1]), pk_bf16(a[2], a[3]), pk_bf16(b[0], b[1]), pk_bf16(b[2], b[3])}; }
DI void glds16(const void* gsrc, unsigned lds_addr) {
    asm volatile("s_mov_b32 m0, %1\n\ts_nop 0\n\tglobal_load_lds_dwordx4 %0, off" :: "v"(gsrc), "s"(lds_addr) : "memory");
}
DI void glds16s(const void* sbase, unsigned voff, unsigned lds_addr) {
    asm volatile("s_mov_b32 m0, %2\n\ts_nop 0\n\tglobal_load_lds_dwordx4 %0, %1" :: "v"(voff), "s"(sbase), "s"(lds_addr) : "memory");
}
DI void glds4(const void* gsrc, unsigned lds_addr) {
    asm volatile("s_mov_b32 m0, %1\n\ts_nop 0\n\tglobal_load_lds_dword %0, off" :: "v"(gsrc), "s"(lds_addr) : "memory");
}
DI void gl128(u32x4& d, const void* p) { asm volatile("global_load_dwordx4 %0, %1, off" : "=v"(d) : "v"(p) : "memory"); }
DI void gl128_sc1(u32x4& d, const void* base, unsigned off) { asm volatile("global_load_dwordx4 %0, %1, %2 sc1" : "=v"(d) : "v"(off), "s"(base) : "memory"); }
DI void gl64_sc1(u32x2& d, const void* base, unsigned off) { asm volatile("global_load_dwordx2 %0, %1, %2 sc1" : "=v"(d) : "v"(off), "s"(base) : "memory"); }
DI void gl32_sc1(unsigned& d, const void* base, unsigned off) { asm volatile("global_load_dword %0, %1, %2 sc1" : "=v"(d) : "v"(off), "s"(base) : "memory"); }
DI void gl16(unsigned& d, const void* base, unsigned off) { asm volatile("global_load_ushort %0, %1, %2" : "=v"(d) : "v"(off), "s"(base) : "memory"); }
template <int N> DI void wait_vm() { static_assert(N >= 0 && N < 64, "vmcnt"); asm volatile("s_waitcnt vmcnt(%0)" :: "n"(N) : "memory"); }
template <class T> DI void pin(T& v) { asm volatile("" : "+v"(v)); }
#define WAIT_LGKM0() asm volatile("s_waitcnt lgkmcnt(0)" ::: "memory")
#define BAR() do { asm volatile("" ::: "memory"); __builtin_amdgcn_s_barrier(); asm volatile("" ::: "memory"); } while (0)
#define SB() __builtin_amdgcn_sched_barrier(0)
DI void st_sc1(__amdgpu_buffer_rsrc_t rs, int off, u32x4 v) { __builtin_amdgcn_raw_buffer_store_b128(v, rs, off, 0, 16); }
DI void st_sc1_64(__amdgpu_buffer_rsrc_t rs, int off, u32x2 v) { __builtin_amdgcn_raw_buffer_store_b64(v, rs, off, 0, 16); }
template <int CTRL> DI float dpp_add(float v) {
    return v + __builtin_bit_cast(float, __builtin_amdgcn_update_dpp(0, __builtin_bit_cast(int, v), CTRL, 0xf, 0xf, true));
}
DI float wave_sum(float v) {
    v = dpp_add<0xB1>(v);
    v = dpp_add<0x4E>(v);
    v = dpp_add<0x141>(v);
    v = dpp_add<0x140>(v);
    const int vi = __builtin_bit_cast(int, v);
    return (__builtin_bit_cast(float, __builtin_amdgcn_readlane(vi, 0)) + __builtin_bit_cast(float, __builtin_amdgcn_readlane(vi, 16))) +
           (__builtin_bit_cast(float, __builtin_amdgcn_readlane(vi, 32)) + __builtin_bit_cast(float, __builtin_amdgcn_readlane(vi, 48)));
}
DI void give_up(const unsigned char* ws, int lane) { if (lane == 0) __hip_atomic_store((gu32*)ws + W_TMO, 1u, RLX_AGENT); }
DI void poll16(const unsigned char* ws, int word, unsigned tag, int lane) {
#pragma unroll 1
    for (unsigned spins = 0;; ++spins) {
        unsigned f; gl32_sc1(f, ws, (unsigned)(word + (lane & 15) * FSTR) * 4u); wait_vm<0>(); pin(f);
        if (__all(f == tag)) break;
        if (spins > SPIN_CAP) { give_up(ws, lane); break; }
        __builtin_amdgcn_s_sleep(PSLEEP);
    }
}
DI void fx_load(unsigned (&w)[8], unsigned o0, unsigned o1, unsigned o2, unsigned o3, const void* wr, const void* wi, int k4) {
    asm volatile("s_cmp_lg_u32 %14, 0\n\ts_cbranch_scc1 .Lnofx%=\n\t"
                 "global_load_ushort %0, %8, %12\n\tglobal_load_ushort %1, %9, %12\n\tglobal_load_ushort %2, %10, %12\n\tglobal_load_ushort %3, %11, %12\n\t"
                 "global_load_ushort %4, %8, %13\n\tglobal_load_ushort %5, %9, %13\n\tglobal_load_ushort %6, %10, %13\n\tglobal_load_ushort %7, %11, %13\n"
                 ".Lnofx%=:" : "=&v"(w[0]), "=&v"(w[1]), "=&v"(w[2]), "=&v"(w[3]), "=&v"(w[4]), "=&v"(w[5]), "=&v"(w[6]), "=&v"(w[7])
                 : "v"(o0), "v"(o1), "v"(o2), "v"(o3), "s"(wr), "s"(wi), "s"(k4) : "memory", "scc");
}
DI float cosr(float x) { return __builtin_amdgcn_cosf(x); }
DI float sinr(float x) { return __builtin_amdgcn_sinf(x); }
DI u32x4 frag_cs(int a, int b0, int n) {
    float c[4], sn[4];
#pragma unroll
    for (int j = 0; j < 4; ++j) { const float th = (float)((a * (b0 + j)) & (n - 1)) * (1.0f / n); c[j] = cosr(th); sn[j] = -sinr(th); }
    return pack8(c, sn);
}

constexpr int P1_STAGE = 49152, P1_BOFF = 16384;
constexpr int L_MISC = 147456;
constexpr int L_XI = L_MISC;
constexpr int L_ST = L_MISC + 8704;
constexpr int L_YX = L_ST + 1024;
constexpr int L_SCR = L_YX + 4096;
constexpr int L_FLG = L_SCR + 256;
constexpr int L_END = L_FLG + 16;
static_assert(L_END <= 163840, "lds");
constexpr int LDS_BYTES = 163840;
constexpr int L_RAW = 98304;
constexpr int P3_SLOT = 16384;
DI constexpr int p3_slot(int s) { return (s % 9) * P3_SLOT; }

DI int panel_row(int r, int m) { return 64 * (m & 31) + 4 * r + (m >> 5); }
DI int p3_ks(int s, int kg) { const int n = 4 * s + kg; if (s >= 12) return 7 * (n - 48) + 3; const int p = n / 6, o = n - 6 * p; return 7 * p + o + (o >= 3); }

struct LdSrc { unsigned a[4]; const unsigned char* b[8]; long sB64; const unsigned char* t; long tadv; };
DI void p1_issue(LdSrc& s, const unsigned char* x, unsigned Lb, int lw, int j) {
    const unsigned sb = Lb + (j % 3) * P1_STAGE;
#pragma unroll
    for (int i = 0; i < 4; ++i) glds16s(x + j * 128, s.a[i], sb + (4 * lw + i) * 1024);
#pragma unroll
    for (int u = 0; u < 8; ++u) { const int i = (lw & 1) + 2 * (u & 1) + 4 * (lw >> 1) + 8 * ((u >> 1) & 1) + 16 * (u >> 2);
        glds16(s.b[u], sb + P1_BOFF + i * 1024); s.b[u] += s.sB64; }
}
DI void p1_touch(LdSrc& s, unsigned Lb) { glds4(s.t, Lb + L_SCR); s.t += s.tadv; }
DI void p3_issue(const Params& p, unsigned Lb, int lw, int cc, int s, unsigned wv, unsigned fv) {
    const unsigned sb = Lb + p3_slot(s) + lw * 4096;
    if (s < 14) { const unsigned char* base = (const unsigned char*)p.wd + (size_t)p3_ks(s, lw) * 32768;
#pragma unroll
        for (int i = 0; i < 4; ++i) glds16s(base + i * 8192, wv, sb + i * 1024); }
    else { const unsigned char* base = (const unsigned char*)&g_inv + cc * 65536 + (s - 14) * 16384 + lw * 4096;
#pragma unroll
        for (int i = 0; i < 4; ++i) glds16s(base + i * 1024, fv, sb + i * 1024); }
}

DI void p3_issue2(const Params& p, unsigned Lb, int w, int cc, int s, unsigned wv, unsigned fv) {
    const unsigned sb = Lb + p3_slot(s) + w * 2048;
    if (s < 14) { const unsigned char* base = (const unsigned char*)p.wd + (size_t)p3_ks(s, w >> 1) * 32768 + (w & 1) * 16384;
        glds16s(base, wv, sb); glds16s(base + 8192, wv, sb + 1024); }
    else { const unsigned char* base = (const unsigned char*)&g_inv + cc * 65536 + (s - 14) * 16384 + w * 2048;
        glds16s(base, fv, sb); glds16s(base + 1024, fv, sb + 1024); }
}

template <bool DFTW>
DI void p1_compute(LAS unsigned char* L, unsigned Lb, int wc, int lane, f32x4 (&acc)[4][8], f32x4 (&csm)[4]) {
    asm volatile("" : "+v"(lane));
    const int fr = lane & 15, g = lane >> 4;
    const int qq = fr >> 2, p = lane & 3;
    int aoff0 = fr * 128 + (((0 + g) ^ (fr & 7)) << 4);
    int aoff1 = fr * 128 + (((4 + g) ^ (fr & 7)) << 4);
    const int sw = 2 * qq + 8 * (g & 1);
    int boff[4];
#pragma unroll
    for (int q = 0; q < 4; ++q) boff[q] = P1_BOFF + (8 * g + qq) * 512 + (((8 * wc + 2 * q + (p >> 1)) ^ sw) << 4) + 8 * (p & 1);
    u32x4 xa[2][4], wb[2][4];
    auto rdx = [&](int ks, int mh, int i) { xa[mh][i] = lds128(L, (ks ? aoff1 : aoff0) + (4 * mh + i) * 2048); };
    auto rdw = [&](int ks, int q) { const int o = boff[q] + ks * 16384; wb[ks][q] = lds_tr2(L, o, o + 2048); };
    auto rebase = [&](int d) { aoff0 += d; aoff1 += d; boff[0] += d; boff[1] += d; boff[2] += d; boff[3] += d;
        asm volatile("" : "+v"(aoff0), "+v"(aoff1), "+v"(boff[0]), "+v"(boff[1]), "+v"(boff[2]), "+v"(boff[3])); };
    auto step = [&](int ks, int mh, bool more) {
#pragma unroll
        for (int i = 0; i < 16; ++i) {
            const int q = i >> 2, mt = i & 3;
            if (DFTW && q >= 2) acc[q][4 * mh + mt] = mfma(xa[mh][mt], wb[ks][q], acc[q][4 * mh + mt]);
            else acc[q][4 * mh + mt] = mfma(wb[ks][q], xa[mh][mt], acc[q][4 * mh + mt]);
            if (more) {
                if (mh == 0) { if (i >= 1 && i <= 4) rdx(ks, 1, i - 1); }
                else { if (i < 4) rdx(ks ^ 1, 0, i); else if (i < 8) rdw(ks ^ 1, i - 4); }
            }
            if (mh == 0 && (i & 3) == 3) { const int d = i >> 2; if (!DFTW || d < 2) csm[d] = mfma(wb[ks][d], u32x4{0x3F803F80u, 0x3F803F80u, 0x3F803F80u, 0x3F803F80u}, csm[d]); }
            SB();
        }
    };
    BAR();
#pragma unroll
    for (int i = 0; i < 4; ++i) { rdx(0, 0, i); rdw(0, i); }
    SB();
    static_for<0, 8>([&](auto J) {
        constexpr int j = J.v;
        step(0, 0, true);
        step(0, 1, true);
        step(1, 0, true);
        if constexpr (j < 7) { WAIT_LGKM0(); BAR(); rebase((j + 1) % 3 == 0 ? -2 * P1_STAGE : P1_STAGE);
            SB(); }
        step(1, 1, j < 7);
    });
}

constexpr int HD = 5;
constexpr int FTS = 10, FTC = 12;
constexpr int p3_issue_at(int o) { return o < 14 ? o - HD : FTC; }
constexpr int p3_opcnt(int o) { return (o >= 12 && o <= 14) ? 4 : 2; }
enum { K_OP, K_TST, K_W, K_FT };
constexpr int p3_after(int kind, int idx, int s_now) {
    int n = -1;
    auto hit = [&](bool is, int cnt) { if (is) n = 0; else if (n >= 0) n += cnt; };
    for (int o = 0; o < HD; ++o) hit(kind == K_OP && idx == o, 2);
    hit(kind == K_TST, 1); hit(false, 1);
    for (int s = 0; s < s_now; ++s) {
        for (int o = HD; o < 18; ++o) if (p3_issue_at(o) == s) hit(kind == K_OP && idx == o, p3_opcnt(o));
        if (s >= 3 && s + 6 < 18) hit(kind == K_W && idx == s + 6, 2);
        if (s == FTS) hit(kind == K_FT, 1);
    }
    return n;
}
constexpr int cmin(int a, int b) { return a < b ? a : b; }
constexpr int p3_wait(int s) {
    int n = p3_after(K_OP, s, s);
    if (s == 3) n = cmin(n, p3_after(K_TST, 0, s));
    if (s % 3 == 0 && s >= 9) n = cmin(n, p3_after(K_W, s + 2, s));
    if (s == FTC) n = cmin(n, p3_after(K_FT, 0, s));
    return n;
}
static_assert(p3_wait(0) == 2 * (HD - 1) + 2 && p3_wait(3) == 1 + 6, "queue model");

struct P3Ctx { unsigned hbase, h2base, tbase; int woff[4]; };
DI void p3_rdw(LAS unsigned char* L, const P3Ctx& c, int kg, int lane, int s, u32x4 (&wf)[4]) {
#pragma unroll
    for (int dt = 0; dt < 4; ++dt) {
        if (s < 14) wf[dt] = lds_tr2(L, p3_slot(s) + c.woff[dt], p3_slot(s) + c.woff[dt] + 2048);
        else wf[dt] = lds128(L, p3_slot(s) + (kg * 4 + dt) * 1024 + lane * 16);
    }
}

__global__ void __launch_bounds__(NT, 2) k_fused(Params p) {
    extern __shared__ __attribute__((aligned(16))) unsigned char smem[];
    LAS unsigned char* L = (LAS unsigned char*)smem;
    const unsigned Lb = (unsigned)(size_t)L;
    const int wid = __builtin_amdgcn_readfirstlane(threadIdx.x >> 6);
    const int wc = wid & 3;
    const int b = blockIdx.x, xcd = b & 7, bi = b >> 3;
    const int r = (xcd & 1) * 8 + (bi & 7), c = (xcd >> 1) * 4 + (bi >> 3);
    const int jq = b & 7, kq = b >> 3;
    const int cc = (xcd & 1) * 4 + (bi & 3), hp = (xcd >> 1) * 8 + (bi >> 2), r3 = hp >> 1, h3 = hp & 1;
    const unsigned char* ws = p.ws;
    const int odd = c & 1;
    const int u2 = wid >> 2, w2 = wid & 3, k4 = 2 * kq + u2;
    LAS f32x2* ST = (LAS f32x2*)(L + L_ST);
    f32x4 acc[4][8], csm[4];
    u32x4 eaC[2], eaS[2]; float tcA[2][4], tsA[2][4];
#pragma unroll
    for (int i = 0; i < 4; ++i) csm[i] = f32x4{0.f, 0.f, 0.f, 0.f};
    unsigned tag;
    {
    const int tid = threadIdx.x, lane = tid & 63;
    if (wid >= 4) {
        const int lw = wid - 4;
        unsigned epoch; gl32_sc1(epoch, ws, 0u);
        LdSrc ls;
        {
            const int idx = lw * 22 + (lane < 22 ? lane : 0);
            if (idx < 32) { ls.t = (const unsigned char*)p.x + (size_t)panel_row(r, 32 * (c & 3) + idx) * 1024; ls.tadv = 128; }
            else { const int i = idx - 32, row = 8 * (r & 7) + i / 7, it = i % 7;
                if (it == 6) { ls.t = (const unsigned char*)&g_fwd + c * 32768 + row * 64; ls.tadv = 64 * 64; }
                else { const int it3 = it % 3; ls.t = (const unsigned char*)(it < 3 ? p.wg : p.wu) + (size_t)row * (F * 2) + c * 224 + (it3 == 2 ? 220 : it3 * 128); ls.tadv = 64L * F * 2; } }
        }
        p1_touch(ls, Lb);
        SB();
#pragma unroll
        for (int i = 0; i < 4; ++i) { const int m = 8 * (4 * lw + i) + (lane >> 3); ls.a[i] = (unsigned)panel_row(r, m) * 1024u + (((lane & 7) ^ (lane >> 3)) << 4); }
        {
            const int b0 = lw & 1, b2 = lw >> 1;
            const int swz = 2 * (2 * b0 + (lane >> 5)) + 8 * b2;
            const int ch = (lane & 31) ^ swz, nt = ch >> 1, half = ch & 1, cw = nt >> 2, q = nt & 3;
            const unsigned char* base; int col, strideB;
            if (cw < 3) { const int ft = odd + 2 * cw + (q & 1); base = (const unsigned char*)(q < 2 ? p.wg : p.wu); col = 112 * c + 16 * ft + 8 * half; strideB = F * 2; }
            else if (q < 2) { const int ft = odd ? 0 : 6; base = (const unsigned char*)(q == 0 ? p.wg : p.wu); col = 112 * c + 16 * ft + 8 * half; strideB = F * 2; }
            else { base = (const unsigned char*)&g_fwd + c * 32768; col = 16 * (q - 2) + 8 * half; strideB = 64; }
            const unsigned char* b00 = base + (size_t)(2 * b0 + 8 * b2 + (lane >> 5)) * strideB + col * 2;
#pragma unroll
            for (int u = 0; u < 8; ++u) ls.b[u] = b00 + (size_t)(4 * (u & 1) + 16 * ((u >> 1) & 1) + 32 * (u >> 2)) * strideB;
            ls.sB64 = (long)strideB * 64;
        }
        p1_issue(ls, (const unsigned char*)p.x, Lb, lw, 0);
        p1_touch(ls, Lb); p1_touch(ls, Lb); p1_touch(ls, Lb);
        p1_issue(ls, (const unsigned char*)p.x, Lb, lw, 1);
        if (wid == 4 && lane < 4) *((volatile LAS unsigned*)(L + L_FLG) + lane) = 0u;
        wait_vm<15>(); pin(epoch); tag = epoch + 1u;
        if (r == 0 && tid == 256) __hip_atomic_store((gu32*)p.ws + W_CT + c * 32, tag << 8, RLX_AGENT);
        WAIT_LGKM0();
        BAR();
        p1_touch(ls, Lb);
        p1_issue(ls, (const unsigned char*)p.x, Lb, lw, 2);
        SB();
        const unsigned char *st0, *st1;
        {
            auto wline = [&](int i) { const int st = i >> 2, ln = (bi >> 2) * 16 + lw * 4 + (i & 3);
                return st < 14 ? (const unsigned char*)p.wd + (size_t)(32 * p3_ks(st, ln >> 5) + (ln & 31)) * 1024 + cc * 128
                               : (const unsigned char*)&g_inv + (size_t)cc * 65536 + (st - 14) * 16384 + ln * 128; };
            st0 = wline(lane);
            const int fi = (bi * 4 + lw) * 20 + (lane >= 8 && lane < 28 ? lane - 8 : 0);
            const int arr = fi / 1280, rr = (fi % 1280) / 5, ll = (fi % 1280) % 5;
            const int jrow = 4 * jq + (rr & 3) + 32 * (rr >> 2);
            st1 = lane < 8 ? wline(64 + lane) : (const unsigned char*)(arr ? p.wi : p.wr) + (size_t)jrow * (DF * 2) + min(ll * 128, 510);
        }
        u32x2 gran; const unsigned goff = (unsigned)WS_STAT + (unsigned)(r * 128 + (tid & 127)) * 8u;
        static_for<0, 7>([&](auto J) {
            constexpr int j = J.v;
            if constexpr (j < 4) wait_vm<13>(); else if constexpr (j == 4) wait_vm<12>();
            else if constexpr (j == 5) wait_vm<15>(); else { wait_vm<3>(); WAIT_LGKM0(); }
            BAR();
            if constexpr (j < 3) p1_touch(ls, Lb);
            if constexpr (j + 3 < 8) p1_issue(ls, (const unsigned char*)p.x, Lb, lw, j + 3);
            if constexpr (j == 4) {
                gl64_sc1(gran, ws, goff);
                glds4(st0, Lb + L_SCR); glds4(st1, Lb + L_SCR);
            }
            if constexpr (j == 5) {
                wait_vm<2>();
                pin(gran);
                if (lw < 2) {
                    if (!__all(gran.y == tag)) {
#pragma unroll 1
                        for (unsigned spins = 0;; ++spins) {
                            u32x2 g2; gl64_sc1(g2, ws, goff); wait_vm<0>(); pin(g2); gran = g2;
                            if (__all(g2.y == tag)) break;
                            if (spins > SPIN_CAP) { give_up(ws, lane); break; }
                            __builtin_amdgcn_s_sleep(1);
                        }
                    }
                    ST[tid & 127] = f32x2{bf_hi(gran.x), 1.0f / bf_lo(gran.x)};
                }
            }
            if constexpr (j == 6) {
                const unsigned wv0 = (unsigned)(lane >> 3) * 1024u + cc * 128u + (unsigned)(((lane & 7) ^ (2 * ((lane >> 4) & 3))) << 4), fv0 = lane * 16u;
#pragma unroll
                for (int st = 0; st < 3; ++st) p3_issue(p, Lb, lw, cc, st, wv0, fv0);
            }
        });
        {
            const int fr = lane & 15, g = lane >> 4;
#pragma unroll
            for (int jt = 0; jt < 2; ++jt) {
                float cv[8], sv[8];
#pragma unroll
                for (int j = 0; j < 8; ++j) { const float th = (float)(((16 * jt + fr) * (8 * g + j)) & 31) * (1.0f / 32.0f); cv[j] = cosr(th); sv[j] = sinr(th); }
                eaC[jt] = u32x4{pk_bf16(cv[0], cv[1]), pk_bf16(cv[2], cv[3]), pk_bf16(cv[4], cv[5]), pk_bf16(cv[6], cv[7])};
                eaS[jt] = u32x4{pk_bf16(sv[0], sv[1]), pk_bf16(sv[2], sv[3]), pk_bf16(sv[4], sv[5]), pk_bf16(sv[6], sv[7])};
#pragma unroll
                for (int e = 0; e < 4; ++e) { const float th = (float)((16 * jt + fr) * (4 * r + e)) * (1.0f / 2048.0f); tcA[jt][e] = cosr(th); tsA[jt][e] = sinr(th); }
            }
        }
#pragma unroll
        for (int q = 0; q < 4; ++q)
#pragma unroll
            for (int mt = 0; mt < 8; ++mt) acc[q][mt] = f32x4{0.f, 0.f, 0.f, 0.f};
    } else {
        if (wid == 0) {
            const unsigned char* pc = (const unsigned char*)__builtin_amdgcn_s_getpc();
            const int ln = bi * 10 + (lane < 10 ? lane : 0);
            glds4(pc - ((size_t)pc & 127) + (size_t)min(ln * 128, CODE_PF_BYTES), Lb + L_SCR);
        }
        if (wid == 1 || wid == 2) {
            const unsigned char* a;
            if (wid == 1) a = lane < 32 ? (const unsigned char*)p.x + (size_t)lane * 65536 : (const unsigned char*)p.wg + (size_t)min(lane - 32, 27) * 65536;
            else a = lane < 32 ? (const unsigned char*)p.wu + (size_t)min(lane, 27) * 65536 : (const unsigned char*)&g_fwd + (size_t)(lane & 7) * 65536;
            glds4(a, Lb + L_SCR);
        }
        unsigned epoch; gl32_sc1(epoch, ws, 0u);
        u32x4 xrow[2];
#pragma unroll
        for (int i = 0; i < 2; ++i) gl128(xrow[i], (const unsigned char*)p.x + (size_t)panel_row(r, 8 * c + 2 * wid + i) * 1024 + lane * 16);
        wait_vm<0>(); pin(epoch); pin(xrow[0]); pin(xrow[1]);
        tag = epoch + 1u;
#pragma unroll
        for (int i = 0; i < 2; ++i) {
            float v[8];
#pragma unroll
            for (int e = 0; e < 4; ++e) { v[2 * e] = bf_lo(xrow[i][e]); v[2 * e + 1] = bf_hi(xrow[i][e]); }
            float s = ((v[0] + v[1]) + (v[2] + v[3])) + ((v[4] + v[5]) + (v[6] + v[7]));
            s = wave_sum(s);
            const float mean = s * (1.0f / 512.0f);
            float q2 = 0.f;
#pragma unroll
            for (int e = 0; e < 8; ++e) { const float d = v[e] - mean; q2 += d * d; }
            q2 = wave_sum(q2);
            const float var = q2 * (1.0f / 512.0f);
            const float m_bf = bfr(mean), v_bf = bfr(var), s_bf = bfr(sqrtf(bfr(v_bf + 1e-5f)));
            const unsigned val = (__builtin_bit_cast(unsigned, m_bf) & 0xffff0000u) | (__builtin_bit_cast(unsigned, s_bf) >> 16);
            if (lane == 0) __hip_atomic_store((gu64*)(p.ws + WS_STAT) + (r * 128 + 8 * c + 2 * wid + i), ((unsigned long long)tag << 32) | val, RLX_AGENT);
        }
#pragma unroll
        for (int q = 0; q < 4; ++q)
#pragma unroll
            for (int mt = 0; mt < 8; ++mt) acc[q][mt] = f32x4{0.f, 0.f, 0.f, 0.f};
        if (wid < 3) p1_compute<false>(L, Lb, wc, lane, acc, csm); else p1_compute<true>(L, Lb, wc, lane, acc, csm);
        WAIT_LGKM0();
        if (lane == 0) __hip_atomic_fetch_add((LAS unsigned*)(L + L_FLG) + 3, 1u, __ATOMIC_RELAXED, __HIP_MEMORY_SCOPE_WORKGROUP);
    }
    }
    int tz = threadIdx.x; asm volatile("" : "+v"(tz));
    const int tid = tz, lane = tz & 63, fr = lane & 15, g = lane >> 4;
    const __amdgpu_buffer_rsrc_t rs = __builtin_amdgcn_make_buffer_rsrc((void*)p.ws, 0, (int)WS_END, 0x00020000);
    const unsigned wv = (unsigned)(lane >> 3) * 1024u + cc * 128u + (unsigned)(((lane & 7) ^ (2 * ((lane >> 4) & 3))) << 4), fv = lane * 16u;
    volatile LAS unsigned* flg = (volatile LAS unsigned*)(L + L_FLG);
    const int fj1 = 4 * jq + (fr & 3), fk = 4 * k4 + (fr >> 2);
    unsigned fw[4], fwi[4];
    auto signal = [&](int cnt, int word) {
        if (lane == 0) { const unsigned old = __hip_atomic_fetch_add((LAS unsigned*)(L + L_FLG) + cnt, 1u, __ATOMIC_RELAXED, __HIP_MEMORY_SCOPE_WORKGROUP);
            if (old == 3u) __hip_atomic_store((gu32*)p.ws + word, tag, RLX_AGENT); }
    };
    if (wid < 3) {
        const f32x4 cg0 = csm[0], cg1 = csm[1], cu0 = csm[2], cu1 = csm[3];
#pragma unroll
        for (int mt = 0; mt < 8; ++mt) {
            const f32x2 st = ST[16 * mt + fr];
            const float r2 = st.y * st.y;
            const f32x4 h0 = (acc[0][mt] - st.x * cg0) * (acc[2][mt] - st.x * cu0) * r2;
            const f32x4 h1 = (acc[1][mt] - st.x * cg1) * (acc[3][mt] - st.x * cu1) * r2;
            st_sc1(rs, (int)WS_H + ((((r * 8 + mt) * 48 + 3 * c + wc) * 64 + lane) << 4), pack8(h0, h1));
        }
    } else if (wid == 3) {
#pragma unroll
        for (int q = 0; q < 2; ++q)
#pragma unroll
            for (int mt = 0; mt < 8; ++mt) *(LAS f32x4*)(L + L_RAW + ((q * 8 + mt) * 64 + lane) * 16) = acc[2 + q][mt];
        WAIT_LGKM0();
        flg[0] = 1u;
        const f32x4 cg0 = csm[0], cu0 = csm[1];
#pragma unroll
        for (int mt = 0; mt < 8; ++mt) {
            const f32x2 st = ST[16 * mt + fr];
            const float r2 = st.y * st.y;
            const f32x4 h0 = (acc[0][mt] - st.x * cg0) * (acc[1][mt] - st.x * cu0) * r2;
            st_sc1_64(rs, (int)WS_H2 + ((((r * 8 + mt) * 16 + c) * 64 + lane) << 3), u32x2{pk_bf16(h0[0], h0[1]), pk_bf16(h0[2], h0[3])});
        }
    } else {
        const int mt = wid - 4;
        const int kq_ = fr >> 2, s2l = fr & 3;
#pragma unroll 1
        for (unsigned spins = 0; flg[0] == 0u && spins < (1u << 20); ++spins) __builtin_amdgcn_s_sleep(2);
        asm volatile("" ::: "memory");
        __builtin_amdgcn_s_setprio(3);
        u32x4 are, aim;
        {
            const int m0 = 32 * s2l + 8 * g, tl = 2 * s2l + (g >> 1), ln = 4 * mt + kq_ + 32 * (g & 1);
            const f32x4 r0 = *(LAS f32x4*)(L + L_RAW + (tl * 64 + ln) * 16), r1 = *(LAS f32x4*)(L + L_RAW + (tl * 64 + ln + 16) * 16);
            const f32x4 i0 = *(LAS f32x4*)(L + L_RAW + ((8 + tl) * 64 + ln) * 16), i1 = *(LAS f32x4*)(L + L_RAW + ((8 + tl) * 64 + ln + 16) * 16);
            const f32x4 sa = *(LAS f32x4*)(ST + m0), sb = *(LAS f32x4*)(ST + m0 + 2), sc = *(LAS f32x4*)(ST + m0 + 4), sd = *(LAS f32x4*)(ST + m0 + 6);
            f32x4 q0 = r0, q1 = r1;
            if (c == 0 && mt == 0) {
                const float csf = kq_ == 0 ? 512.0f : 0.0f;
                q0 = f32x4{r0[0] - sa[0] * csf, r0[1] - sa[2] * csf, r0[2] - sb[0] * csf, r0[3] - sb[2] * csf};
                q1 = f32x4{r1[0] - sc[0] * csf, r1[1] - sc[2] * csf, r1[2] - sd[0] * csf, r1[3] - sd[2] * csf};
            }
            are = u32x4{pk_bf16(q0[0] * sa[1], q0[1] * sa[3]), pk_bf16(q0[2] * sb[1], q0[3] * sb[3]), pk_bf16(q1[0] * sc[1], q1[1] * sc[3]), pk_bf16(q1[2] * sd[1], q1[3] * sd[3])};
            aim = u32x4{pk_bf16(i0[0] * sa[1], i0[1] * sa[3]), pk_bf16(i0[2] * sb[1], i0[3] * sb[3]), pk_bf16(i1[0] * sc[1], i1[1] * sc[3]), pk_bf16(i1[2] * sd[1], i1[3] * sd[3])};
        }
        const f32x4 z = {0.f, 0.f, 0.f, 0.f};
#pragma unroll
        for (int jt = 0; jt < 2; ++jt) {
            const f32x4 Prc = mfma(are, eaC[jt], z), Pis = mfma(aim, eaS[jt], z), Prs = mfma(are, eaS[jt], z), Pic = mfma(aim, eaC[jt], z);
            const int j1 = 16 * jt + fr;
            f32x4 ar = Prc + Pis, ai = Pic - Prs;
            if (c == 0 && mt == 0) {
                float pr2[4], qi2[4];
#pragma unroll
                for (int e = 0; e < 4; ++e) { const float cw = tcA[jt][e], sw = tsA[jt][e], br = Pic[e], bi2 = -Pis[e];
                    pr2[e] = br * cw + bi2 * sw; qi2[e] = -(bi2 * cw - br * sw); }
                if (g == 0) { ar = Prc; ai = -Prs;
                    st_sc1(rs, (int)WS_AX + (((((j1 >> 2) * 4 + (r >> 2)) * 4 + (r & 3)) * 4 + (j1 & 3)) << 4), pack8(pr2, qi2)); }
            }
            float pr[4], qi[4];
#pragma unroll
            for (int e = 0; e < 4; ++e) { const float cw = tcA[jt][e], sw = tsA[jt][e];
                pr[e] = ar[e] * cw + ai[e] * sw; qi[e] = -(ai[e] * cw - ar[e] * sw); }
            const int U = (j1 >> 2) * 64 + 4 * c + mt, lp = 16 * (r & 3) + 4 * g + (j1 & 3);
            st_sc1(rs, (int)WS_AP + (((U * 4 + (r >> 2)) * 64 + lp) << 4), pack8(pr, qi));
        }
        __builtin_amdgcn_s_setprio(0);
        wait_vm<0>();
        signal(1, W_FA + (c * 16 + r) * FSTR);
    }
#pragma unroll
    for (int e = 0; e < 4; ++e) { const unsigned o = (unsigned)((fj1 + 32 * (16 * w2 + 4 * g + e)) * DF + fk) * 2u; gl16(fw[e], p.wr, o); gl16(fwi[e], p.wi, o); }
    unsigned fx[8];
    {
        const unsigned o0 = (unsigned)((fj1 + 32 * (16 * w2 + 4 * g)) * DF + 256) * 2u;
        fx_load(fx, o0, o0 + 32u * DF * 2u, o0 + 64u * DF * 2u, o0 + 96u * DF * 2u, p.wr, p.wi, k4);
    }
    if (wid < 4) {
        if (k4 == 0) wait_vm<16>(); else wait_vm<8>();
        signal(2, W_FH + (r * 16 + c) * FSTR);
#pragma unroll 1
        for (unsigned spins = 0; flg[3] < 4u && spins < (1u << 22); ++spins) __builtin_amdgcn_s_sleep(1);
        asm volatile("" ::: "memory");
#pragma unroll
        for (int st = 3; st < 6; ++st) p3_issue(p, Lb, wid, cc, st, wv, fv);
#pragma unroll 1
        for (unsigned spins = 0; flg[1] < 4u && spins < (1u << 22); ++spins) __builtin_amdgcn_s_sleep(1);
        asm volatile("" ::: "memory");
#pragma unroll
        for (int st = 6; st < 9; ++st) p3_issue(p, Lb, wid, cc, st, wv, fv);
    }
    {
        const int kg = wid & 3, rh = wid >> 2;
        u32x4 E1[4], E2[4];
#pragma unroll
        for (int t = 0; t < 4; ++t) E1[t] = frag_cs(16 * w2 + fr, 16 * t + 4 * g, 64);
#pragma unroll
        for (int i = 0; i < 4; ++i) E2[i] = frag_cs(16 * (i >> 1) + fr, 16 * (i & 1) + 4 * g, 32);
        float tc[4], tsn[4];
#pragma unroll
        for (int e = 0; e < 4; ++e) { const float th = (float)((4 * jq + e) * (16 * w2 + fr)) * (1.0f / 2048.0f); tc[e] = cosr(th); tsn[e] = sinr(th); }
        P3Ctx cx;
        {
            const int qq = fr >> 2, pp = lane & 3, row = 32 * kg + 4 * g + qq, sw = 2 * ((row >> 1) & 3);
#pragma unroll
            for (int dt = 0; dt < 4; ++dt) cx.woff[dt] = row * 128 + (((2 * dt + (pp >> 1)) ^ sw) << 4) + 8 * (pp & 1);
            cx.hbase = (unsigned)WS_H + (unsigned)(((r3 * 8 + 4 * h3 + 2 * rh) * 48 + kg) * 64 + lane) * 16u;
            cx.h2base = (unsigned)WS_H2 + (unsigned)(((r3 * 8 + 4 * h3 + 2 * rh) * 16 + 2 * kg) * 64 + lane) * 8u;
            cx.tbase = (unsigned)WS_T + (unsigned)((((4 * r3 + 2 * h3 + rh) * 16 + 4 * kg) * 2) * 64 + lane) * 16u;
        }
        SB();
        if (wid == 0) poll16(ws, W_FA + (kq >> 1) * 16 * FSTR, tag, lane);
        if (wid == 1) poll16(ws, W_FH + r3 * 16 * FSTR, tag, lane);
        BAR();
        const int U = jq * 64 + k4;
        u32x4 Bf[4], Bx[4];
        u32x4 hb[HD][2]; u32x2 hh[2][4];
#pragma unroll
        for (int t = 0; t < 4; ++t) gl128_sc1(Bf[t], ws, (unsigned)WS_AP + (unsigned)((U * 4 + t) * 64 + lane) * 16u);
        {
            const unsigned xo = (unsigned)WS_AX + (unsigned)((jq * 16 + g) * 4 + (fr & 3)) * 16u;
            asm volatile("s_cmp_lg_u32 %6, 0\n\ts_cbranch_scc1 .Lnox%=\n\t"
                         "global_load_dwordx4 %0, %4, %5 sc1\n\tglobal_load_dwordx4 %1, %4, %5 offset:256 sc1\n\t"
                         "global_load_dwordx4 %2, %4, %5 offset:512 sc1\n\tglobal_load_dwordx4 %3, %4, %5 offset:768 sc1\n"
                         ".Lnox%=:" : "=&v"(Bx[0]), "=&v"(Bx[1]), "=&v"(Bx[2]), "=&v"(Bx[3]) : "v"(xo), "s"(ws), "s"(k4) : "memory", "scc");
        }
        static_for<0, HD>([&](auto Sx) { constexpr int o = Sx.v;
            gl128_sc1(hb[o][0], ws, cx.hbase + o * 4096u); gl128_sc1(hb[o][1], ws, cx.hbase + 48u * 1024u + o * 4096u); });
        wait_vm<2 * HD>();
        pin(Bf[0]); pin(Bf[1]); pin(Bf[2]); pin(Bf[3]); pin(Bx[0]); pin(Bx[1]); pin(Bx[2]); pin(Bx[3]);
        pin(fw[0]); pin(fw[1]); pin(fw[2]); pin(fw[3]); pin(fwi[0]); pin(fwi[1]); pin(fwi[2]); pin(fwi[3]);
        pin(fx[0]); pin(fx[1]); pin(fx[2]); pin(fx[3]); pin(fx[4]); pin(fx[5]); pin(fx[6]); pin(fx[7]);
        {
            f32x4 a1 = {0.f, 0.f, 0.f, 0.f}, a2 = a1;
#pragma unroll
            for (int t = 0; t < 4; ++t) { a1 = mfma(E1[t], Bf[t], a1); a2 = mfma(E1[t], swap_neg(Bf[t]), a2); }
            constexpr float SC = 2.0f / (2048.0f * 512.0f);
            const float sc = (k4 == 0 && (fr >> 2) == 0) ? 0.5f * SC : SC;
            float yr[4], yi[4];
#pragma unroll
            for (int e = 0; e < 4; ++e) { const float xr = a1[e], xi = -a2[e], wre = bf_lo(fw[e]), wim = bf_lo(fwi[e]);
                yr[e] = (xr * wre - xi * wim) * sc; yi[e] = (xr * wim + xi * wre) * sc; }
            if (k4 == 0) {
                f32x4 n1 = {0.f, 0.f, 0.f, 0.f}, n2 = n1;
#pragma unroll
                for (int t = 0; t < 4; ++t) { n1 = mfma(E1[t], Bx[t], n1); n2 = mfma(E1[t], swap_neg(Bx[t]), n2); }
                float xr2[4], xi2[4];
#pragma unroll
                for (int e = 0; e < 4; ++e) { const float xr = n1[e], xi = -n2[e], wre = bf_lo(fx[e]), wim = bf_lo(fx[4 + e]);
                    xr2[e] = (xr * wre - xi * wim) * (0.5f * SC); xi2[e] = (xr * wim + xi * wre) * (0.5f * SC); }
                *(LAS u32x4*)(L + L_YX + (w2 * 64 + lane) * 16) = pack8(xr2, xi2);
            }
            *(LAS u32x4*)(L + L_XI + ((u2 * 4 + w2) * 64 + lane) * 16) = pack8(yr, yi);
        }
        WAIT_LGKM0(); BAR();
        u32x4 wf[2][4];
        p3_rdw(L, cx, kg, lane, 0, wf[0]);
        {
            f32x4 t1 = {0.f, 0.f, 0.f, 0.f}, t2 = t1;
#pragma unroll
            for (int t = 0; t < 4; ++t) { const u32x4 y = lds128(L, L_XI + ((u2 * 4 + t) * 64 + lane) * 16);
                t1 = mfma(y, E1[t], t1); t2 = mfma(swap_neg(y), E1[t], t2); }
            const int s2 = 16 * w2 + fr, ko = 4 * k4 + g;
            float tr[4], ti[4];
#pragma unroll
            for (int e = 0; e < 4; ++e) { tr[e] = t1[e] * tc[e] - t2[e] * tsn[e]; ti[e] = t2[e] * tc[e] + t1[e] * tsn[e]; }
            st_sc1(rs, (int)WS_T + ((((s2 * 16 + (ko >> 4)) * 2 + (jq >> 2)) * 64 + (jq & 3) * 16 + (ko & 15)) << 4), pack8(tr, ti));
            if (k4 == 0) {
                f32x4 x1 = {0.f, 0.f, 0.f, 0.f}, x2 = x1;
#pragma unroll
                for (int t = 0; t < 4; ++t) { const u32x4 y = lds128(L, L_YX + (t * 64 + lane) * 16);
                    x1 = mfma(y, E1[t], x1); x2 = mfma(swap_neg(y), E1[t], x2); }
#pragma unroll
                for (int e = 0; e < 4; ++e) { tr[e] = x1[e] * tc[e] - x2[e] * tsn[e]; ti[e] = x2[e] * tc[e] + x1[e] * tsn[e]; }
                if (g == 0) st_sc1(rs, (int)WS_TX + ((s2 * 8 + jq) << 4), pack8(tr, ti));
            }
        }
        u32x4 xres; gl128(xres, (const unsigned char*)p.x + ((size_t)panel_row(r3, 64 * h3 + (tid >> 3)) * 512 + 64 * cc + 8 * (tid & 7)) * 2);
        f32x4 acc3[4][2];
#pragma unroll
        for (int dt = 0; dt < 4; ++dt) { acc3[dt][0] = f32x4{0.f, 0.f, 0.f, 0.f}; acc3[dt][1] = acc3[dt][0]; }
        unsigned ftf; u32x4 tx[2];
        static_for<0, 14>([&](auto Sx) {
            constexpr int s = Sx.v;
            wait_vm<p3_wait(s)>();
            if constexpr (s == 12 || s == 13) { pin(hh[s - 12][0]); pin(hh[s - 12][1]); pin(hh[s - 12][2]); pin(hh[s - 12][3]); }
            else { pin(hb[s % HD][0]); pin(hb[s % HD][1]); }
            if constexpr (s == FTC) {
                pin(ftf);
                if (wid == 0) {
                    if (!__all(ftf == (tag << 8) + 16u)) {
#pragma unroll 1
                        for (unsigned spins = 0;; ++spins) {
                            __builtin_amdgcn_s_sleep(PSLEEP);
                            unsigned f2; gl32_sc1(f2, ws, (unsigned)(W_CT + (lane & 15) * 32) * 4u); wait_vm<0>(); pin(f2);
                            if (__all(f2 == (tag << 8) + 16u)) break;
                            if (spins > SPIN_CAP) { give_up(ws, lane); break; }
                        }
                    }
                    if (b == 0 && lane == 0) __hip_atomic_store((gu32*)p.ws, tag, RLX_AGENT);
                }
            }
            if constexpr (s % 3 == 0 && s > 0) {
                WAIT_LGKM0(); BAR();
                if constexpr (s == 3) { if (tid == 0) __hip_atomic_fetch_add((gu32*)p.ws + W_CT + (kq >> 1) * 32, 1u, RLX_AGENT); }
                p3_rdw(L, cx, kg, lane, s, wf[s & 1]);
            }
            if constexpr (s + 1 < 14 && (s + 1) % 3 != 0) p3_rdw(L, cx, kg, lane, s + 1, wf[(s + 1) & 1]);
            u32x4 b0, b1;
            if constexpr (s == 12 || s == 13) { b0 = u32x4{hh[s - 12][0].x, hh[s - 12][0].y, hh[s - 12][1].x, hh[s - 12][1].y}; b1 = u32x4{hh[s - 12][2].x, hh[s - 12][2].y, hh[s - 12][3].x, hh[s - 12][3].y}; }
            else { b0 = hb[s % HD][0]; b1 = hb[s % HD][1]; }
#pragma unroll
            for (int dt = 0; dt < 4; ++dt) { acc3[dt][0] = mfma(wf[s & 1][dt], b0, acc3[dt][0]); acc3[dt][1] = mfma(wf[s & 1][dt], b1, acc3[dt][1]); }
            SB();
            static_for<HD, 18>([&](auto Ox) {
                constexpr int o = Ox.v;
                if constexpr (p3_issue_at(o) == s) {
                    if constexpr (o < 12) { gl128_sc1(hb[o % HD][0], ws, cx.hbase + o * 4096u); gl128_sc1(hb[o % HD][1], ws, cx.hbase + 48u * 1024u + o * 4096u); }
                    else if constexpr (o < 14) {
                        gl64_sc1(hh[o - 12][0], ws, cx.h2base + (o - 12) * 4096u); gl64_sc1(hh[o - 12][1], ws, cx.h2base + (o - 12) * 4096u + 512u);
                        gl64_sc1(hh[o - 12][2], ws, cx.h2base + 8192u + (o - 12) * 4096u); gl64_sc1(hh[o - 12][3], ws, cx.h2base + 8192u + (o - 12) * 4096u + 512u); }
                    else { gl128_sc1(hb[o % HD][0], ws, cx.tbase + (o - 14) * 2048u); gl128_sc1(hb[o % HD][1], ws, cx.tbase + (o - 14) * 2048u + 1024u);
                        if constexpr (o == 14) { const unsigned txo = (unsigned)WS_TX + (unsigned)((4 * r3 + 2 * h3 + rh) * 8 + g) * 16u; gl128_sc1(tx[0], ws, txo); gl128_sc1(tx[1], ws, txo + 64u); } }
                }
            });
            if constexpr (s >= 3 && s + 6 < 18) p3_issue2(p, Lb, wid, cc, s + 6, wv, fv);
            if constexpr (s == FTS) gl32_sc1(ftf, ws, wid == 0 ? (unsigned)(W_CT + (lane & 15) * 32) * 4u : cx.hbase - lane * 16u);
            SB();
        });
        wait_vm<0>();
        static_for<14, 18>([&](auto Ox) { pin(hb[Ox.v % HD][0]); pin(hb[Ox.v % HD][1]); });
        pin(tx[0]); pin(tx[1]); pin(xres);
        WAIT_LGKM0(); BAR();
        {
            u32x4 wz[2][4];
            p3_rdw(L, cx, kg, lane, 14, wz[0]); p3_rdw(L, cx, kg, lane, 15, wz[1]);
            u32x4 zf[4][2];
            static_for<0, 4>([&](auto Zx) {
                constexpr int z = Zx.v;
                const u32x4 T0 = hb[(14 + z) % HD][0], T1 = hb[(14 + z) % HD][1];
                u32x4 T0s = swap_neg(T0), T1s = swap_neg(T1);
                if constexpr (z == 0) {
                    if (kg == 0 && fr == 0) { T0s = tx[0]; T1s = tx[1]; }
                }
                f32x4 zr[2], zi[2];
#pragma unroll
                for (int st = 0; st < 2; ++st) { const f32x4 z0 = {0.f, 0.f, 0.f, 0.f}; zr[st] = mfma(T0, E2[st * 2 + 0], z0); zi[st] = mfma(T0s, E2[st * 2 + 0], z0); }
#pragma unroll
                for (int st = 0; st < 2; ++st) { zr[st] = mfma(T1, E2[st * 2 + 1], zr[st]); zi[st] = mfma(T1s, E2[st * 2 + 1], zi[st]); }
#pragma unroll
                for (int st = 0; st < 2; ++st) zf[z][st] = pack8(zr[st], zi[st]);
            });
#pragma unroll
            for (int z = 0; z < 2; ++z)
#pragma unroll
                for (int dt = 0; dt < 4; ++dt) { acc3[dt][0] = mfma(wz[z][dt], zf[z][0], acc3[dt][0]); acc3[dt][1] = mfma(wz[z][dt], zf[z][1], acc3[dt][1]); }
            p3_rdw(L, cx, kg, lane, 16, wz[0]); p3_rdw(L, cx, kg, lane, 17, wz[1]);
#pragma unroll
            for (int z = 0; z < 2; ++z)
#pragma unroll
                for (int dt = 0; dt < 4; ++dt) { acc3[dt][0] = mfma(wz[z][dt], zf[2 + z][0], acc3[dt][0]); acc3[dt][1] = mfma(wz[z][dt], zf[2 + z][1], acc3[dt][1]); }
        }
        WAIT_LGKM0(); BAR();
        constexpr int RP = 272;
#pragma unroll
        for (int dt = 0; dt < 4; ++dt)
#pragma unroll
            for (int st = 0; st < 2; ++st)
                *(LAS f32x4*)(L + (kg * 64 + 32 * rh + 16 * st + fr) * RP + (16 * dt + 4 * g) * 4) = acc3[dt][st];
        WAIT_LGKM0(); BAR();
        const int mm = tid >> 3, cg = tid & 7;
        f32x4 v0 = {0.f, 0.f, 0.f, 0.f}, v1 = v0;
#pragma unroll
        for (int q = 0; q < 4; ++q) { v0 += *(LAS f32x4*)(L + (q * 64 + mm) * RP + cg * 32); v1 += *(LAS f32x4*)(L + (q * 64 + mm) * RP + cg * 32 + 16); }
        const size_t o = (size_t)panel_row(r3, 64 * h3 + mm) * 512 + 64 * cc + 8 * cg;
        {
            const u32x4 ov = u32x4{pk_bf16(bf_lo(xres.x) + v0[0], bf_hi(xres.x) + v0[1]), pk_bf16(bf_lo(xres.y) + v0[2], bf_hi(xres.y) + v0[3]),
                                   pk_bf16(bf_lo(xres.z) + v1[0], bf_hi(xres.z) + v1[1]), pk_bf16(bf_lo(xres.w) + v1[2], bf_hi(xres.w) + v1[3])};
            const __amdgpu_buffer_rsrc_t ro = __builtin_amdgcn_make_buffer_rsrc((void*)p.out, 0, S * D * 2, 0x00020000);
            __builtin_amdgcn_raw_buffer_store_b128(ov, ro, (int)(o * 2), 0, 16);
        }
    }
}

extern "C" void kernel_launch(void* const* d_in, const int* in_sizes, int n_in, void* d_out, int out_size, void* d_ws, size_t ws_size,
                              hipStream_t stream) {
    Params p;
    p.x = (const uint16_t*)d_in[0]; p.wr = (const uint16_t*)d_in[1]; p.wi = (const uint16_t*)d_in[2];
    p.wg = (const uint16_t*)d_in[4]; p.wu = (const uint16_t*)d_in[5]; p.wd = (const uint16_t*)d_in[6];
    p.out = (uint16_t*)d_out; p.ws = (unsigned char*)d_ws;
    (void)hipFuncSetAttribute((const void*)k_fused, hipFuncAttributeMaxDynamicSharedMemorySize, LDS_BYTES);
    k_fused<<<256, NT, LDS_BYTES, stream>>>(p);
}
```
